```python
import torch

def _softmax(x, axis=-1):
    return torch.softmax(x, dim=axis)

def _gelu(x):
    return torch.nn.functional.gelu(x, approximate='tanh')

def _silu(x):
    return torch.nn.functional.silu(x)

def _sigmoid(x):
    return torch.sigmoid(x)

def _layernorm(x, axis=-1, eps=1e-05):
    m = x.mean(dim=axis, keepdim=True)
    v = x.var(dim=axis, keepdim=True, unbiased=False)
    return (x - m) / torch.sqrt(v + eps)

def _scatter_add(tgt, idx, src, axis=0):
    return tgt.clone().index_add_(axis, idx, src)

def _segment_sum(src, seg, n):
    out = torch.zeros((n,) + src.shape[1:], dtype=src.dtype)
    return out.index_add_(0, seg, src)

def _pad(x, pw):
    flat = tuple((v for pair in reversed(pw) for v in pair))
    return torch.nn.functional.pad(x, flat)
kernel_name = 'adaLN_cond__sliding_block_cached__glu_gated_bilinear__prompt'

def setup_inputs():

    _w = lambda a: (lambda t: t.to(torch.bfloat16) if t.is_floating_point() else t)(torch.as_tensor(a))

    def _A_setup():
        B, Sq, D = (1, 2048, 640)
        Hq, Hkv, Sk, Dh = (32, 32, 2048, 128)
        assert Hq % Hkv == 0, (Hq, Hkv)
        return {'x': _w((lambda *s: torch.randn(*s, dtype=torch.bfloat16) * 0.05)(B, Sq, D)), 'k_cache': _w((lambda *s: torch.randn(*s, dtype=torch.bfloat16) * 1.6)(B, Hkv, Sk, Dh)), 'v_cache': _w((lambda *s: torch.randn(*s, dtype=torch.bfloat16) * 0.8)(B, Hkv, Sk, Dh)), 'wq': _w((lambda *s: torch.randn(*s, dtype=torch.bfloat16) * 1.6)(D, Hq * Dh) * D ** (-0.5)), 'wo': _w((lambda *s: torch.randn(*s, dtype=torch.bfloat16) * 0.8)(Hq * Dh, D) * (Hq * Dh) ** (-0.5))}
    _A_in = _A_setup()

    def _B_setup():
        B, S, D, F = (1, 2048, 640, 1728)
        return {'x': _w((lambda *s: torch.randn(*s, dtype=torch.bfloat16) * 0.05)(B, S, D)), 'w_gate': _w((lambda *s: torch.randn(*s, dtype=torch.bfloat16) * 0.4)(D, F) * D ** (-0.5)), 'w_up': _w((lambda *s: torch.randn(*s, dtype=torch.bfloat16) * 0.4)(D, F) * D ** (-0.5)), 'w_down': _w((lambda *s: torch.randn(*s, dtype=torch.bfloat16) * 0.4)(F, D) * F ** (-0.5))}
    _B_in = _B_setup()
    return {'A_x': _A_in['x'], 'A_k_cache': _A_in['k_cache'], 'A_v_cache': _A_in['v_cache'], 'A_wq': _A_in['wq'], 'A_wo': _A_in['wo'], 'B_x': _B_in['x'], 'B_w_gate': _B_in['w_gate'], 'B_w_up': _B_in['w_up'], 'B_w_down': _B_in['w_down'], 'c': _w((lambda *s: torch.randn(*s, dtype=torch.bfloat16) * 0.2)(1, 640) * 8.0), 'w_mod': _w((lambda *s: torch.randn(*s, dtype=torch.bfloat16) * 0.2)(640, 6 * 640) * 640 ** (-0.5) * 8.0)}

def reference(A_x, A_k_cache, A_v_cache, A_wq, A_wo, B_x, B_w_gate, B_w_up, B_w_down, c, w_mod):

    def _A_ref(x, k_cache, v_cache, wq, wo):
        B, Sq, D = x.shape
        Hq, Dh = (32, 128)
        Hkv, Sk = (k_cache.shape[1], k_cache.shape[2])
        W = min(4096, max(256, Sk // 4))
        rep = Hq // Hkv
        q = (lambda x, *d: x.permute(*d))((x @ wq).reshape(B, Sq, Hq, Dh), 0, 2, 1, 3)
        k = (lambda x, n, axis: torch.repeat_interleave(x, n, dim=axis))(k_cache[:, :, Sk - W:, :], rep, axis=1)
        v = (lambda x, n, axis: torch.repeat_interleave(x, n, dim=axis))(v_cache[:, :, Sk - W:, :], rep, axis=1)
        scores = torch.einsum('bhqd,bhkd->bhqk', q, k) * Dh ** (-0.5)
        p = _softmax(scores, axis=-1)
        o = torch.einsum('bhqk,bhkd->bhqd', p, v)
        return (lambda x, *d: x.permute(*d))(o, 0, 2, 1, 3).reshape(B, Sq, Hq * Dh) @ wo

    def _B_ref(x, w_gate, w_up, w_down):
        g = torch.einsum('bsd,df->bsf', x, w_gate)
        u = torch.einsum('bsd,df->bsf', x, w_up)
        h = g * u
        return torch.einsum('bsf,fd->bsd', h, w_down)
    x = A_x
    mod = (_silu(c) @ w_mod).reshape(c.shape[0], 6, 1, -1)
    sa, ca, ga, sb, cb, gb = (mod[:, 0].clone(), mod[:, 1].clone(), mod[:, 2].clone(), mod[:, 3].clone(), mod[:, 4].clone(), mod[:, 5].clone())
    h = _layernorm(x, axis=-1) * (1 + ca) + sa
    x = x + ga * _A_ref(h, A_k_cache, A_v_cache, A_wq, A_wo)
    h = _layernorm(x, axis=-1) * (1 + cb) + sb
    x = x + gb * _B_ref(h, B_w_gate, B_w_up, B_w_down)
    return x
```

```cpp
#include <hip/hip_runtime.h>
#include <hip/hip_bf16.h>
#include <stdint.h>
typedef unsigned short u16;
typedef __bf16 bf16x8 __attribute__((ext_vector_type(8)));
typedef float f32x4 __attribute__((ext_vector_type(4)));
__device__ __forceinline__ float bf2f(u16 x){ return __uint_as_float(((unsigned)x)<<16); }
__device__ __forceinline__ u16 f2bf(float f){ __bf16 b=(__bf16)f; return __builtin_bit_cast(u16,b); }

constexpr int S=2048, D=640, H=32, DH=128, SK=2048, W=512, F=1728;

__global__ void k_mod(const u16* c, const u16* wmod, float* mod){
  int n = blockIdx.x*64 + (threadIdx.x&63); int part = threadIdx.x>>6;
  __shared__ float red[4][64];
  float acc=0;
  for(int k=part*160;k<part*160+160;k++){ float cv=bf2f(c[k]); float s=cv/(1.f+__expf(-cv)); acc+= s*bf2f(wmod[(size_t)k*3840+n]); }
  red[part][threadIdx.x&63]=acc; __syncthreads();
  if(part==0) mod[n]=red[0][threadIdx.x]+red[1][threadIdx.x]+red[2][threadIdx.x]+red[3][threadIdx.x];
}
template<bool F32IN>
__global__ void k_ln(const void* xin, const float* shift, const float* scale, u16* h){
  int row = blockIdx.x*4 + (threadIdx.x>>6); int lane=threadIdx.x&63;
  float v[10]; float s=0;
  for(int i=0;i<10;i++){ int k=lane+64*i; v[i]= F32IN? ((const float*)xin)[(size_t)row*D+k] : bf2f(((const u16*)xin)[(size_t)row*D+k]); s+=v[i]; }
  for(int o=32;o>0;o>>=1) s+=__shfl_xor(s,o);
  float m=s/D; float q=0;
  for(int i=0;i<10;i++){ float d=v[i]-m; q+=d*d; }
  for(int o=32;o>0;o>>=1) q+=__shfl_xor(q,o);
  float r=rsqrtf(q/D+1e-5f);
  for(int i=0;i<10;i++){ int k=lane+64*i; h[(size_t)row*D+k]=f2bf((v[i]-m)*r*(1.f+scale[k])+shift[k]); }
}
template<int EPI>
__global__ void k_gemm(const u16* A, const u16* B, int M,int N,int K, u16* Cb, float* Cf, const u16* residb, const float* residf, const float* gate){
  __shared__ u16 As[64][40]; __shared__ u16 Bs[64][40];
  int bm=blockIdx.y*64, bn=blockIdx.x*64; int t=threadIdx.x, lane=t&63, w=t>>6; int wr=w>>1, wc=w&1;
  f32x4 acc[2][2]={};
  for(int k0=0;k0<K;k0+=32){
    { int r=t>>2, c=(t&3)*8; *(uint4*)&As[r][c] = *(const uint4*)&A[(size_t)(bm+r)*K+k0+c]; }
    { int r=t>>3, c=(t&7)*8; uint4 v=*(const uint4*)&B[(size_t)(k0+r)*N+bn+c]; const u16* p=(const u16*)&v; for(int j=0;j<8;j++) Bs[c+j][r]=p[j]; }
    __syncthreads();
    bf16x8 a[2],b[2];
    for(int m=0;m<2;m++) a[m]=*(const bf16x8*)&As[wr*32+m*16+(lane&15)][(lane>>4)*8];
    for(int n=0;n<2;n++) b[n]=*(const bf16x8*)&Bs[wc*32+n*16+(lane&15)][(lane>>4)*8];
    for(int m=0;m<2;m++) for(int n=0;n<2;n++) acc[m][n]=__builtin_amdgcn_mfma_f32_16x16x32_bf16(a[m],b[n],acc[m][n],0,0,0);
    __syncthreads();
  }
  for(int m=0;m<2;m++) for(int n=0;n<2;n++) for(int j=0;j<4;j++){
    int r=bm+wr*32+m*16+(lane>>4)*4+j, c=bn+wc*32+n*16+(lane&15); size_t o=(size_t)r*N+c; float v=acc[m][n][j];
    if(EPI==0) Cb[o]=f2bf(v);
    if(EPI==1) Cf[o]=bf2f(residb[o])+gate[c]*v;
    if(EPI==2) Cb[o]=f2bf(residf[o]+gate[c]*v);
  }
}
__global__ __launch_bounds__(256) void k_attn(const u16* q, const u16* kc, const u16* vc, u16* o){
  int h=blockIdx.y; int row=blockIdx.x*4+(threadIdx.x>>6); int lane=threadIdx.x&63;
  __shared__ float qs[4][128]; __shared__ float ps[4][512];
  int w=threadIdx.x>>6;
  qs[w][lane]=bf2f(q[(size_t)row*4096+h*128+lane]); qs[w][lane+64]=bf2f(q[(size_t)row*4096+h*128+lane+64]);
  __syncthreads();
  const u16* K=kc+((size_t)h*SK+(SK-W))*DH; const u16* V=vc+((size_t)h*SK+(SK-W))*DH;
  float s[8]; float m=-1e30f;
  _Pragma("unroll") for(int i=0;i<8;i++){ int key=lane+64*i; float a=0; _Pragma("unroll 1") for(int d=0;d<128;d+=8){ uint4 v=*(const uint4*)&K[(size_t)key*DH+d]; a+=qs[w][d]*bf2f(v.x&0xffff)+qs[w][d+1]*bf2f(v.x>>16)+qs[w][d+2]*bf2f(v.y&0xffff)+qs[w][d+3]*bf2f(v.y>>16)+qs[w][d+4]*bf2f(v.z&0xffff)+qs[w][d+5]*bf2f(v.z>>16)+qs[w][d+6]*bf2f(v.w&0xffff)+qs[w][d+7]*bf2f(v.w>>16); } s[i]=a*0.08838834764831845f; m=fmaxf(m,s[i]); }
  for(int of=32;of>0;of>>=1) m=fmaxf(m,__shfl_xor(m,of));
  float l=0; _Pragma("unroll") for(int i=0;i<8;i++){ s[i]=__expf(s[i]-m); l+=s[i]; }
  for(int of=32;of>0;of>>=1) l+=__shfl_xor(l,of);
  _Pragma("unroll") for(int i=0;i<8;i++) ps[w][lane+64*i]=s[i]/l;
  __syncthreads();
  float a0=0,a1=0; for(int key=0;key<512;key++){ float p=ps[w][key]; unsigned vv=*(const unsigned*)&V[(size_t)key*DH+lane*2]; a0+=p*bf2f(vv&0xffff); a1+=p*bf2f(vv>>16); }
  unsigned ov=f2bf(a0)|((unsigned)f2bf(a1)<<16); *(unsigned*)&o[(size_t)row*4096+h*128+lane*2]=ov;
}
__global__ void k_mul(const u16* g,const u16* u,u16* hh,int n){ int i=blockIdx.x*256+threadIdx.x; if(i<n) hh[i]=f2bf(bf2f(g[i])*bf2f(u[i])); }

extern "C" void kernel_launch(void* const* d_in, const int* in_sizes, int n_in, void* d_out, int out_size, void* d_ws, size_t ws_size, hipStream_t stream){
  const u16* x=(const u16*)d_in[0]; const u16* kc=(const u16*)d_in[1]; const u16* vc=(const u16*)d_in[2]; const u16* wq=(const u16*)d_in[3]; const u16* wo=(const u16*)d_in[4];
  const u16* wg=(const u16*)d_in[6]; const u16* wu=(const u16*)d_in[7]; const u16* wd=(const u16*)d_in[8]; const u16* c=(const u16*)d_in[9]; const u16* wmod=(const u16*)d_in[10];
  char* ws=(char*)d_ws; size_t off=0; auto alloc=[&](size_t b){ void* p=ws+off; off+=(b+255)&~(size_t)255; return p; };
  float* mod=(float*)alloc(3840*4); u16* h1=(u16*)alloc(S*D*2); u16* q=(u16*)alloc((size_t)S*4096*2); u16* o=(u16*)alloc((size_t)S*4096*2);
  float* x1=(float*)alloc(S*D*4); u16* h2=(u16*)alloc(S*D*2); u16* g=(u16*)alloc(S*F*2); u16* u=(u16*)alloc(S*F*2); u16* hh=(u16*)alloc(S*F*2);
  k_mod<<<60,256,0,stream>>>(c,wmod,mod);
  k_ln<false><<<S/4,256,0,stream>>>(x,mod,mod+640,h1);
  k_gemm<0><<<dim3(4096/64,S/64),256,0,stream>>>(h1,wq,S,4096,D,q,nullptr,nullptr,nullptr,nullptr);
  k_attn<<<dim3(S/4,H),256,0,stream>>>(q,kc,vc,o);
  k_gemm<1><<<dim3(D/64,S/64),256,0,stream>>>(o,wo,S,D,4096,nullptr,x1,x,nullptr,mod+2*640);
  k_ln<true><<<S/4,256,0,stream>>>(x1,mod+3*640,mod+4*640,h2);
  k_gemm<0><<<dim3(F/64,S/64),256,0,stream>>>(h2,wg,S,F,D,g,nullptr,nullptr,nullptr,nullptr);
  k_gemm<0><<<dim3(F/64,S/64),256,0,stream>>>(h2,wu,S,F,D,u,nullptr,nullptr,nullptr,nullptr);
  k_mul<<<(S*F+255)/256,256,0,stream>>>(g,u,hh,S*F);
  k_gemm<2><<<dim3(D/64,S/64),256,0,stream>>>(hh,wd,S,D,F,(u16*)d_out,nullptr,nullptr,x1,mod+5*640);
}
```

```cpp
#include <hip/hip_runtime.h>
#include <hip/hip_bf16.h>
#include <stdint.h>
typedef unsigned short u16;
typedef unsigned long long u64;
typedef unsigned u32x4 __attribute__((ext_vector_type(4)));
typedef float f32x4 __attribute__((ext_vector_type(4)));
typedef float f32x2 __attribute__((ext_vector_type(2)));
typedef __bf16 bf16x2 __attribute__((ext_vector_type(2)));
__device__ __forceinline__ float bf2f(u16 x) { return __uint_as_float(((unsigned)x) << 16); }
__device__ __forceinline__ float blo(unsigned x) { return __uint_as_float(x << 16); }
__device__ __forceinline__ float bhi(unsigned x) { return __uint_as_float(x & 0xffff0000u); }
__device__ __forceinline__ unsigned pk2(float a, float b) { f32x2 v = {a, b}; return __builtin_bit_cast(unsigned, __builtin_convertvector(v, bf16x2)); }
__device__ __forceinline__ float rbf(float f) { return blo(pk2(f, 0.f)); }
constexpr int S = 2048, D = 640, F = 1728;
constexpr float QSC = 0.12751743082459868f;

constexpr size_t al64k(size_t x) { return (x + 65535) & ~(size_t)65535; }
constexpr size_t WS_FLG = 0,
                 WS_MODG = 32768,
                 WS_MOD = 65536,
                 WS_VECS = 98304, WS_XH = 196608, WS_OF = al64k(WS_XH + (size_t)S * D * 2),
                 WS_X1F = al64k(WS_OF + (size_t)S * 4096 * 2), WS_X1C = al64k(WS_X1F + (size_t)S * D * 2), WS_ST = al64k(WS_X1C + (size_t)S * D * 2),
                 WS_WOP = al64k(WS_ST + (size_t)S * 128), WS_WDP = al64k(WS_WOP + (size_t)4096 * D * 2), WS_WFP = al64k(WS_WDP + (size_t)1792 * D * 2),
                 WS_HH = al64k(WS_WFP + (size_t)2 * 16 * 640 * 224), WS_END = al64k(WS_HH + (size_t)S * 1792 * 2);
constexpr unsigned F0 = 1024, F1 = 2048, F2 = 4096, F3 = 8192, F4 = 12288, F5 = 16384;

struct Args {
  const char *x, *kc, *vc, *wq, *wo, *wg, *wu, *wd, *c, *wmod;
  char *out, *ws;
};

__device__ __forceinline__ void st16_dev(void* p, u32x4 v) { asm volatile("global_store_dwordx4 %0, %1, off sc1\n\ts_nop 1" ::"v"(p), "v"(v) : "memory"); }
__device__ __forceinline__ void st4_dev(float* p, float v) { __hip_atomic_store(p, v, __ATOMIC_RELAXED, __HIP_MEMORY_SCOPE_AGENT); }
__device__ __forceinline__ float ld4_dev(const float* p) { return __hip_atomic_load(p, __ATOMIC_RELAXED, __HIP_MEMORY_SCOPE_AGENT); }
__device__ __forceinline__ void ld16x4_dev(const void* p, unsigned stride, f32x4& a, f32x4& b, f32x4& c, f32x4& d) {
  const char* q = (const char*)p;
  asm volatile("global_load_dwordx4 %0, %4, off sc1\n\tglobal_load_dwordx4 %1, %5, off sc1\n\tglobal_load_dwordx4 %2, %6, off sc1\n\tglobal_load_dwordx4 %3, %7, off sc1\n\ts_waitcnt vmcnt(0)"
               : "=&v"(a), "=&v"(b), "=&v"(c), "=&v"(d) : "v"(q), "v"(q + stride), "v"(q + 2 * stride), "v"(q + 3 * stride) : "memory");
}
__device__ __forceinline__ void ld16x2_dev(const void* p, const void* r, f32x4& a, f32x4& b) {
  asm volatile("global_load_dwordx4 %0, %2, off sc1\n\tglobal_load_dwordx4 %1, %3, off sc1\n\ts_waitcnt vmcnt(0)" : "=&v"(a), "=&v"(b) : "v"(p), "v"(r) : "memory");
}
__device__ __forceinline__ void ld16x4p_dev(const void* p0, const void* p1, const void* p2, const void* p3, f32x4& a, f32x4& b, f32x4& c, f32x4& d) {
  asm volatile("global_load_dwordx4 %0, %4, off sc1\n\tglobal_load_dwordx4 %1, %5, off sc1\n\tglobal_load_dwordx4 %2, %6, off sc1\n\tglobal_load_dwordx4 %3, %7, off sc1\n\ts_waitcnt vmcnt(0)"
               : "=&v"(a), "=&v"(b), "=&v"(c), "=&v"(d) : "v"(p0), "v"(p1), "v"(p2), "v"(p3) : "memory");
}
__device__ __forceinline__ void ld16x8_dev(const void* p, const void* r, unsigned stride, f32x4& a, f32x4& b, f32x4& c, f32x4& d, f32x4& e, f32x4& f, f32x4& g, f32x4& h) {
  const char* q = (const char*)p; const char* s = (const char*)r;
  asm volatile("global_load_dwordx4 %0, %8, off sc1\n\tglobal_load_dwordx4 %1, %9, off sc1\n\tglobal_load_dwordx4 %2, %10, off sc1\n\tglobal_load_dwordx4 %3, %11, off sc1\n\t"
               "global_load_dwordx4 %4, %12, off sc1\n\tglobal_load_dwordx4 %5, %13, off sc1\n\tglobal_load_dwordx4 %6, %14, off sc1\n\tglobal_load_dwordx4 %7, %15, off sc1\n\ts_waitcnt vmcnt(0)"
               : "=&v"(a), "=&v"(b), "=&v"(c), "=&v"(d), "=&v"(e), "=&v"(f), "=&v"(g), "=&v"(h)
               : "v"(q), "v"(q + stride), "v"(q + 2 * stride), "v"(q + 3 * stride), "v"(s), "v"(s + stride), "v"(s + 2 * stride), "v"(s + 3 * stride) : "memory");
}
__device__ __forceinline__ void tr_frags5(unsigned addr, u32x4& a, u32x4& b, u32x4& c, u32x4& d, u32x4& e) {
  typedef unsigned u32x2 __attribute__((ext_vector_type(2)));
  u32x2 r0, r1, r2, r3, r4, r5, r6, r7, r8, r9;
  asm volatile("s_waitcnt lgkmcnt(0)\n\t"
               "ds_read_b64_tr_b16 %0, %10\n\tds_read_b64_tr_b16 %1, %10 offset:2560\n\t"
               "ds_read_b64_tr_b16 %2, %10 offset:32\n\tds_read_b64_tr_b16 %3, %10 offset:2592\n\t"
               "ds_read_b64_tr_b16 %4, %10 offset:64\n\tds_read_b64_tr_b16 %5, %10 offset:2624\n\t"
               "ds_read_b64_tr_b16 %6, %10 offset:96\n\tds_read_b64_tr_b16 %7, %10 offset:2656\n\t"
               "ds_read_b64_tr_b16 %8, %10 offset:128\n\tds_read_b64_tr_b16 %9, %10 offset:2688\n\t"
               "s_waitcnt lgkmcnt(0)"
               : "=&v"(r0), "=&v"(r1), "=&v"(r2), "=&v"(r3), "=&v"(r4), "=&v"(r5), "=&v"(r6), "=&v"(r7), "=&v"(r8), "=&v"(r9) : "v"(addr) : "memory");
  a = (u32x4){r0[0], r0[1], r1[0], r1[1]}; b = (u32x4){r2[0], r2[1], r3[0], r3[1]}; c = (u32x4){r4[0], r4[1], r5[0], r5[1]};
  d = (u32x4){r6[0], r6[1], r7[0], r7[1]}; e = (u32x4){r8[0], r8[1], r9[0], r9[1]};
}
__device__ __forceinline__ void st_granule(char* p, float v, unsigned ep) {
  __hip_atomic_store((u64*)p, ((u64)ep << 32) | __float_as_uint(v), __ATOMIC_RELAXED, __HIP_MEMORY_SCOPE_AGENT);
}
template <int N>
__device__ __forceinline__ void sweep_granules(const char* const (&p)[N], unsigned ep, u32x4 (&v)[N]) {
  for (int spin = 0; spin < 100000; ++spin) {
    if constexpr (N == 5)
      asm volatile("global_load_dwordx4 %0, %5, off sc1\n\tglobal_load_dwordx4 %1, %6, off sc1\n\tglobal_load_dwordx4 %2, %7, off sc1\n\tglobal_load_dwordx4 %3, %8, off sc1\n\tglobal_load_dwordx4 %4, %9, off sc1\n\ts_waitcnt vmcnt(0)"
                   : "=&v"(v[0]), "=&v"(v[1]), "=&v"(v[2]), "=&v"(v[3]), "=&v"(v[4]) : "v"(p[0]), "v"(p[1]), "v"(p[2]), "v"(p[3]), "v"(p[4]) : "memory");
    else
      asm volatile("global_load_dwordx4 %0, %3, off sc1\n\tglobal_load_dwordx4 %1, %4, off sc1\n\tglobal_load_dwordx4 %2, %5, off sc1\n\ts_waitcnt vmcnt(0)"
                   : "=&v"(v[0]), "=&v"(v[1]), "=&v"(v[2]) : "v"(p[0]), "v"(p[1]), "v"(p[2]) : "memory");
    bool ok = true;
#pragma unroll
    for (int i = 0; i < N; ++i) ok = ok && v[i][1] == ep && v[i][3] == ep;
    if (__all(ok)) break;
  }
}
__device__ __forceinline__ void publish(char* ws, unsigned off, u64 tag) {
  asm volatile("s_waitcnt vmcnt(0)" ::: "memory");
  __syncthreads();
  if (threadIdx.x == 0) __hip_atomic_store((u64*)(ws + off), tag, __ATOMIC_RELAXED, __HIP_MEMORY_SCOPE_AGENT);
}
__device__ __forceinline__ void poll_flags(const char* ws, unsigned off, unsigned n, u64 tag) {
  const unsigned lane = threadIdx.x & 63u;
  const u64* f0 = (const u64*)(ws + off) + (lane < n ? lane : 0u);
  const u64* f1 = (const u64*)(ws + off) + (lane + 64u < n ? lane + 64u : 0u);
  for (int spin = 0; spin < 100000; ++spin) {
    const u64 a = __hip_atomic_load(f0, __ATOMIC_RELAXED, __HIP_MEMORY_SCOPE_AGENT), b = __hip_atomic_load(f1, __ATOMIC_RELAXED, __HIP_MEMORY_SCOPE_AGENT);
    if (__all(a == tag && b == tag)) break;
    __builtin_amdgcn_s_sleep(1);
  }
}

__global__ __launch_bounds__(256, 1) void mega(const Args a ) {
  __shared__ __attribute__((aligned(16))) char lds[163840];
  const unsigned lb = (unsigned)(size_t)lds;
  const unsigned b = blockIdx.x;
  char* const ws = a.ws;
#define STAMP_OUT(blk)
#define STAMP_P0(i)
  const unsigned wv = __builtin_amdgcn_readfirstlane(threadIdx.x >> 6);
  {
    const unsigned piece = __builtin_amdgcn_readfirstlane(((b >> 3) * 4u + wv) * 1024u), l16 = (threadIdx.x & 63u) * 16u;
    const unsigned m0d = __builtin_amdgcn_readfirstlane(lb + 155648u + wv * 1024u);
    unsigned tmp;
    asm volatile("s_getpc_b64 s[4:5]\n"
                 "mega_code_pc:\n\t"
                 "s_mov_b32 s6, mega_code_end-mega_code_pc-16\n\t"
                 "v_add_u32 %0, %1, %2\n\t"
                 "v_min_u32 %0, s6, %0\n\t"
                 "v_and_b32 %0, -16, %0\n\t"
                 "s_mov_b32 m0, %3\n\t"
                 "s_nop 0\n\t"
                 "global_load_lds_dwordx4 %0, s[4:5]"
                 : "=&v"(tmp) : "s"(piece), "v"(l16), "s"(m0d) : "s4", "s5", "s6", "memory");
  }
  const u64 g0 = __hip_atomic_load((const u64*)ws, __ATOMIC_RELAXED, __HIP_MEMORY_SCOPE_AGENT);
  auto do_P = [&]() __attribute__((always_inline)) {
    const unsigned lane = threadIdx.x & 63u, w = wv, j = b >> 3, h = 4u * (b & 7u) + (j & 3u);
    const char* sW = a.wq + h * 256u - 4096;
    const unsigned woff = lb + 4096u * w;
    const unsigned r7 = (lane >> 2) & 7u, sub = lane >> 5, slot = lane & 3u;
    unsigned wo[4];
#pragma unroll
    for (int ii = 0; ii < 4; ++ii) {
      const unsigned rg = 2u * w + (ii >> 1), cq0 = 2u * (ii & 1), xr = (2u * rg + (r7 >> 2)) & 3u;
      wo[ii] = (8u * rg + r7) * 8192u + (4u * (cq0 + sub) + (slot ^ xr)) * 16u + 4096u - 1024u * ii;
    }
    asm volatile(
    "s_mov_b32 m0, s48\n\t"
    "s_nop 0\n\t"
    "global_load_lds_dwordx4 v2, s[38:39] offset:0\n\t"
    "global_load_lds_dwordx4 v3, s[38:39] offset:1024\n\t"
    "global_load_lds_dwordx4 v4, s[38:39] offset:2048\n\t"
    "global_load_lds_dwordx4 v5, s[38:39] offset:3072\n\t"
    "s_add_u32 s38, s38, 524288\n\t"
    "s_addc_u32 s39, s39, 0\n\t"
    "s_add_u32 m0, s48, 16384\n\t"
    "s_nop 0\n\t"
    "global_load_lds_dwordx4 v2, s[38:39] offset:0\n\t"
    "global_load_lds_dwordx4 v3, s[38:39] offset:1024\n\t"
    "global_load_lds_dwordx4 v4, s[38:39] offset:2048\n\t"
    "global_load_lds_dwordx4 v5, s[38:39] offset:3072\n\t"
      : "+{s[38:39]}"(sW)
      : "{v2}"(wo[0]), "{v3}"(wo[1]), "{v4}"(wo[2]), "{v5}"(wo[3]), "{s48}"(woff)
      : "memory");
  };
  u64 tag;
#define MAKE_TAG ((((u64)__builtin_amdgcn_readfirstlane((unsigned)(g0 >> 32)) << 32) | __builtin_amdgcn_readfirstlane((unsigned)g0)) + 1ull)
  unsigned Axo, Awo[4], Ako[4], Awa[4], Aka[2], Avb[2], Aoo, Ac1[5], Ac2[7], Act, Apl1, Apl2;
  const unsigned jA = b >> 3, hA = 4u * (b & 7u) + (jA & 3u), rbA = jA >> 2;
  const unsigned WvA = b * 4u + wv, nt1 = WvA >> 7, kg1 = WvA & 127u;
  const bool act2 = WvA < 640u;
  const unsigned W2 = act2 ? WvA : 0u, t2 = W2 / 320u, r2 = W2 - t2 * 320u, ct2 = r2 / 20u, kg2 = r2 - ct2 * 20u;
  auto do_glue = [&]() __attribute__((always_inline)) {
    const unsigned lane = threadIdx.x & 63u, w = wv;
    const unsigned r7 = (lane >> 2) & 7u, sub = lane >> 5, slot = lane & 3u;
#pragma unroll
    for (int ii = 0; ii < 4; ++ii) {
      const unsigned rg = 2u * w + (ii >> 1), cq0 = 2u * (ii & 1), xr = (2u * rg + (r7 >> 2)) & 3u;
      const unsigned ch = 4u * (cq0 + sub) + (slot ^ xr);
      Awo[ii] = (8u * rg + r7) * 8192u + ch * 16u + 4096u - 1024u * ii;
      Ako[ii] = (8u * rg + r7) * 256u + ch * 16u + 4096u - 1024u * ii;
    }
    const unsigned g = lane >> 4, q = (lane >> 2) & 3u, pp = lane & 3u, hh = lane >> 5, r = lane & 31u;
#pragma unroll
    for (int e = 0; e < 2; ++e)
#pragma unroll
      for (int u = 0; u < 2; ++u)
        Awa[2 * e + u] = lb + 2048u * g + 64u * (4u * u + q) + 16u * ((2u * e + (pp >> 1)) ^ ((2u * g + u) & 3u)) + 8u * (pp & 1u);
#pragma unroll
    for (int par = 0; par < 2; ++par) Aka[par] = lb + 2048u * (r >> 3) + 64u * (r & 7u) + 16u * ((2u * par + hh) ^ ((r >> 2) & 3u));
#pragma unroll
    for (int u = 0; u < 2; ++u) Avb[u] = lb + 65536u + 64u * (4u * hh + q) + 16u * ((2u * (g & 1u) + (pp >> 1)) ^ (2u * u + hh)) + 8u * (pp & 1u);
    Aoo = (r >> 4) * 1024u + (hh * 16u + (r & 15u)) * 16u;
    Axo = lane * 16u;
    const unsigned odd1 = nt1 & 1u, odd2 = ct2 & 1u;
#pragma unroll
    for (int ii = 0; ii < 5; ++ii) {
      const unsigned c = 64u * ii + lane, row = c / 10u, cc = c - row * 10u, jj = cc >> 1, pj = odd1 ? (jj < 4u ? jj + 1u : 0u) : jj;
      Ac1[ii] = row * 1280u + pj * 32u + (cc & 1u) * 16u + 4096u - 1024u * ii;
    }
    Act = lb + 131072u + w * 5120u + (16u * (g >> 1) + 4u * (g & 1u) + q) * 160u + 8u * pp;
#pragma unroll
    for (int ii = 0; ii < 7; ++ii) {
      const unsigned c = 64u * ii + lane, row = c / 14u, cc = c - row * 14u, jj = cc >> 1, pj = odd2 ? (jj < 6u ? jj + 1u : 0u) : jj;
      Ac2[ii] = (7u * ct2 + pj < 108u) ? row * 3456u + pj * 32u + (cc & 1u) * 16u : 0x7ffffff0u;
    }
    Apl1 = F1 + 8u * (16u * rbA + (lane & 15u));
    Apl2 = F2 + 32u * lane;
    asm volatile("" : "+v"(Axo), "+v"(Awo[0]), "+v"(Awo[1]), "+v"(Awo[2]), "+v"(Awo[3]), "+v"(Ako[0]), "+v"(Ako[1]), "+v"(Ako[2]), "+v"(Ako[3]),
                      "+v"(Awa[0]), "+v"(Awa[1]), "+v"(Awa[2]), "+v"(Awa[3]), "+v"(Aka[0]), "+v"(Aka[1]), "+v"(Avb[0]), "+v"(Avb[1]), "+v"(Aoo));
    asm volatile("" : "+v"(Ac1[0]), "+v"(Ac1[1]), "+v"(Ac1[2]), "+v"(Ac1[3]), "+v"(Ac1[4]), "+v"(Ac2[0]), "+v"(Ac2[1]), "+v"(Ac2[2]), "+v"(Ac2[3]),
                      "+v"(Ac2[4]), "+v"(Ac2[5]), "+v"(Ac2[6]), "+v"(Act), "+v"(Apl1), "+v"(Apl2));
  };
  auto do_prefetch = [&]() __attribute__((always_inline)) {
    const unsigned lane = threadIdx.x & 63u, w = wv;
    const unsigned m0d = __builtin_amdgcn_readfirstlane(lb + 155648u + w * 1024u);
    const char* pw = a.wq + hA * 256u + (size_t)(rbA * 160u + w * 40u + (lane >> 4)) * 8192u + (lane & 15u) * 16u;
    asm volatile("s_mov_b32 m0, %0\n\ts_nop 0" ::"s"(m0d) : "memory");
#pragma unroll 1
    for (int i = 0; i < 10; ++i) asm volatile("global_load_lds_dwordx4 %0, off" ::"v"(pw + (size_t)i * 4 * 8192) : "memory");
  };
  {
    const int t = threadIdx.x, lane = t & 63, w = wv;
    float* sm = (float*)(lds + 65536);
    const bool crit = b < 40u, gemv = b < 70u, vec = b >= 74u && b < 128u, norm = b >= 128u;
    const int cc = t & 7, r = t >> 3;
    const int c16 = lane & 15, g = lane >> 4;
    char* const modg = ws + WS_MODG;
    float* const modf = (float*)(ws + WS_MOD);
    float* const slt = sm + 2560;
    auto silu_load = [&](u16 (&cr)[3]) __attribute__((always_inline)) {
#pragma unroll
      for (int i = 0; i < 3; ++i) cr[i] = ((const u16*)a.c)[t + 256 * i < 640 ? t + 256 * i : t];
    };
    auto silu_table = [&](const u16 (&cr)[3]) __attribute__((always_inline)) {
#pragma unroll
      for (int i = 0; i < 3; ++i) {
        const float cv = bf2f(cr[i]);
        if (t + 256 * i < 640) slt[t + 256 * i] = rbf(cv * __builtin_amdgcn_rcpf(1.f + __expf(-cv)));
      }
      __syncthreads();
    };
    auto silu_fma = [&](const u32x4& vv, float sl, float (&acc)[8]) __attribute__((always_inline)) {
#pragma unroll
      for (int jj = 0; jj < 4; ++jj) { acc[2 * jj] = fmaf(sl, blo(vv[jj]), acc[2 * jj]); acc[2 * jj + 1] = fmaf(sl, bhi(vv[jj]), acc[2 * jj + 1]); }
    };
    auto col_sum = [&](float (&acc)[8]) __attribute__((always_inline)) -> float {
#pragma unroll
      for (int jj = 0; jj < 8; ++jj) { acc[jj] += __shfl_xor(acc[jj], 8); acc[jj] += __shfl_xor(acc[jj], 16); acc[jj] += __shfl_xor(acc[jj], 32); }
      if (lane < 8) {
#pragma unroll
        for (int jj = 0; jj < 8; ++jj) sm[w * 64 + cc * 8 + jj] = acc[jj];
      }
      __syncthreads();
      const int tt = t & 63;
      return sm[tt] + sm[64 + tt] + sm[128 + tt] + sm[192 + tt];
    };
    if (crit) {
      const int gq = b >> 1, half = b & 1u;
      const char* wp = a.wmod + gq * 128 + cc * 16 + (size_t)(half * 320 + r) * 7680;
      u32x4 v[10]; u16 cr[3];
      silu_load(cr);
#pragma unroll
      for (int i = 0; i < 10; ++i) v[i] = *(const u32x4*)(wp + (size_t)i * 32 * 7680);
      do_glue();
      silu_table(cr);
      float acc[8] = {};
#pragma unroll
      for (int i = 0; i < 10; ++i) silu_fma(v[i], slt[half * 320 + r + 32 * i], acc);
      const float s4 = col_sum(acc);
      tag = MAKE_TAG;
      if (t < 64) st_granule(modg + ((gq * 64 + t) * 2 + half) * 8, s4, (unsigned)tag);
      STAMP_P0(0)
      do_P();
      u32x4 y[15];
#pragma unroll
      for (int u = 0; u < 3; ++u) {
        const int G0 = ((int)b * 4 + w) * 3 + u, G = G0 < 448 ? G0 : 447, nt = G / 56, kg = G - nt * 56, odd = nt & 1;
#pragma unroll
        for (int ii = 0; ii < 5; ++ii) {
          const int cidx = 64 * ii + lane, rw = cidx / 10, c10 = cidx - rw * 10, jj = c10 >> 1;
          const int nb = 5 * nt + (odd ? (jj < 4 ? jj + 1 : 0) : jj);
          y[5 * u + ii] = *(const u32x4*)(a.wd + (size_t)((kg < 54 ? kg : 0) * 32 + rw) * 1280 + nb * 32 + (c10 & 1) * 16);
        }
      }
      char* tile = (char*)(sm + 3200) + w * 5120;
#pragma unroll
      for (int u = 0; u < 3; ++u) {
        const int G0 = ((int)b * 4 + w) * 3 + u, G = G0 < 448 ? G0 : 447, kg = G % 56;
#pragma unroll
        for (int i = 0; i < 5; ++i) *(u32x4*)(tile + i * 1024 + lane * 16) = kg < 54 ? y[5 * u + i] : (u32x4){0u, 0u, 0u, 0u};
        u32x4 o0, o1, o2, o3, o4;
        tr_frags5((unsigned)(size_t)tile + (4u * g + ((lane >> 2) & 3u)) * 160u + 8u * (lane & 3u), o0, o1, o2, o3, o4);
        char* dp = ws + WS_WDP + (size_t)G * 5120 + lane * 16;
        st16_dev(dp, o0); st16_dev(dp + 1024, o1); st16_dev(dp + 2048, o2); st16_dev(dp + 3072, o3); st16_dev(dp + 4096, o4);
      }
      do_prefetch();
    } else if (!norm) {
      const int gq = b < 60u ? (int)b - 10 : (int)b - 40;
      const int u = b - 74u, ten = u / 27, cg = u - ten * 27;
      const char* wp = vec ? (ten ? a.wu : a.wg) + cg * 128 + cc * 16 + (size_t)r * 3456 : a.wmod + (gemv ? gq : 0) * 128 + cc * 16 + (size_t)r * 7680;
      const size_t rs = vec ? 32 * 3456 : 32 * 7680;
      if (vec) __builtin_amdgcn_s_sleep(48);
      u32x4 y[20]; u16 cr[3];
      silu_load(cr);
#pragma unroll
      for (int i = 0; i < 20; ++i) y[i] = *(const u32x4*)(wp + (size_t)i * rs);
      do_glue();
      tag = MAKE_TAG;
      if (gemv) {
        silu_table(cr);
        float acc[8] = {};
#pragma unroll
        for (int i = 0; i < 20; ++i) silu_fma(y[i], slt[r + 32 * i], acc);
        if (b >= 60u) {
          const char* wp2 = wp + 30 * 128;
#pragma unroll
          for (int i = 0; i < 20; ++i) y[i] = *(const u32x4*)(wp2 + (size_t)i * rs);
        }
        const float s4 = col_sum(acc);
        if (t < 64) st4_dev(modf + gq * 64 + t, rbf(s4));
        if (b < 60u) {
          if (t < 64) st_granule(modg + 20480 + ((gq - 30) * 64 + t) * 8, rbf(s4), (unsigned)tag);
        } else {
          float acc2[8] = {};
#pragma unroll
          for (int i = 0; i < 20; ++i) silu_fma(y[i], slt[r + 32 * i], acc2);
          __syncthreads();
          const float s5 = col_sum(acc2);
          if (t < 64) st4_dev(modf + (gq + 30) * 64 + t, rbf(s5));
        }
        STAMP_P0(0)
      } else if (vec) {
        do_P();
        do_prefetch();
        float* sv = sm + 512;
        {
          const int p0 = t, p1 = t + 256, p2 = t < 128 ? t + 512 : t;
          const char* const gp[3] = {modg + 20480 + p0 * 16, modg + 20480 + p1 * 16, modg + 20480 + p2 * 16};
          u32x4 gv[3];
          sweep_granules<3>(gp, (unsigned)tag, gv);
#pragma unroll
          for (int i = 0; i < 3; ++i) {
            const int pp_ = i == 0 ? p0 : i == 1 ? p1 : p2, col = 2 * pp_;
            const float v0 = __uint_as_float(gv[i][0]), v1 = __uint_as_float(gv[i][2]);
            if (col < 640) { sv[640 + col] = v0; sv[640 + col + 1] = v1; }
            else { sv[col - 640] = rbf(1.f + v0); sv[col - 640 + 1] = rbf(1.f + v1); }
          }
        }
        __syncthreads();
        float ac[8] = {}, bs[8] = {};
#pragma unroll
        for (int i = 0; i < 20; ++i) {
          const float c2 = sv[r + 32 * i], sb = sv[640 + r + 32 * i];
#pragma unroll
          for (int jj = 0; jj < 4; ++jj) {
            const float lo = blo(y[i][jj]), hi = bhi(y[i][jj]);
            ac[2 * jj] = fmaf(c2, lo, ac[2 * jj]); ac[2 * jj + 1] = fmaf(c2, hi, ac[2 * jj + 1]);
            bs[2 * jj] = fmaf(sb, lo, bs[2 * jj]); bs[2 * jj + 1] = fmaf(sb, hi, bs[2 * jj + 1]);
          }
        }
#pragma unroll
        for (int jj = 0; jj < 8; ++jj) {
          ac[jj] += __shfl_xor(ac[jj], 8); ac[jj] += __shfl_xor(ac[jj], 16); ac[jj] += __shfl_xor(ac[jj], 32);
          bs[jj] += __shfl_xor(bs[jj], 8); bs[jj] += __shfl_xor(bs[jj], 16); bs[jj] += __shfl_xor(bs[jj], 32);
        }
        float* rd2 = sm + 1792;
        if (lane < 8) {
#pragma unroll
          for (int jj = 0; jj < 8; ++jj) { rd2[w * 128 + cc * 8 + jj] = ac[jj]; rd2[w * 128 + 64 + cc * 8 + jj] = bs[jj]; }
        }
        __syncthreads();
        float* vecs = (float*)(ws + WS_VECS);
        if (t < 128) {
          const float v2 = rd2[t] + rd2[128 + t] + rd2[256 + t] + rd2[384 + t];
          const int n = cg * 64 + (t & 63), fb = n >> 4, ct = fb / 7, rel = fb - 7 * ct;
          const int jj = (ct & 1) ? (rel == 0 ? 6 : rel - 1) : rel;
          st4_dev(vecs + ct * 512 + jj * 64 + (2 * ten + (t >> 6)) * 16 + (n & 15), v2);
        }
        if (u == 0) st4_dev(vecs + 15 * 512 + 2 * 64 + t, 0.f);
      }
      if (!vec) {
        do_P();
        do_prefetch();
      }
    } else {
      const int bb = b - 128u;
      u32x4 v[5];
#pragma unroll
      for (int i = 0; i < 5; ++i) v[i] = *(const u32x4*)(a.x + (size_t)(bb * 16 + c16) * 1280 + (4 * w + g + 16 * i) * 16);
      do_glue();
      float* red = sm + 1280;
      float s = 0.f;
#pragma unroll
      for (int i = 0; i < 5; ++i)
#pragma unroll
        for (int jj = 0; jj < 4; ++jj) s += blo(v[i][jj]) + bhi(v[i][jj]);
      s += __shfl_xor(s, 16); s += __shfl_xor(s, 32);
      if (lane < 16) red[w * 16 + c16] = s;
      __syncthreads();
      const float mean = (red[c16] + red[16 + c16] + red[32 + c16] + red[48 + c16]) * (1.f / 640.f);
      float q = 0.f;
#pragma unroll
      for (int i = 0; i < 5; ++i)
#pragma unroll
        for (int jj = 0; jj < 4; ++jj) { const float d0 = blo(v[i][jj]) - mean, d1 = bhi(v[i][jj]) - mean; q = fmaf(d0, d0, q); q = fmaf(d1, d1, q); }
      q += __shfl_xor(q, 16); q += __shfl_xor(q, 32);
      if (lane < 16) red[64 + w * 16 + c16] = q;
      tag = MAKE_TAG;
      STAMP_P0(1)
      {
        const char* const gp[5] = {modg + t * 16, modg + (t + 256) * 16, modg + (t + 512) * 16, modg + (t + 768) * 16, modg + (t + 1024) * 16};
        u32x4 gv[5];
        sweep_granules<5>(gp, (unsigned)tag, gv);
#pragma unroll
        for (int i = 0; i < 5; ++i) sm[t + 256 * i] = rbf(__uint_as_float(gv[i][0]) + __uint_as_float(gv[i][2]));
      }
      STAMP_P0(2)
      __syncthreads();
      const float rstd = rsqrtf((red[64 + c16] + red[80 + c16] + red[96 + c16] + red[112 + c16]) * (1.f / 640.f) + 1e-5f);
      const int q64 = bb >> 2, qb = bb & 3;
#pragma unroll
      for (int i = 0; i < 5; ++i) {
        const int ch = 4 * w + g + 16 * i, kk = ch * 8;
        const f32x4 sa0 = *(const f32x4*)(sm + kk), sa1 = *(const f32x4*)(sm + kk + 4), ca0 = *(const f32x4*)(sm + 640 + kk), ca1 = *(const f32x4*)(sm + 640 + kk + 4);
        u32x4 o;
#pragma unroll
        for (int jj = 0; jj < 4; ++jj) {
          const float s0 = jj < 2 ? sa0[2 * jj] : sa1[2 * jj - 4], s1 = jj < 2 ? sa0[2 * jj + 1] : sa1[2 * jj - 3];
          const float c0 = rbf(1.f + (jj < 2 ? ca0[2 * jj] : ca1[2 * jj - 4])), c1 = rbf(1.f + (jj < 2 ? ca0[2 * jj + 1] : ca1[2 * jj - 3]));
          const float h0 = fmaf((blo(v[i][jj]) - mean) * rstd, c0, s0), h1 = fmaf((bhi(v[i][jj]) - mean) * rstd, c1, s1);
          o[jj] = pk2(h0 * QSC, h1 * QSC);
        }
        st16_dev(ws + WS_XH + ((size_t)(q64 * 20 + (w + 4 * i)) * 4 + qb) * 1024 + lane * 16, o);
      }
      publish(ws, F1 + 8u * (unsigned)bb, tag);
      STAMP_P0(3)
      do_P();
    }
    STAMP_P0(4)
  }
  {
    const unsigned w = wv;
    const char* sXH = ws + WS_XH + (size_t)(rbA * 4u + w) * (20u * 4096u);
    const char* sW = a.wq + hA * 256u - 4096 + 2u * 524288u;
    const char* sK = a.kc + ((size_t)hA * 2048u + 1536u) * 256u - 4096;
    const char* sV = a.vc + ((size_t)hA * 2048u + 1536u) * 256u - 4096;
    char* sO = ws + WS_OF + (size_t)(rbA * 4u + w) * (128u * 4096u) + hA * (4u * 4096u);
    const unsigned woff = lb + 4096u * w;
    const char* src1 = a.wo + (size_t)kg1 * (32u * 1280u) + nt1 * 160u - 2048;
    char* dst1 = ws + WS_WOP + ((size_t)nt1 * 128u + kg1) * 5120u + 2048;
    const unsigned m0c = __builtin_amdgcn_readfirstlane(lb + 131072u + w * 5120u + 2048u);
    const unsigned so2 = kg2 * (32u * 3456u) + ct2 * 224u;
    const char* src2 = (t2 ? a.wu : a.wg) + so2;
    char* dst2 = ws + WS_WFP + ((size_t)(t2 * 16u + ct2) * 640u + kg2 * 32u) * 224u;
    const unsigned rem2 = __builtin_amdgcn_readfirstlane(act2 ? 640u * 3456u - so2 : 0u);
    const unsigned myf = __builtin_amdgcn_readfirstlane(F2 + 8u * b);
    const char* pfk = a.kc + ((size_t)hA * 2048u + 1536u) * 256u + rbA * 16384u + w * 4096u;
    const char* pfv = a.vc + ((size_t)hA * 2048u + 1536u) * 256u + rbA * 16384u + w * 4096u;
    unsigned m0p = __builtin_amdgcn_readfirstlane(lb + 151552u + w * 2048u);
    asm volatile(
    "v_mov_b32 v64, 0\n\t"
    "v_mov_b32 v65, 0\n\t"
    "v_mov_b32 v66, 0\n\t"
    "v_mov_b32 v67, 0\n\t"
    "v_mov_b32 v68, 0\n\t"
    "v_mov_b32 v69, 0\n\t"
    "v_mov_b32 v70, 0\n\t"
    "v_mov_b32 v71, 0\n\t"
    "v_mov_b32 v72, 0\n\t"
    "v_mov_b32 v73, 0\n\t"
    "v_mov_b32 v74, 0\n\t"
    "v_mov_b32 v75, 0\n\t"
    "v_mov_b32 v76, 0\n\t"
    "v_mov_b32 v77, 0\n\t"
    "v_mov_b32 v78, 0\n\t"
    "v_mov_b32 v79, 0\n\t"
    "v_mov_b32 v80, 0\n\t"
    "v_mov_b32 v81, 0\n\t"
    "v_mov_b32 v82, 0\n\t"
    "v_mov_b32 v83, 0\n\t"
    "v_mov_b32 v84, 0\n\t"
    "v_mov_b32 v85, 0\n\t"
    "v_mov_b32 v86, 0\n\t"
    "v_mov_b32 v87, 0\n\t"
    "v_mov_b32 v88, 0\n\t"
    "v_mov_b32 v89, 0\n\t"
    "v_mov_b32 v90, 0\n\t"
    "v_mov_b32 v91, 0\n\t"
    "v_mov_b32 v92, 0\n\t"
    "v_mov_b32 v93, 0\n\t"
    "v_mov_b32 v94, 0\n\t"
    "v_mov_b32 v95, 0\n\t"
    "v_mov_b32 v96, 0\n\t"
    "v_mov_b32 v97, 0\n\t"
    "v_mov_b32 v98, 0\n\t"
    "v_mov_b32 v99, 0\n\t"
    "v_mov_b32 v100, 0\n\t"
    "v_mov_b32 v101, 0\n\t"
    "v_mov_b32 v102, 0\n\t"
    "v_mov_b32 v103, 0\n\t"
    "v_mov_b32 v104, 0\n\t"
    "v_mov_b32 v105, 0\n\t"
    "v_mov_b32 v106, 0\n\t"
    "v_mov_b32 v107, 0\n\t"
    "v_mov_b32 v108, 0\n\t"
    "v_mov_b32 v109, 0\n\t"
    "v_mov_b32 v110, 0\n\t"
    "v_mov_b32 v111, 0\n\t"
    "v_mov_b32 v112, 0\n\t"
    "v_mov_b32 v113, 0\n\t"
    "v_mov_b32 v114, 0\n\t"
    "v_mov_b32 v115, 0\n\t"
    "v_mov_b32 v116, 0\n\t"
    "v_mov_b32 v117, 0\n\t"
    "v_mov_b32 v118, 0\n\t"
    "v_mov_b32 v119, 0\n\t"
    "v_mov_b32 v120, 0\n\t"
    "v_mov_b32 v121, 0\n\t"
    "v_mov_b32 v122, 0\n\t"
    "v_mov_b32 v123, 0\n\t"
    "v_mov_b32 v124, 0\n\t"
    "v_mov_b32 v125, 0\n\t"
    "v_mov_b32 v126, 0\n\t"
    "v_mov_b32 v127, 0\n\t"
    "v_mov_b32 v128, 0\n\t"
    "v_mov_b32 v129, 0\n\t"
    "v_mov_b32 v130, 0\n\t"
    "v_mov_b32 v131, 0\n\t"
    "v_mov_b32 v132, 0\n\t"
    "v_mov_b32 v133, 0\n\t"
    "v_mov_b32 v134, 0\n\t"
    "v_mov_b32 v135, 0\n\t"
    "v_mov_b32 v136, 0\n\t"
    "v_mov_b32 v137, 0\n\t"
    "v_mov_b32 v138, 0\n\t"
    "v_mov_b32 v139, 0\n\t"
    "v_mov_b32 v140, 0\n\t"
    "v_mov_b32 v141, 0\n\t"
    "v_mov_b32 v142, 0\n\t"
    "v_mov_b32 v143, 0\n\t"
    "v_mov_b32 v144, 0\n\t"
    "v_mov_b32 v145, 0\n\t"
    "v_mov_b32 v146, 0\n\t"
    "v_mov_b32 v147, 0\n\t"
    "v_mov_b32 v148, 0\n\t"
    "v_mov_b32 v149, 0\n\t"
    "v_mov_b32 v150, 0\n\t"
    "v_mov_b32 v151, 0\n\t"
    "v_mov_b32 v152, 0\n\t"
    "v_mov_b32 v153, 0\n\t"
    "v_mov_b32 v154, 0\n\t"
    "v_mov_b32 v155, 0\n\t"
    "v_mov_b32 v156, 0\n\t"
    "v_mov_b32 v157, 0\n\t"
    "v_mov_b32 v158, 0\n\t"
    "v_mov_b32 v159, 0\n\t"
    "v_mov_b32 v160, 0\n\t"
    "v_mov_b32 v161, 0\n\t"
    "v_mov_b32 v162, 0\n\t"
    "v_mov_b32 v163, 0\n\t"
    "v_mov_b32 v164, 0\n\t"
    "v_mov_b32 v165, 0\n\t"
    "v_mov_b32 v166, 0\n\t"
    "v_mov_b32 v167, 0\n\t"
    "v_mov_b32 v168, 0\n\t"
    "v_mov_b32 v169, 0\n\t"
    "v_mov_b32 v170, 0\n\t"
    "v_mov_b32 v171, 0\n\t"
    "v_mov_b32 v172, 0\n\t"
    "v_mov_b32 v173, 0\n\t"
    "v_mov_b32 v174, 0\n\t"
    "v_mov_b32 v175, 0\n\t"
    "v_mov_b32 v176, 0\n\t"
    "v_mov_b32 v177, 0\n\t"
    "v_mov_b32 v178, 0\n\t"
    "v_mov_b32 v179, 0\n\t"
    "v_mov_b32 v180, 0\n\t"
    "v_mov_b32 v181, 0\n\t"
    "v_mov_b32 v182, 0\n\t"
    "v_mov_b32 v183, 0\n\t"
    "v_mov_b32 v184, 0\n\t"
    "v_mov_b32 v185, 0\n\t"
    "v_mov_b32 v186, 0\n\t"
    "v_mov_b32 v187, 0\n\t"
    "v_mov_b32 v188, 0\n\t"
    "v_mov_b32 v189, 0\n\t"
    "v_mov_b32 v190, 0\n\t"
    "v_mov_b32 v191, 0\n\t"
    "s_mov_b32 s69, 100000\n\t"
    "global_load_dwordx2 v[30:31], v61, s[66:67] sc1\n\t"
    "s_sleep 2\n\t"
    "LM_poll1_%=:\n\t"
    "global_load_dwordx2 v[32:33], v61, s[66:67] sc1\n\t"
    "s_waitcnt vmcnt(1)\n\t"
    "v_cmp_eq_u64 vcc, s[64:65], v[30:31]\n\t"
    "s_cmp_eq_u64 vcc, -1\n\t"
    "s_cbranch_scc1 LM_polld2_%=\n\t"
    "global_load_dwordx2 v[30:31], v61, s[66:67] sc1\n\t"
    "s_waitcnt vmcnt(1)\n\t"
    "v_cmp_eq_u64 vcc, s[64:65], v[32:33]\n\t"
    "s_cmp_eq_u64 vcc, -1\n\t"
    "s_cbranch_scc1 LM_polld2_%=\n\t"
    "s_sub_u32 s69, s69, 1\n\t"
    "s_cmp_lg_u32 s69, 0\n\t"
    "s_cbranch_scc1 LM_poll1_%=\n\t"
    "LM_polld2_%=:\n\t"
    "global_load_dwordx4 a[0:3], v1, s[36:37] offset:0 sc1\n\t"
    "global_load_dwordx4 a[4:7], v1, s[36:37] offset:1024 sc1\n\t"
    "global_load_dwordx4 a[8:11], v1, s[36:37] offset:2048 sc1\n\t"
    "global_load_dwordx4 a[12:15], v1, s[36:37] offset:3072 sc1\n\t"
    "s_add_u32 s36, s36, 4096\n\t"
    "s_addc_u32 s37, s37, 0\n\t"
    "global_load_dwordx4 a[16:19], v1, s[36:37] offset:0 sc1\n\t"
    "global_load_dwordx4 a[20:23], v1, s[36:37] offset:1024 sc1\n\t"
    "global_load_dwordx4 a[24:27], v1, s[36:37] offset:2048 sc1\n\t"
    "global_load_dwordx4 a[28:31], v1, s[36:37] offset:3072 sc1\n\t"
    "s_add_u32 s36, s36, 4096\n\t"
    "s_addc_u32 s37, s37, 0\n\t"
    "global_load_dwordx4 a[32:35], v1, s[36:37] offset:0 sc1\n\t"
    "global_load_dwordx4 a[36:39], v1, s[36:37] offset:1024 sc1\n\t"
    "global_load_dwordx4 a[40:43], v1, s[36:37] offset:2048 sc1\n\t"
    "global_load_dwordx4 a[44:47], v1, s[36:37] offset:3072 sc1\n\t"
    "s_add_u32 s36, s36, 4096\n\t"
    "s_addc_u32 s37, s37, 0\n\t"
    "s_mov_b32 s88, s62\n\t"
    "s_and_b32 s89, s63, 0xffff\n\t"
    "s_mov_b32 s90, s57\n\t"
    "s_mov_b32 s91, 0x00020000\n\t"
    "s_mov_b32 s92, s96\n\t"
    "s_and_b32 s93, s97, 0xffff\n\t"
    "s_cmp_eq_u32 s57, 0\n\t"
    "s_cselect_b32 s94, 0, 7168\n\t"
    "s_mov_b32 s95, 0x00020000\n\t"
    "s_movk_i32 s98, 0x1000\n\t"
    "s_xor_b32 s50, s48, 32768\n\t"
    "s_mov_b32 s51, s48\n\t"
    "s_add_u32 s52, s48, 65536\n\t"
    "v_mov_b32 v28, 0xff800000\n\t"
    "v_mov_b32 v29, 0xff800000\n\t"
    "s_mov_b32 s56, 0\n\t"
    "s_barrier\n\t"
    "ds_read_b64_tr_b16 a[128:129], v10 offset:0\n\t"
    "ds_read_b64_tr_b16 a[130:131], v11 offset:0\n\t"
    "ds_read_b64_tr_b16 a[132:133], v12 offset:0\n\t"
    "ds_read_b64_tr_b16 a[134:135], v13 offset:0\n\t"
    "ds_read_b64_tr_b16 a[136:137], v10 offset:512\n\t"
    "ds_read_b64_tr_b16 a[138:139], v11 offset:512\n\t"
    "s_mov_b32 s49, 4\n\t"
    "LM_3_%=:\n\t"
    "s_waitcnt vmcnt(8)\n\t"
    "s_waitcnt lgkmcnt(4)\n\t"
    "v_mfma_f32_16x16x32_bf16 v[64:67], a[128:131], a[0:3], v[64:67]\n\t"
    "ds_read_b64_tr_b16 a[140:141], v12 offset:512\n\t"
    "global_load_dwordx4 a[48:51], v1, s[36:37] offset:0 sc1\n\t"
    "v_mfma_f32_16x16x32_bf16 v[68:71], a[128:131], a[4:7], v[68:71]\n\t"
    "ds_read_b64_tr_b16 a[142:143], v13 offset:512\n\t"
    "v_mfma_f32_16x16x32_bf16 v[72:75], a[128:131], a[8:11], v[72:75]\n\t"
    "v_mfma_f32_16x16x32_bf16 v[76:79], a[128:131], a[12:15], v[76:79]\n\t"
    "s_waitcnt lgkmcnt(4)\n\t"
    "v_mfma_f32_16x16x32_bf16 v[80:83], a[132:135], a[0:3], v[80:83]\n\t"
    "ds_read_b64_tr_b16 a[128:129], v10 offset:1024\n\t"
    "v_mfma_f32_16x16x32_bf16 v[84:87], a[132:135], a[4:7], v[84:87]\n\t"
    "ds_read_b64_tr_b16 a[130:131], v11 offset:1024\n\t"
    "global_load_dwordx4 a[52:55], v1, s[36:37] offset:1024 sc1\n\t"
    "v_mfma_f32_16x16x32_bf16 v[88:91], a[132:135], a[8:11], v[88:91]\n\t"
    "v_mfma_f32_16x16x32_bf16 v[92:95], a[132:135], a[12:15], v[92:95]\n\t"
    "s_waitcnt lgkmcnt(4)\n\t"
    "v_mfma_f32_16x16x32_bf16 v[96:99], a[136:139], a[0:3], v[96:99]\n\t"
    "ds_read_b64_tr_b16 a[132:133], v12 offset:1024\n\t"
    "v_mfma_f32_16x16x32_bf16 v[100:103], a[136:139], a[4:7], v[100:103]\n\t"
    "ds_read_b64_tr_b16 a[134:135], v13 offset:1024\n\t"
    "v_mfma_f32_16x16x32_bf16 v[104:107], a[136:139], a[8:11], v[104:107]\n\t"
    "global_load_dwordx4 a[56:59], v1, s[36:37] offset:2048 sc1\n\t"
    "v_mfma_f32_16x16x32_bf16 v[108:111], a[136:139], a[12:15], v[108:111]\n\t"
    "s_waitcnt lgkmcnt(4)\n\t"
    "v_mfma_f32_16x16x32_bf16 v[112:115], a[140:143], a[0:3], v[112:115]\n\t"
    "ds_read_b64_tr_b16 a[136:137], v10 offset:1536\n\t"
    "v_mfma_f32_16x16x32_bf16 v[116:119], a[140:143], a[4:7], v[116:119]\n\t"
    "ds_read_b64_tr_b16 a[138:139], v11 offset:1536\n\t"
    "v_mfma_f32_16x16x32_bf16 v[120:123], a[140:143], a[8:11], v[120:123]\n\t"
    "v_mfma_f32_16x16x32_bf16 v[124:127], a[140:143], a[12:15], v[124:127]\n\t"
    "s_waitcnt lgkmcnt(4)\n\t"
    "v_mfma_f32_16x16x32_bf16 v[128:131], a[128:131], a[0:3], v[128:131]\n\t"
    "ds_read_b64_tr_b16 a[140:141], v12 offset:1536\n\t"
    "global_load_dwordx4 a[60:63], v1, s[36:37] offset:3072 sc1\n\t"
    "v_mfma_f32_16x16x32_bf16 v[132:135], a[128:131], a[4:7], v[132:135]\n\t"
    "ds_read_b64_tr_b16 a[142:143], v13 offset:1536\n\t"
    "s_add_u32 s36, s36, 4096\n\t"
    "s_addc_u32 s37, s37, 0\n\t"
    "v_mfma_f32_16x16x32_bf16 v[136:139], a[128:131], a[8:11], v[136:139]\n\t"
    "v_mfma_f32_16x16x32_bf16 v[140:143], a[128:131], a[12:15], v[140:143]\n\t"
    "s_waitcnt lgkmcnt(4)\n\t"
    "v_mfma_f32_16x16x32_bf16 v[144:147], a[132:135], a[0:3], v[144:147]\n\t"
    "ds_read_b64_tr_b16 a[128:129], v10 offset:8192\n\t"
    "v_mfma_f32_16x16x32_bf16 v[148:151], a[132:135], a[4:7], v[148:151]\n\t"
    "ds_read_b64_tr_b16 a[130:131], v11 offset:8192\n\t"
    "s_add_u32 m0, s50, 0\n\t"
    "s_nop 0\n\t"
    "global_load_lds_dwordx4 v2, s[38:39] offset:0\n\t"
    "v_mfma_f32_16x16x32_bf16 v[152:155], a[132:135], a[8:11], v[152:155]\n\t"
    "v_mfma_f32_16x16x32_bf16 v[156:159], a[132:135], a[12:15], v[156:159]\n\t"
    "s_waitcnt lgkmcnt(4)\n\t"
    "v_mfma_f32_16x16x32_bf16 v[160:163], a[136:139], a[0:3], v[160:163]\n\t"
    "ds_read_b64_tr_b16 a[132:133], v12 offset:8192\n\t"
    "v_mfma_f32_16x16x32_bf16 v[164:167], a[136:139], a[4:7], v[164:167]\n\t"
    "ds_read_b64_tr_b16 a[134:135], v13 offset:8192\n\t"
    "v_mfma_f32_16x16x32_bf16 v[168:171], a[136:139], a[8:11], v[168:171]\n\t"
    "global_load_lds_dwordx4 v3, s[38:39] offset:1024\n\t"
    "v_mfma_f32_16x16x32_bf16 v[172:175], a[136:139], a[12:15], v[172:175]\n\t"
    "s_waitcnt lgkmcnt(4)\n\t"
    "v_mfma_f32_16x16x32_bf16 v[176:179], a[140:143], a[0:3], v[176:179]\n\t"
    "ds_read_b64_tr_b16 a[136:137], v10 offset:8704\n\t"
    "v_mfma_f32_16x16x32_bf16 v[180:183], a[140:143], a[4:7], v[180:183]\n\t"
    "ds_read_b64_tr_b16 a[138:139], v11 offset:8704\n\t"
    "v_mfma_f32_16x16x32_bf16 v[184:187], a[140:143], a[8:11], v[184:187]\n\t"
    "v_mfma_f32_16x16x32_bf16 v[188:191], a[140:143], a[12:15], v[188:191]\n\t"
    "s_waitcnt vmcnt(10)\n\t"
    "s_waitcnt lgkmcnt(4)\n\t"
    "v_mfma_f32_16x16x32_bf16 v[64:67], a[128:131], a[16:19], v[64:67]\n\t"
    "ds_read_b64_tr_b16 a[140:141], v12 offset:8704\n\t"
    "global_load_dwordx4 a[0:3], v1, s[36:37] offset:0 sc1\n\t"
    "v_mfma_f32_16x16x32_bf16 v[68:71], a[128:131], a[20:23], v[68:71]\n\t"
    "ds_read_b64_tr_b16 a[142:143], v13 offset:8704\n\t"
    "v_mfma_f32_16x16x32_bf16 v[72:75], a[128:131], a[24:27], v[72:75]\n\t"
    "v_mfma_f32_16x16x32_bf16 v[76:79], a[128:131], a[28:31], v[76:79]\n\t"
    "s_waitcnt lgkmcnt(4)\n\t"
    "v_mfma_f32_16x16x32_bf16 v[80:83], a[132:135], a[16:19], v[80:83]\n\t"
    "ds_read_b64_tr_b16 a[128:129], v10 offset:9216\n\t"
    "v_mfma_f32_16x16x32_bf16 v[84:87], a[132:135], a[20:23], v[84:87]\n\t"
    "ds_read_b64_tr_b16 a[130:131], v11 offset:9216\n\t"
    "global_load_dwordx4 a[4:7], v1, s[36:37] offset:1024 sc1\n\t"
    "v_mfma_f32_16x16x32_bf16 v[88:91], a[132:135], a[24:27], v[88:91]\n\t"
    "v_mfma_f32_16x16x32_bf16 v[92:95], a[132:135], a[28:31], v[92:95]\n\t"
    "s_waitcnt lgkmcnt(4)\n\t"
    "v_mfma_f32_16x16x32_bf16 v[96:99], a[136:139], a[16:19], v[96:99]\n\t"
    "ds_read_b64_tr_b16 a[132:133], v12 offset:9216\n\t"
    "v_mfma_f32_16x16x32_bf16 v[100:103], a[136:139], a[20:23], v[100:103]\n\t"
    "ds_read_b64_tr_b16 a[134:135], v13 offset:9216\n\t"
    "v_mfma_f32_16x16x32_bf16 v[104:107], a[136:139], a[24:27], v[104:107]\n\t"
    "global_load_dwordx4 a[8:11], v1, s[36:37] offset:2048 sc1\n\t"
    "v_mfma_f32_16x16x32_bf16 v[108:111], a[136:139], a[28:31], v[108:111]\n\t"
    "s_waitcnt lgkmcnt(4)\n\t"
    "v_mfma_f32_16x16x32_bf16 v[112:115], a[140:143], a[16:19], v[112:115]\n\t"
    "ds_read_b64_tr_b16 a[136:137], v10 offset:9728\n\t"
    "v_mfma_f32_16x16x32_bf16 v[116:119], a[140:143], a[20:23], v[116:119]\n\t"
    "ds_read_b64_tr_b16 a[138:139], v11 offset:9728\n\t"
    "v_mfma_f32_16x16x32_bf16 v[120:123], a[140:143], a[24:27], v[120:123]\n\t"
    "v_mfma_f32_16x16x32_bf16 v[124:127], a[140:143], a[28:31], v[124:127]\n\t"
    "s_waitcnt lgkmcnt(4)\n\t"
    "v_mfma_f32_16x16x32_bf16 v[128:131], a[128:131], a[16:19], v[128:131]\n\t"
    "ds_read_b64_tr_b16 a[140:141], v12 offset:9728\n\t"
    "global_load_dwordx4 a[12:15], v1, s[36:37] offset:3072 sc1\n\t"
    "v_mfma_f32_16x16x32_bf16 v[132:135], a[128:131], a[20:23], v[132:135]\n\t"
    "ds_read_b64_tr_b16 a[142:143], v13 offset:9728\n\t"
    "s_add_u32 s36, s36, 4096\n\t"
    "s_addc_u32 s37, s37, 0\n\t"
    "v_mfma_f32_16x16x32_bf16 v[136:139], a[128:131], a[24:27], v[136:139]\n\t"
    "v_mfma_f32_16x16x32_bf16 v[140:143], a[128:131], a[28:31], v[140:143]\n\t"
    "s_waitcnt vmcnt(10)\n\t"
    "s_barrier\n\t"
    "s_waitcnt lgkmcnt(4)\n\t"
    "v_mfma_f32_16x16x32_bf16 v[144:147], a[132:135], a[16:19], v[144:147]\n\t"
    "ds_read_b64_tr_b16 a[128:129], v10 offset:16384\n\t"
    "v_mfma_f32_16x16x32_bf16 v[148:151], a[132:135], a[20:23], v[148:151]\n\t"
    "ds_read_b64_tr_b16 a[130:131], v11 offset:16384\n\t"
    "global_load_lds_dwordx4 v4, s[38:39] offset:2048\n\t"
    "v_mfma_f32_16x16x32_bf16 v[152:155], a[132:135], a[24:27], v[152:155]\n\t"
    "v_mfma_f32_16x16x32_bf16 v[156:159], a[132:135], a[28:31], v[156:159]\n\t"
    "s_waitcnt lgkmcnt(4)\n\t"
    "v_mfma_f32_16x16x32_bf16 v[160:163], a[136:139], a[16:19], v[160:163]\n\t"
    "ds_read_b64_tr_b16 a[132:133], v12 offset:16384\n\t"
    "v_mfma_f32_16x16x32_bf16 v[164:167], a[136:139], a[20:23], v[164:167]\n\t"
    "ds_read_b64_tr_b16 a[134:135], v13 offset:16384\n\t"
    "v_mfma_f32_16x16x32_bf16 v[168:171], a[136:139], a[24:27], v[168:171]\n\t"
    "global_load_lds_dwordx4 v5, s[38:39] offset:3072\n\t"
    "v_mfma_f32_16x16x32_bf16 v[172:175], a[136:139], a[28:31], v[172:175]\n\t"
    "s_waitcnt lgkmcnt(4)\n\t"
    "v_mfma_f32_16x16x32_bf16 v[176:179], a[140:143], a[16:19], v[176:179]\n\t"
    "ds_read_b64_tr_b16 a[136:137], v10 offset:16896\n\t"
    "v_mfma_f32_16x16x32_bf16 v[180:183], a[140:143], a[20:23], v[180:183]\n\t"
    "ds_read_b64_tr_b16 a[138:139], v11 offset:16896\n\t"
    "v_mfma_f32_16x16x32_bf16 v[184:187], a[140:143], a[24:27], v[184:187]\n\t"
    "v_mfma_f32_16x16x32_bf16 v[188:191], a[140:143], a[28:31], v[188:191]\n\t"
    "s_add_u32 s38, s38, 524288\n\t"
    "s_addc_u32 s39, s39, 0\n\t"
    "s_waitcnt vmcnt(12)\n\t"
    "s_waitcnt lgkmcnt(4)\n\t"
    "v_mfma_f32_16x16x32_bf16 v[64:67], a[128:131], a[32:35], v[64:67]\n\t"
    "ds_read_b64_tr_b16 a[140:141], v12 offset:16896\n\t"
    "global_load_dwordx4 a[16:19], v1, s[36:37] offset:0 sc1\n\t"
    "v_mfma_f32_16x16x32_bf16 v[68:71], a[128:131], a[36:39], v[68:71]\n\t"
    "ds_read_b64_tr_b16 a[142:143], v13 offset:16896\n\t"
    "v_mfma_f32_16x16x32_bf16 v[72:75], a[128:131], a[40:43], v[72:75]\n\t"
    "v_mfma_f32_16x16x32_bf16 v[76:79], a[128:131], a[44:47], v[76:79]\n\t"
    "s_waitcnt lgkmcnt(4)\n\t"
    "v_mfma_f32_16x16x32_bf16 v[80:83], a[132:135], a[32:35], v[80:83]\n\t"
    "ds_read_b64_tr_b16 a[128:129], v10 offset:17408\n\t"
    "v_mfma_f32_16x16x32_bf16 v[84:87], a[132:135], a[36:39], v[84:87]\n\t"
    "ds_read_b64_tr_b16 a[130:131], v11 offset:17408\n\t"
    "global_load_dwordx4 a[20:23], v1, s[36:37] offset:1024 sc1\n\t"
    "v_mfma_f32_16x16x32_bf16 v[88:91], a[132:135], a[40:43], v[88:91]\n\t"
    "v_mfma_f32_16x16x32_bf16 v[92:95], a[132:135], a[44:47], v[92:95]\n\t"
    "s_waitcnt lgkmcnt(4)\n\t"
    "v_mfma_f32_16x16x32_bf16 v[96:99], a[136:139], a[32:35], v[96:99]\n\t"
    "ds_read_b64_tr_b16 a[132:133], v12 offset:17408\n\t"
    "v_mfma_f32_16x16x32_bf16 v[100:103], a[136:139], a[36:39], v[100:103]\n\t"
    "ds_read_b64_tr_b16 a[134:135], v13 offset:17408\n\t"
    "v_mfma_f32_16x16x32_bf16 v[104:107], a[136:139], a[40:43], v[104:107]\n\t"
    "global_load_dwordx4 a[24:27], v1, s[36:37] offset:2048 sc1\n\t"
    "v_mfma_f32_16x16x32_bf16 v[108:111], a[136:139], a[44:47], v[108:111]\n\t"
    "s_waitcnt lgkmcnt(4)\n\t"
    "v_mfma_f32_16x16x32_bf16 v[112:115], a[140:143], a[32:35], v[112:115]\n\t"
    "ds_read_b64_tr_b16 a[136:137], v10 offset:17920\n\t"
    "v_mfma_f32_16x16x32_bf16 v[116:119], a[140:143], a[36:39], v[116:119]\n\t"
    "ds_read_b64_tr_b16 a[138:139], v11 offset:17920\n\t"
    "v_mfma_f32_16x16x32_bf16 v[120:123], a[140:143], a[40:43], v[120:123]\n\t"
    "v_mfma_f32_16x16x32_bf16 v[124:127], a[140:143], a[44:47], v[124:127]\n\t"
    "s_waitcnt lgkmcnt(4)\n\t"
    "v_mfma_f32_16x16x32_bf16 v[128:131], a[128:131], a[32:35], v[128:131]\n\t"
    "ds_read_b64_tr_b16 a[140:141], v12 offset:17920\n\t"
    "global_load_dwordx4 a[28:31], v1, s[36:37] offset:3072 sc1\n\t"
    "v_mfma_f32_16x16x32_bf16 v[132:135], a[128:131], a[36:39], v[132:135]\n\t"
    "ds_read_b64_tr_b16 a[142:143], v13 offset:17920\n\t"
    "s_add_u32 s36, s36, 4096\n\t"
    "s_addc_u32 s37, s37, 0\n\t"
    "v_mfma_f32_16x16x32_bf16 v[136:139], a[128:131], a[40:43], v[136:139]\n\t"
    "v_mfma_f32_16x16x32_bf16 v[140:143], a[128:131], a[44:47], v[140:143]\n\t"
    "s_waitcnt lgkmcnt(4)\n\t"
    "v_mfma_f32_16x16x32_bf16 v[144:147], a[132:135], a[32:35], v[144:147]\n\t"
    "ds_read_b64_tr_b16 a[128:129], v10 offset:24576\n\t"
    "v_mfma_f32_16x16x32_bf16 v[148:151], a[132:135], a[36:39], v[148:151]\n\t"
    "ds_read_b64_tr_b16 a[130:131], v11 offset:24576\n\t"
    "s_add_u32 m0, s50, 16384\n\t"
    "s_nop 0\n\t"
    "global_load_lds_dwordx4 v2, s[38:39] offset:0\n\t"
    "v_mfma_f32_16x16x32_bf16 v[152:155], a[132:135], a[40:43], v[152:155]\n\t"
    "v_mfma_f32_16x16x32_bf16 v[156:159], a[132:135], a[44:47], v[156:159]\n\t"
    "s_waitcnt lgkmcnt(4)\n\t"
    "v_mfma_f32_16x16x32_bf16 v[160:163], a[136:139], a[32:35], v[160:163]\n\t"
    "ds_read_b64_tr_b16 a[132:133], v12 offset:24576\n\t"
    "v_mfma_f32_16x16x32_bf16 v[164:167], a[136:139], a[36:39], v[164:167]\n\t"
    "ds_read_b64_tr_b16 a[134:135], v13 offset:24576\n\t"
    "v_mfma_f32_16x16x32_bf16 v[168:171], a[136:139], a[40:43], v[168:171]\n\t"
    "global_load_lds_dwordx4 v3, s[38:39] offset:1024\n\t"
    "v_mfma_f32_16x16x32_bf16 v[172:175], a[136:139], a[44:47], v[172:175]\n\t"
    "s_waitcnt lgkmcnt(4)\n\t"
    "v_mfma_f32_16x16x32_bf16 v[176:179], a[140:143], a[32:35], v[176:179]\n\t"
    "ds_read_b64_tr_b16 a[136:137], v10 offset:25088\n\t"
    "v_mfma_f32_16x16x32_bf16 v[180:183], a[140:143], a[36:39], v[180:183]\n\t"
    "ds_read_b64_tr_b16 a[138:139], v11 offset:25088\n\t"
    "v_mfma_f32_16x16x32_bf16 v[184:187], a[140:143], a[40:43], v[184:187]\n\t"
    "v_mfma_f32_16x16x32_bf16 v[188:191], a[140:143], a[44:47], v[188:191]\n\t"
    "s_waitcnt vmcnt(14)\n\t"
    "s_waitcnt lgkmcnt(4)\n\t"
    "v_mfma_f32_16x16x32_bf16 v[64:67], a[128:131], a[48:51], v[64:67]\n\t"
    "ds_read_b64_tr_b16 a[140:141], v12 offset:25088\n\t"
    "global_load_dwordx4 a[32:35], v1, s[36:37] offset:0 sc1\n\t"
    "v_mfma_f32_16x16x32_bf16 v[68:71], a[128:131], a[52:55], v[68:71]\n\t"
    "ds_read_b64_tr_b16 a[142:143], v13 offset:25088\n\t"
    "v_mfma_f32_16x16x32_bf16 v[72:75], a[128:131], a[56:59], v[72:75]\n\t"
    "v_mfma_f32_16x16x32_bf16 v[76:79], a[128:131], a[60:63], v[76:79]\n\t"
    "s_waitcnt lgkmcnt(4)\n\t"
    "v_mfma_f32_16x16x32_bf16 v[80:83], a[132:135], a[48:51], v[80:83]\n\t"
    "ds_read_b64_tr_b16 a[128:129], v10 offset:25600\n\t"
    "v_mfma_f32_16x16x32_bf16 v[84:87], a[132:135], a[52:55], v[84:87]\n\t"
    "ds_read_b64_tr_b16 a[130:131], v11 offset:25600\n\t"
    "global_load_dwordx4 a[36:39], v1, s[36:37] offset:1024 sc1\n\t"
    "v_mfma_f32_16x16x32_bf16 v[88:91], a[132:135], a[56:59], v[88:91]\n\t"
    "v_mfma_f32_16x16x32_bf16 v[92:95], a[132:135], a[60:63], v[92:95]\n\t"
    "s_waitcnt lgkmcnt(4)\n\t"
    "v_mfma_f32_16x16x32_bf16 v[96:99], a[136:139], a[48:51], v[96:99]\n\t"
    "ds_read_b64_tr_b16 a[132:133], v12 offset:25600\n\t"
    "v_mfma_f32_16x16x32_bf16 v[100:103], a[136:139], a[52:55], v[100:103]\n\t"
    "ds_read_b64_tr_b16 a[134:135], v13 offset:25600\n\t"
    "v_mfma_f32_16x16x32_bf16 v[104:107], a[136:139], a[56:59], v[104:107]\n\t"
    "global_load_dwordx4 a[40:43], v1, s[36:37] offset:2048 sc1\n\t"
    "v_mfma_f32_16x16x32_bf16 v[108:111], a[136:139], a[60:63], v[108:111]\n\t"
    "s_waitcnt lgkmcnt(4)\n\t"
    "v_mfma_f32_16x16x32_bf16 v[112:115], a[140:143], a[48:51], v[112:115]\n\t"
    "ds_read_b64_tr_b16 a[136:137], v10 offset:26112\n\t"
    "v_mfma_f32_16x16x32_bf16 v[116:119], a[140:143], a[52:55], v[116:119]\n\t"
    "ds_read_b64_tr_b16 a[138:139], v11 offset:26112\n\t"
    "v_mfma_f32_16x16x32_bf16 v[120:123], a[140:143], a[56:59], v[120:123]\n\t"
    "v_xor_b32 v10, 32768, v10\n\t"
    "v_xor_b32 v11, 32768, v11\n\t"
    "v_mfma_f32_16x16x32_bf16 v[124:127], a[140:143], a[60:63], v[124:127]\n\t"
    "s_waitcnt lgkmcnt(4)\n\t"
    "v_mfma_f32_16x16x32_bf16 v[128:131], a[128:131], a[48:51], v[128:131]\n\t"
    "ds_read_b64_tr_b16 a[140:141], v12 offset:26112\n\t"
    "global_load_dwordx4 a[44:47], v1, s[36:37] offset:3072 sc1\n\t"
    "v_mfma_f32_16x16x32_bf16 v[132:135], a[128:131], a[52:55], v[132:135]\n\t"
    "ds_read_b64_tr_b16 a[142:143], v13 offset:26112\n\t"
    "s_add_u32 s36, s36, 4096\n\t"
    "s_addc_u32 s37, s37, 0\n\t"
    "v_mfma_f32_16x16x32_bf16 v[136:139], a[128:131], a[56:59], v[136:139]\n\t"
    "v_xor_b32 v12, 32768, v12\n\t"
    "v_xor_b32 v13, 32768, v13\n\t"
    "v_mfma_f32_16x16x32_bf16 v[140:143], a[128:131], a[60:63], v[140:143]\n\t"
    "s_waitcnt vmcnt(10)\n\t"
    "s_barrier\n\t"
    "s_waitcnt lgkmcnt(4)\n\t"
    "v_mfma_f32_16x16x32_bf16 v[144:147], a[132:135], a[48:51], v[144:147]\n\t"
    "ds_read_b64_tr_b16 a[128:129], v10 offset:0\n\t"
    "v_mfma_f32_16x16x32_bf16 v[148:151], a[132:135], a[52:55], v[148:151]\n\t"
    "ds_read_b64_tr_b16 a[130:131], v11 offset:0\n\t"
    "global_load_lds_dwordx4 v4, s[38:39] offset:2048\n\t"
    "v_mfma_f32_16x16x32_bf16 v[152:155], a[132:135], a[56:59], v[152:155]\n\t"
    "v_mfma_f32_16x16x32_bf16 v[156:159], a[132:135], a[60:63], v[156:159]\n\t"
    "s_waitcnt lgkmcnt(4)\n\t"
    "v_mfma_f32_16x16x32_bf16 v[160:163], a[136:139], a[48:51], v[160:163]\n\t"
    "ds_read_b64_tr_b16 a[132:133], v12 offset:0\n\t"
    "v_mfma_f32_16x16x32_bf16 v[164:167], a[136:139], a[52:55], v[164:167]\n\t"
    "ds_read_b64_tr_b16 a[134:135], v13 offset:0\n\t"
    "v_mfma_f32_16x16x32_bf16 v[168:171], a[136:139], a[56:59], v[168:171]\n\t"
    "global_load_lds_dwordx4 v5, s[38:39] offset:3072\n\t"
    "v_mfma_f32_16x16x32_bf16 v[172:175], a[136:139], a[60:63], v[172:175]\n\t"
    "s_waitcnt lgkmcnt(4)\n\t"
    "v_mfma_f32_16x16x32_bf16 v[176:179], a[140:143], a[48:51], v[176:179]\n\t"
    "ds_read_b64_tr_b16 a[136:137], v10 offset:512\n\t"
    "v_mfma_f32_16x16x32_bf16 v[180:183], a[140:143], a[52:55], v[180:183]\n\t"
    "ds_read_b64_tr_b16 a[138:139], v11 offset:512\n\t"
    "v_mfma_f32_16x16x32_bf16 v[184:187], a[140:143], a[56:59], v[184:187]\n\t"
    "v_mfma_f32_16x16x32_bf16 v[188:191], a[140:143], a[60:63], v[188:191]\n\t"
    "s_add_u32 s38, s38, 524288\n\t"
    "s_addc_u32 s39, s39, 0\n\t"
    "s_xor_b32 s50, s50, 32768\n\t"
    "s_sub_u32 s49, s49, 1\n\t"
    "s_cmp_lg_u32 s49, 0\n\t"
    "s_cbranch_scc1 LM_3_%=\n\t"
    "s_waitcnt lgkmcnt(4)\n\t"
    "v_mfma_f32_16x16x32_bf16 v[64:67], a[128:131], a[0:3], v[64:67]\n\t"
    "ds_read_b64_tr_b16 a[140:141], v12 offset:512\n\t"
    "global_load_dwordx4 a[48:51], v1, s[36:37] offset:0 sc1\n\t"
    "v_mfma_f32_16x16x32_bf16 v[68:71], a[128:131], a[4:7], v[68:71]\n\t"
    "ds_read_b64_tr_b16 a[142:143], v13 offset:512\n\t"
    "v_mfma_f32_16x16x32_bf16 v[72:75], a[128:131], a[8:11], v[72:75]\n\t"
    "v_mfma_f32_16x16x32_bf16 v[76:79], a[128:131], a[12:15], v[76:79]\n\t"
    "s_waitcnt lgkmcnt(4)\n\t"
    "v_mfma_f32_16x16x32_bf16 v[80:83], a[132:135], a[0:3], v[80:83]\n\t"
    "ds_read_b64_tr_b16 a[128:129], v10 offset:1024\n\t"
    "v_mfma_f32_16x16x32_bf16 v[84:87], a[132:135], a[4:7], v[84:87]\n\t"
    "ds_read_b64_tr_b16 a[130:131], v11 offset:1024\n\t"
    "global_load_dwordx4 a[52:55], v1, s[36:37] offset:1024 sc1\n\t"
    "v_mfma_f32_16x16x32_bf16 v[88:91], a[132:135], a[8:11], v[88:91]\n\t"
    "v_mfma_f32_16x16x32_bf16 v[92:95], a[132:135], a[12:15], v[92:95]\n\t"
    "s_waitcnt lgkmcnt(4)\n\t"
    "v_mfma_f32_16x16x32_bf16 v[96:99], a[136:139], a[0:3], v[96:99]\n\t"
    "ds_read_b64_tr_b16 a[132:133], v12 offset:1024\n\t"
    "v_mfma_f32_16x16x32_bf16 v[100:103], a[136:139], a[4:7], v[100:103]\n\t"
    "ds_read_b64_tr_b16 a[134:135], v13 offset:1024\n\t"
    "v_mfma_f32_16x16x32_bf16 v[104:107], a[136:139], a[8:11], v[104:107]\n\t"
    "global_load_dwordx4 a[56:59], v1, s[36:37] offset:2048 sc1\n\t"
    "v_mfma_f32_16x16x32_bf16 v[108:111], a[136:139], a[12:15], v[108:111]\n\t"
    "s_waitcnt lgkmcnt(4)\n\t"
    "v_mfma_f32_16x16x32_bf16 v[112:115], a[140:143], a[0:3], v[112:115]\n\t"
    "ds_read_b64_tr_b16 a[136:137], v10 offset:1536\n\t"
    "v_mfma_f32_16x16x32_bf16 v[116:119], a[140:143], a[4:7], v[116:119]\n\t"
    "ds_read_b64_tr_b16 a[138:139], v11 offset:1536\n\t"
    "v_mfma_f32_16x16x32_bf16 v[120:123], a[140:143], a[8:11], v[120:123]\n\t"
    "v_mfma_f32_16x16x32_bf16 v[124:127], a[140:143], a[12:15], v[124:127]\n\t"
    "s_waitcnt lgkmcnt(4)\n\t"
    "v_mfma_f32_16x16x32_bf16 v[128:131], a[128:131], a[0:3], v[128:131]\n\t"
    "ds_read_b64_tr_b16 a[140:141], v12 offset:1536\n\t"
    "global_load_dwordx4 a[60:63], v1, s[36:37] offset:3072 sc1\n\t"
    "v_mfma_f32_16x16x32_bf16 v[132:135], a[128:131], a[4:7], v[132:135]\n\t"
    "ds_read_b64_tr_b16 a[142:143], v13 offset:1536\n\t"
    "s_add_u32 s36, s36, 4096\n\t"
    "s_addc_u32 s37, s37, 0\n\t"
    "v_mfma_f32_16x16x32_bf16 v[136:139], a[128:131], a[8:11], v[136:139]\n\t"
    "v_mfma_f32_16x16x32_bf16 v[140:143], a[128:131], a[12:15], v[140:143]\n\t"
    "s_waitcnt lgkmcnt(4)\n\t"
    "v_mfma_f32_16x16x32_bf16 v[144:147], a[132:135], a[0:3], v[144:147]\n\t"
    "ds_read_b64_tr_b16 a[128:129], v10 offset:8192\n\t"
    "v_mfma_f32_16x16x32_bf16 v[148:151], a[132:135], a[4:7], v[148:151]\n\t"
    "ds_read_b64_tr_b16 a[130:131], v11 offset:8192\n\t"
    "s_add_u32 m0, s51, 32768\n\t"
    "s_nop 0\n\t"
    "global_load_lds_dwordx4 v6, s[42:43] offset:0\n\t"
    "v_mfma_f32_16x16x32_bf16 v[152:155], a[132:135], a[8:11], v[152:155]\n\t"
    "v_mfma_f32_16x16x32_bf16 v[156:159], a[132:135], a[12:15], v[156:159]\n\t"
    "s_waitcnt lgkmcnt(4)\n\t"
    "v_mfma_f32_16x16x32_bf16 v[160:163], a[136:139], a[0:3], v[160:163]\n\t"
    "ds_read_b64_tr_b16 a[132:133], v12 offset:8192\n\t"
    "v_mfma_f32_16x16x32_bf16 v[164:167], a[136:139], a[4:7], v[164:167]\n\t"
    "ds_read_b64_tr_b16 a[134:135], v13 offset:8192\n\t"
    "v_mfma_f32_16x16x32_bf16 v[168:171], a[136:139], a[8:11], v[168:171]\n\t"
    "global_load_lds_dwordx4 v7, s[42:43] offset:1024\n\t"
    "v_mfma_f32_16x16x32_bf16 v[172:175], a[136:139], a[12:15], v[172:175]\n\t"
    "s_waitcnt lgkmcnt(4)\n\t"
    "v_mfma_f32_16x16x32_bf16 v[176:179], a[140:143], a[0:3], v[176:179]\n\t"
    "ds_read_b64_tr_b16 a[136:137], v10 offset:8704\n\t"
    "v_mfma_f32_16x16x32_bf16 v[180:183], a[140:143], a[4:7], v[180:183]\n\t"
    "ds_read_b64_tr_b16 a[138:139], v11 offset:8704\n\t"
    "global_load_lds_dwordx4 v8, s[42:43] offset:2048\n\t"
    "v_mfma_f32_16x16x32_bf16 v[184:187], a[140:143], a[8:11], v[184:187]\n\t"
    "v_mfma_f32_16x16x32_bf16 v[188:191], a[140:143], a[12:15], v[188:191]\n\t"
    "global_load_lds_dwordx4 v9, s[42:43] offset:3072\n\t"
    "s_add_u32 s42, s42, 16384\n\t"
    "s_addc_u32 s43, s43, 0\n\t"
    "s_waitcnt vmcnt(16)\n\t"
    "s_waitcnt lgkmcnt(4)\n\t"
    "v_mfma_f32_16x16x32_bf16 v[64:67], a[128:131], a[16:19], v[64:67]\n\t"
    "ds_read_b64_tr_b16 a[140:141], v12 offset:8704\n\t"
    "s_mov_b32 m0, s52\n\t"
    "s_nop 0\n\t"
    "global_load_lds_dwordx4 v6, s[44:45] offset:0\n\t"
    "v_mfma_f32_16x16x32_bf16 v[68:71], a[128:131], a[20:23], v[68:71]\n\t"
    "ds_read_b64_tr_b16 a[142:143], v13 offset:8704\n\t"
    "v_mfma_f32_16x16x32_bf16 v[72:75], a[128:131], a[24:27], v[72:75]\n\t"
    "v_mfma_f32_16x16x32_bf16 v[76:79], a[128:131], a[28:31], v[76:79]\n\t"
    "s_waitcnt lgkmcnt(4)\n\t"
    "v_mfma_f32_16x16x32_bf16 v[80:83], a[132:135], a[16:19], v[80:83]\n\t"
    "ds_read_b64_tr_b16 a[128:129], v10 offset:9216\n\t"
    "global_load_lds_dwordx4 v7, s[44:45] offset:1024\n\t"
    "v_mfma_f32_16x16x32_bf16 v[84:87], a[132:135], a[20:23], v[84:87]\n\t"
    "ds_read_b64_tr_b16 a[130:131], v11 offset:9216\n\t"
    "v_mfma_f32_16x16x32_bf16 v[88:91], a[132:135], a[24:27], v[88:91]\n\t"
    "v_mfma_f32_16x16x32_bf16 v[92:95], a[132:135], a[28:31], v[92:95]\n\t"
    "s_waitcnt lgkmcnt(4)\n\t"
    "v_mfma_f32_16x16x32_bf16 v[96:99], a[136:139], a[16:19], v[96:99]\n\t"
    "ds_read_b64_tr_b16 a[132:133], v12 offset:9216\n\t"
    "global_load_lds_dwordx4 v8, s[44:45] offset:2048\n\t"
    "v_mfma_f32_16x16x32_bf16 v[100:103], a[136:139], a[20:23], v[100:103]\n\t"
    "ds_read_b64_tr_b16 a[134:135], v13 offset:9216\n\t"
    "v_mfma_f32_16x16x32_bf16 v[104:107], a[136:139], a[24:27], v[104:107]\n\t"
    "v_mfma_f32_16x16x32_bf16 v[108:111], a[136:139], a[28:31], v[108:111]\n\t"
    "s_waitcnt lgkmcnt(4)\n\t"
    "v_mfma_f32_16x16x32_bf16 v[112:115], a[140:143], a[16:19], v[112:115]\n\t"
    "ds_read_b64_tr_b16 a[136:137], v10 offset:9728\n\t"
    "global_load_lds_dwordx4 v9, s[44:45] offset:3072\n\t"
    "v_mfma_f32_16x16x32_bf16 v[116:119], a[140:143], a[20:23], v[116:119]\n\t"
    "ds_read_b64_tr_b16 a[138:139], v11 offset:9728\n\t"
    "v_mfma_f32_16x16x32_bf16 v[120:123], a[140:143], a[24:27], v[120:123]\n\t"
    "v_mfma_f32_16x16x32_bf16 v[124:127], a[140:143], a[28:31], v[124:127]\n\t"
    "s_waitcnt lgkmcnt(4)\n\t"
    "v_mfma_f32_16x16x32_bf16 v[128:131], a[128:131], a[16:19], v[128:131]\n\t"
    "ds_read_b64_tr_b16 a[140:141], v12 offset:9728\n\t"
    "s_add_u32 s44, s44, 16384\n\t"
    "s_addc_u32 s45, s45, 0\n\t"
    "s_add_u32 m0, s52, 16384\n\t"
    "s_nop 0\n\t"
    "global_load_lds_dwordx4 v6, s[44:45] offset:0\n\t"
    "v_mfma_f32_16x16x32_bf16 v[132:135], a[128:131], a[20:23], v[132:135]\n\t"
    "ds_read_b64_tr_b16 a[142:143], v13 offset:9728\n\t"
    "v_mfma_f32_16x16x32_bf16 v[136:139], a[128:131], a[24:27], v[136:139]\n\t"
    "v_mfma_f32_16x16x32_bf16 v[140:143], a[128:131], a[28:31], v[140:143]\n\t"
    "s_waitcnt vmcnt(13)\n\t"
    "s_barrier\n\t"
    "s_waitcnt lgkmcnt(4)\n\t"
    "v_mfma_f32_16x16x32_bf16 v[144:147], a[132:135], a[16:19], v[144:147]\n\t"
    "ds_read_b64_tr_b16 a[128:129], v10 offset:16384\n\t"
    "global_load_lds_dwordx4 v7, s[44:45] offset:1024\n\t"
    "v_mfma_f32_16x16x32_bf16 v[148:151], a[132:135], a[20:23], v[148:151]\n\t"
    "ds_read_b64_tr_b16 a[130:131], v11 offset:16384\n\t"
    "v_mfma_f32_16x16x32_bf16 v[152:155], a[132:135], a[24:27], v[152:155]\n\t"
    "v_mfma_f32_16x16x32_bf16 v[156:159], a[132:135], a[28:31], v[156:159]\n\t"
    "s_waitcnt lgkmcnt(4)\n\t"
    "v_mfma_f32_16x16x32_bf16 v[160:163], a[136:139], a[16:19], v[160:163]\n\t"
    "ds_read_b64_tr_b16 a[132:133], v12 offset:16384\n\t"
    "global_load_lds_dwordx4 v8, s[44:45] offset:2048\n\t"
    "v_mfma_f32_16x16x32_bf16 v[164:167], a[136:139], a[20:23], v[164:167]\n\t"
    "ds_read_b64_tr_b16 a[134:135], v13 offset:16384\n\t"
    "v_mfma_f32_16x16x32_bf16 v[168:171], a[136:139], a[24:27], v[168:171]\n\t"
    "v_mfma_f32_16x16x32_bf16 v[172:175], a[136:139], a[28:31], v[172:175]\n\t"
    "s_waitcnt lgkmcnt(4)\n\t"
    "v_mfma_f32_16x16x32_bf16 v[176:179], a[140:143], a[16:19], v[176:179]\n\t"
    "ds_read_b64_tr_b16 a[136:137], v10 offset:16896\n\t"
    "global_load_lds_dwordx4 v9, s[44:45] offset:3072\n\t"
    "v_mfma_f32_16x16x32_bf16 v[180:183], a[140:143], a[20:23], v[180:183]\n\t"
    "ds_read_b64_tr_b16 a[138:139], v11 offset:16896\n\t"
    "v_mfma_f32_16x16x32_bf16 v[184:187], a[140:143], a[24:27], v[184:187]\n\t"
    "v_mfma_f32_16x16x32_bf16 v[188:191], a[140:143], a[28:31], v[188:191]\n\t"
    "s_waitcnt lgkmcnt(4)\n\t"
    "v_mfma_f32_16x16x32_bf16 v[64:67], a[128:131], a[32:35], v[64:67]\n\t"
    "ds_read_b64_tr_b16 a[140:141], v12 offset:16896\n\t"
    "s_add_u32 m0, s51, 49152\n\t"
    "s_nop 0\n\t"
    "global_load_lds_dwordx4 v6, s[42:43] offset:0\n\t"
    "v_mfma_f32_16x16x32_bf16 v[68:71], a[128:131], a[36:39], v[68:71]\n\t"
    "ds_read_b64_tr_b16 a[142:143], v13 offset:16896\n\t"
    "v_mfma_f32_16x16x32_bf16 v[72:75], a[128:131], a[40:43], v[72:75]\n\t"
    "v_mfma_f32_16x16x32_bf16 v[76:79], a[128:131], a[44:47], v[76:79]\n\t"
    "s_waitcnt lgkmcnt(4)\n\t"
    "v_mfma_f32_16x16x32_bf16 v[80:83], a[132:135], a[32:35], v[80:83]\n\t"
    "ds_read_b64_tr_b16 a[128:129], v10 offset:17408\n\t"
    "global_load_lds_dwordx4 v7, s[42:43] offset:1024\n\t"
    "v_mfma_f32_16x16x32_bf16 v[84:87], a[132:135], a[36:39], v[84:87]\n\t"
    "ds_read_b64_tr_b16 a[130:131], v11 offset:17408\n\t"
    "v_mfma_f32_16x16x32_bf16 v[88:91], a[132:135], a[40:43], v[88:91]\n\t"
    "v_mfma_f32_16x16x32_bf16 v[92:95], a[132:135], a[44:47], v[92:95]\n\t"
    "s_waitcnt lgkmcnt(4)\n\t"
    "v_mfma_f32_16x16x32_bf16 v[96:99], a[136:139], a[32:35], v[96:99]\n\t"
    "ds_read_b64_tr_b16 a[132:133], v12 offset:17408\n\t"
    "global_load_lds_dwordx4 v8, s[42:43] offset:2048\n\t"
    "v_mfma_f32_16x16x32_bf16 v[100:103], a[136:139], a[36:39], v[100:103]\n\t"
    "ds_read_b64_tr_b16 a[134:135], v13 offset:17408\n\t"
    "v_mfma_f32_16x16x32_bf16 v[104:107], a[136:139], a[40:43], v[104:107]\n\t"
    "v_mfma_f32_16x16x32_bf16 v[108:111], a[136:139], a[44:47], v[108:111]\n\t"
    "s_waitcnt lgkmcnt(4)\n\t"
    "v_mfma_f32_16x16x32_bf16 v[112:115], a[140:143], a[32:35], v[112:115]\n\t"
    "ds_read_b64_tr_b16 a[136:137], v10 offset:17920\n\t"
    "global_load_lds_dwordx4 v9, s[42:43] offset:3072\n\t"
    "v_mfma_f32_16x16x32_bf16 v[116:119], a[140:143], a[36:39], v[116:119]\n\t"
    "ds_read_b64_tr_b16 a[138:139], v11 offset:17920\n\t"
    "v_mfma_f32_16x16x32_bf16 v[120:123], a[140:143], a[40:43], v[120:123]\n\t"
    "v_mfma_f32_16x16x32_bf16 v[124:127], a[140:143], a[44:47], v[124:127]\n\t"
    "s_waitcnt lgkmcnt(4)\n\t"
    "v_mfma_f32_16x16x32_bf16 v[128:131], a[128:131], a[32:35], v[128:131]\n\t"
    "ds_read_b64_tr_b16 a[140:141], v12 offset:17920\n\t"
    "s_mov_b32 m0, s99\n\t"
    "s_nop 0\n\t"
    "global_load_lds_dwordx4 v192, s[40:41] offset:-2048\n\t"
    "v_mfma_f32_16x16x32_bf16 v[132:135], a[128:131], a[36:39], v[132:135]\n\t"
    "ds_read_b64_tr_b16 a[142:143], v13 offset:17920\n\t"
    "v_mfma_f32_16x16x32_bf16 v[136:139], a[128:131], a[40:43], v[136:139]\n\t"
    "v_mfma_f32_16x16x32_bf16 v[140:143], a[128:131], a[44:47], v[140:143]\n\t"
    "s_waitcnt lgkmcnt(4)\n\t"
    "v_mfma_f32_16x16x32_bf16 v[144:147], a[132:135], a[32:35], v[144:147]\n\t"
    "ds_read_b64_tr_b16 a[128:129], v10 offset:24576\n\t"
    "global_load_lds_dwordx4 v193, s[40:41] offset:-1024\n\t"
    "v_mfma_f32_16x16x32_bf16 v[148:151], a[132:135], a[36:39], v[148:151]\n\t"
    "ds_read_b64_tr_b16 a[130:131], v11 offset:24576\n\t"
    "v_mfma_f32_16x16x32_bf16 v[152:155], a[132:135], a[40:43], v[152:155]\n\t"
    "v_mfma_f32_16x16x32_bf16 v[156:159], a[132:135], a[44:47], v[156:159]\n\t"
    "s_waitcnt lgkmcnt(4)\n\t"
    "v_mfma_f32_16x16x32_bf16 v[160:163], a[136:139], a[32:35], v[160:163]\n\t"
    "ds_read_b64_tr_b16 a[132:133], v12 offset:24576\n\t"
    "global_load_lds_dwordx4 v194, s[40:41] offset:0\n\t"
    "v_mfma_f32_16x16x32_bf16 v[164:167], a[136:139], a[36:39], v[164:167]\n\t"
    "ds_read_b64_tr_b16 a[134:135], v13 offset:24576\n\t"
    "v_mfma_f32_16x16x32_bf16 v[168:171], a[136:139], a[40:43], v[168:171]\n\t"
    "v_mfma_f32_16x16x32_bf16 v[172:175], a[136:139], a[44:47], v[172:175]\n\t"
    "s_waitcnt lgkmcnt(4)\n\t"
    "v_mfma_f32_16x16x32_bf16 v[176:179], a[140:143], a[32:35], v[176:179]\n\t"
    "ds_read_b64_tr_b16 a[136:137], v10 offset:25088\n\t"
    "v_mfma_f32_16x16x32_bf16 v[180:183], a[140:143], a[36:39], v[180:183]\n\t"
    "ds_read_b64_tr_b16 a[138:139], v11 offset:25088\n\t"
    "v_mfma_f32_16x16x32_bf16 v[184:187], a[140:143], a[40:43], v[184:187]\n\t"
    "v_mfma_f32_16x16x32_bf16 v[188:191], a[140:143], a[44:47], v[188:191]\n\t"
    "s_waitcnt vmcnt(19)\n\t"
    "s_waitcnt lgkmcnt(4)\n\t"
    "v_mfma_f32_16x16x32_bf16 v[64:67], a[128:131], a[48:51], v[64:67]\n\t"
    "ds_read_b64_tr_b16 a[140:141], v12 offset:25088\n\t"
    "global_load_lds_dwordx4 v195, s[40:41] offset:1024\n\t"
    "v_mfma_f32_16x16x32_bf16 v[68:71], a[128:131], a[52:55], v[68:71]\n\t"
    "ds_read_b64_tr_b16 a[142:143], v13 offset:25088\n\t"
    "v_mfma_f32_16x16x32_bf16 v[72:75], a[128:131], a[56:59], v[72:75]\n\t"
    "v_mfma_f32_16x16x32_bf16 v[76:79], a[128:131], a[60:63], v[76:79]\n\t"
    "s_waitcnt lgkmcnt(4)\n\t"
    "v_mfma_f32_16x16x32_bf16 v[80:83], a[132:135], a[48:51], v[80:83]\n\t"
    "ds_read_b64_tr_b16 a[128:129], v10 offset:25600\n\t"
    "global_load_lds_dwordx4 v196, s[40:41] offset:2048\n\t"
    "v_mfma_f32_16x16x32_bf16 v[84:87], a[132:135], a[52:55], v[84:87]\n\t"
    "ds_read_b64_tr_b16 a[130:131], v11 offset:25600\n\t"
    "v_mfma_f32_16x16x32_bf16 v[88:91], a[132:135], a[56:59], v[88:91]\n\t"
    "v_mfma_f32_16x16x32_bf16 v[92:95], a[132:135], a[60:63], v[92:95]\n\t"
    "s_waitcnt lgkmcnt(4)\n\t"
    "v_mfma_f32_16x16x32_bf16 v[96:99], a[136:139], a[48:51], v[96:99]\n\t"
    "ds_read_b64_tr_b16 a[132:133], v12 offset:25600\n\t"
    "buffer_load_dwordx4 a[180:183], v197, s[88:91], 0 offen\n\t"
    "v_mfma_f32_16x16x32_bf16 v[100:103], a[136:139], a[52:55], v[100:103]\n\t"
    "ds_read_b64_tr_b16 a[134:135], v13 offset:25600\n\t"
    "v_mfma_f32_16x16x32_bf16 v[104:107], a[136:139], a[56:59], v[104:107]\n\t"
    "v_mfma_f32_16x16x32_bf16 v[108:111], a[136:139], a[60:63], v[108:111]\n\t"
    "s_waitcnt lgkmcnt(4)\n\t"
    "v_mfma_f32_16x16x32_bf16 v[112:115], a[140:143], a[48:51], v[112:115]\n\t"
    "ds_read_b64_tr_b16 a[136:137], v10 offset:26112\n\t"
    "buffer_load_dwordx4 a[184:187], v198, s[88:91], 0 offen\n\t"
    "v_mfma_f32_16x16x32_bf16 v[116:119], a[140:143], a[52:55], v[116:119]\n\t"
    "ds_read_b64_tr_b16 a[138:139], v11 offset:26112\n\t"
    "v_mfma_f32_16x16x32_bf16 v[120:123], a[140:143], a[56:59], v[120:123]\n\t"
    "v_mfma_f32_16x16x32_bf16 v[124:127], a[140:143], a[60:63], v[124:127]\n\t"
    "s_waitcnt lgkmcnt(4)\n\t"
    "v_mfma_f32_16x16x32_bf16 v[128:131], a[128:131], a[48:51], v[128:131]\n\t"
    "ds_read_b64_tr_b16 a[140:141], v12 offset:26112\n\t"
    "buffer_load_dwordx4 a[188:191], v199, s[88:91], 0 offen\n\t"
    "v_mfma_f32_16x16x32_bf16 v[132:135], a[128:131], a[52:55], v[132:135]\n\t"
    "ds_read_b64_tr_b16 a[142:143], v13 offset:26112\n\t"
    "v_mfma_f32_16x16x32_bf16 v[136:139], a[128:131], a[56:59], v[136:139]\n\t"
    "v_mfma_f32_16x16x32_bf16 v[140:143], a[128:131], a[60:63], v[140:143]\n\t"
    "s_waitcnt lgkmcnt(4)\n\t"
    "v_mfma_f32_16x16x32_bf16 v[144:147], a[132:135], a[48:51], v[144:147]\n\t"
    "buffer_load_dwordx4 a[192:195], v200, s[88:91], 0 offen\n\t"
    "v_mfma_f32_16x16x32_bf16 v[148:151], a[132:135], a[52:55], v[148:151]\n\t"
    "v_mfma_f32_16x16x32_bf16 v[152:155], a[132:135], a[56:59], v[152:155]\n\t"
    "v_mfma_f32_16x16x32_bf16 v[156:159], a[132:135], a[60:63], v[156:159]\n\t"
    "s_waitcnt lgkmcnt(2)\n\t"
    "v_mfma_f32_16x16x32_bf16 v[160:163], a[136:139], a[48:51], v[160:163]\n\t"
    "buffer_load_dwordx4 a[196:199], v201, s[88:91], 0 offen\n\t"
    "v_mfma_f32_16x16x32_bf16 v[164:167], a[136:139], a[52:55], v[164:167]\n\t"
    "v_mfma_f32_16x16x32_bf16 v[168:171], a[136:139], a[56:59], v[168:171]\n\t"
    "v_mfma_f32_16x16x32_bf16 v[172:175], a[136:139], a[60:63], v[172:175]\n\t"
    "s_waitcnt lgkmcnt(0)\n\t"
    "v_mfma_f32_16x16x32_bf16 v[176:179], a[140:143], a[48:51], v[176:179]\n\t"
    "buffer_load_dwordx4 a[200:203], v202, s[88:91], 0 offen\n\t"
    "buffer_load_dwordx4 a[204:207], v203, s[88:91], 0 offen\n\t"
    "v_mfma_f32_16x16x32_bf16 v[180:183], a[140:143], a[52:55], v[180:183]\n\t"
    "v_mfma_f32_16x16x32_bf16 v[184:187], a[140:143], a[56:59], v[184:187]\n\t"
    "v_mfma_f32_16x16x32_bf16 v[188:191], a[140:143], a[60:63], v[188:191]\n\t"
    "v_cvt_pk_bf16_f32 v192, v64, v65\n\t"
    "v_cvt_pk_bf16_f32 v193, v66, v67\n\t"
    "v_cvt_pk_bf16_f32 v194, v68, v69\n\t"
    "v_cvt_pk_bf16_f32 v195, v70, v71\n\t"
    "v_cvt_pk_bf16_f32 v224, v72, v73\n\t"
    "v_cvt_pk_bf16_f32 v225, v74, v75\n\t"
    "v_cvt_pk_bf16_f32 v226, v76, v77\n\t"
    "v_cvt_pk_bf16_f32 v227, v78, v79\n\t"
    "v_permlane16_swap_b32 v192, v194\n\t"
    "v_permlane16_swap_b32 v193, v195\n\t"
    "v_permlane16_swap_b32 v224, v226\n\t"
    "v_permlane16_swap_b32 v225, v227\n\t"
    "v_cvt_pk_bf16_f32 v196, v80, v81\n\t"
    "v_cvt_pk_bf16_f32 v197, v82, v83\n\t"
    "v_cvt_pk_bf16_f32 v198, v84, v85\n\t"
    "v_cvt_pk_bf16_f32 v199, v86, v87\n\t"
    "v_cvt_pk_bf16_f32 v228, v88, v89\n\t"
    "v_cvt_pk_bf16_f32 v229, v90, v91\n\t"
    "v_cvt_pk_bf16_f32 v230, v92, v93\n\t"
    "v_cvt_pk_bf16_f32 v231, v94, v95\n\t"
    "v_permlane16_swap_b32 v196, v198\n\t"
    "v_permlane16_swap_b32 v197, v199\n\t"
    "v_permlane16_swap_b32 v228, v230\n\t"
    "v_permlane16_swap_b32 v229, v231\n\t"
    "v_cvt_pk_bf16_f32 v200, v96, v97\n\t"
    "v_cvt_pk_bf16_f32 v201, v98, v99\n\t"
    "v_cvt_pk_bf16_f32 v202, v100, v101\n\t"
    "v_cvt_pk_bf16_f32 v203, v102, v103\n\t"
    "v_cvt_pk_bf16_f32 v232, v104, v105\n\t"
    "v_cvt_pk_bf16_f32 v233, v106, v107\n\t"
    "v_cvt_pk_bf16_f32 v234, v108, v109\n\t"
    "v_cvt_pk_bf16_f32 v235, v110, v111\n\t"
    "v_permlane16_swap_b32 v200, v202\n\t"
    "v_permlane16_swap_b32 v201, v203\n\t"
    "v_permlane16_swap_b32 v232, v234\n\t"
    "v_permlane16_swap_b32 v233, v235\n\t"
    "v_cvt_pk_bf16_f32 v204, v112, v113\n\t"
    "v_cvt_pk_bf16_f32 v205, v114, v115\n\t"
    "v_cvt_pk_bf16_f32 v206, v116, v117\n\t"
    "v_cvt_pk_bf16_f32 v207, v118, v119\n\t"
    "v_cvt_pk_bf16_f32 v236, v120, v121\n\t"
    "v_cvt_pk_bf16_f32 v237, v122, v123\n\t"
    "v_cvt_pk_bf16_f32 v238, v124, v125\n\t"
    "v_cvt_pk_bf16_f32 v239, v126, v127\n\t"
    "v_permlane16_swap_b32 v204, v206\n\t"
    "v_permlane16_swap_b32 v205, v207\n\t"
    "v_permlane16_swap_b32 v236, v238\n\t"
    "v_permlane16_swap_b32 v237, v239\n\t"
    "v_cvt_pk_bf16_f32 v208, v128, v129\n\t"
    "v_cvt_pk_bf16_f32 v209, v130, v131\n\t"
    "v_cvt_pk_bf16_f32 v210, v132, v133\n\t"
    "v_cvt_pk_bf16_f32 v211, v134, v135\n\t"
    "v_cvt_pk_bf16_f32 v240, v136, v137\n\t"
    "v_cvt_pk_bf16_f32 v241, v138, v139\n\t"
    "v_cvt_pk_bf16_f32 v242, v140, v141\n\t"
    "v_cvt_pk_bf16_f32 v243, v142, v143\n\t"
    "v_permlane16_swap_b32 v208, v210\n\t"
    "v_permlane16_swap_b32 v209, v211\n\t"
    "v_permlane16_swap_b32 v240, v242\n\t"
    "v_permlane16_swap_b32 v241, v243\n\t"
    "v_cvt_pk_bf16_f32 v212, v144, v145\n\t"
    "v_cvt_pk_bf16_f32 v213, v146, v147\n\t"
    "v_cvt_pk_bf16_f32 v214, v148, v149\n\t"
    "v_cvt_pk_bf16_f32 v215, v150, v151\n\t"
    "v_cvt_pk_bf16_f32 v244, v152, v153\n\t"
    "v_cvt_pk_bf16_f32 v245, v154, v155\n\t"
    "v_cvt_pk_bf16_f32 v246, v156, v157\n\t"
    "v_cvt_pk_bf16_f32 v247, v158, v159\n\t"
    "v_permlane16_swap_b32 v212, v214\n\t"
    "v_permlane16_swap_b32 v213, v215\n\t"
    "v_permlane16_swap_b32 v244, v246\n\t"
    "v_permlane16_swap_b32 v245, v247\n\t"
    "v_cvt_pk_bf16_f32 v216, v160, v161\n\t"
    "v_cvt_pk_bf16_f32 v217, v162, v163\n\t"
    "v_cvt_pk_bf16_f32 v218, v164, v165\n\t"
    "v_cvt_pk_bf16_f32 v219, v166, v167\n\t"
    "v_cvt_pk_bf16_f32 v248, v168, v169\n\t"
    "v_cvt_pk_bf16_f32 v249, v170, v171\n\t"
    "v_cvt_pk_bf16_f32 v250, v172, v173\n\t"
    "v_cvt_pk_bf16_f32 v251, v174, v175\n\t"
    "v_permlane16_swap_b32 v216, v218\n\t"
    "v_permlane16_swap_b32 v217, v219\n\t"
    "v_permlane16_swap_b32 v248, v250\n\t"
    "v_permlane16_swap_b32 v249, v251\n\t"
    "v_cvt_pk_bf16_f32 v220, v176, v177\n\t"
    "v_cvt_pk_bf16_f32 v221, v178, v179\n\t"
    "v_cvt_pk_bf16_f32 v222, v180, v181\n\t"
    "v_cvt_pk_bf16_f32 v223, v182, v183\n\t"
    "v_cvt_pk_bf16_f32 v252, v184, v185\n\t"
    "v_cvt_pk_bf16_f32 v253, v186, v187\n\t"
    "v_cvt_pk_bf16_f32 v254, v188, v189\n\t"
    "v_cvt_pk_bf16_f32 v255, v190, v191\n\t"
    "v_permlane16_swap_b32 v220, v222\n\t"
    "v_permlane16_swap_b32 v221, v223\n\t"
    "v_permlane16_swap_b32 v252, v254\n\t"
    "v_permlane16_swap_b32 v253, v255\n\t"
    "LM_restart4_%=:\n\t"
    "v_mov_b32 v24, 0\n\t"
    "v_mov_b32 v25, 0\n\t"
    "v_mov_b32 v26, 0\n\t"
    "v_mov_b32 v27, 0\n\t"
    "v_mov_b32 v20, v16\n\t"
    "v_mov_b32 v22, v16\n\t"
    "v_mov_b32 v21, v17\n\t"
    "v_mov_b32 v23, v17\n\t"
    "v_add_u32 v44, 32768, v14\n\t"
    "v_add_u32 v45, 32768, v15\n\t"
    "s_mov_b32 s53, 16384\n\t"
    "s_waitcnt vmcnt(20)\n\t"
    "s_barrier\n\t"
    "s_add_u32 s42, s42, 16384\n\t"
    "s_addc_u32 s43, s43, 0\n\t"
    "s_add_u32 s44, s44, 16384\n\t"
    "s_addc_u32 s45, s45, 0\n\t"
    "ds_read_b128 a[128:131], v44 offset:0\n\t"
    "ds_read_b128 a[132:135], v45 offset:0\n\t"
    "ds_read_b128 a[136:139], v44 offset:512\n\t"
    "ds_read_b128 a[140:143], v45 offset:512\n\t"
    "ds_read_b128 a[144:147], v44 offset:1024\n\t"
    "s_waitcnt lgkmcnt(4)\n\t"
    "v_mfma_f32_32x32x16_bf16 v[64:79], a[128:131], v[192:195], 0\n\t"
    "ds_read_b128 a[148:151], v45 offset:1024\n\t"
    "s_mov_b32 m0, s51\n\t"
    "v_mfma_f32_32x32x16_bf16 v[80:95], a[128:131], v[224:227], 0\n\t"
    "global_load_lds_dwordx4 v6, s[42:43] offset:0\n\t"
    "s_waitcnt lgkmcnt(4)\n\t"
    "v_mfma_f32_32x32x16_bf16 v[64:79], a[132:135], v[196:199], v[64:79]\n\t"
    "ds_read_b128 a[152:155], v44 offset:1536\n\t"
    "v_mfma_f32_32x32x16_bf16 v[80:95], a[132:135], v[228:231], v[80:95]\n\t"
    "global_load_lds_dwordx4 v7, s[42:43] offset:1024\n\t"
    "s_waitcnt lgkmcnt(4)\n\t"
    "v_mfma_f32_32x32x16_bf16 v[64:79], a[136:139], v[200:203], v[64:79]\n\t"
    "ds_read_b128 a[156:159], v45 offset:1536\n\t"
    "v_mfma_f32_32x32x16_bf16 v[80:95], a[136:139], v[232:235], v[80:95]\n\t"
    "global_load_lds_dwordx4 v8, s[42:43] offset:2048\n\t"
    "s_waitcnt lgkmcnt(4)\n\t"
    "v_mfma_f32_32x32x16_bf16 v[64:79], a[140:143], v[204:207], v[64:79]\n\t"
    "ds_read_b128 a[128:131], v44 offset:8192\n\t"
    "v_mfma_f32_32x32x16_bf16 v[80:95], a[140:143], v[236:239], v[80:95]\n\t"
    "global_load_lds_dwordx4 v9, s[42:43] offset:3072\n\t"
    "s_waitcnt lgkmcnt(4)\n\t"
    "v_mfma_f32_32x32x16_bf16 v[64:79], a[144:147], v[208:211], v[64:79]\n\t"
    "ds_read_b128 a[132:135], v45 offset:8192\n\t"
    "v_mfma_f32_32x32x16_bf16 v[80:95], a[144:147], v[240:243], v[80:95]\n\t"
    "s_add_u32 m0, s52, 32768\n\t"
    "s_waitcnt lgkmcnt(4)\n\t"
    "v_mfma_f32_32x32x16_bf16 v[64:79], a[148:151], v[212:215], v[64:79]\n\t"
    "ds_read_b128 a[136:139], v44 offset:8704\n\t"
    "global_load_lds_dwordx4 v6, s[44:45] offset:0\n\t"
    "v_mfma_f32_32x32x16_bf16 v[80:95], a[148:151], v[244:247], v[80:95]\n\t"
    "s_waitcnt lgkmcnt(4)\n\t"
    "v_mfma_f32_32x32x16_bf16 v[64:79], a[152:155], v[216:219], v[64:79]\n\t"
    "ds_read_b128 a[140:143], v45 offset:8704\n\t"
    "global_load_lds_dwordx4 v7, s[44:45] offset:1024\n\t"
    "v_mfma_f32_32x32x16_bf16 v[80:95], a[152:155], v[248:251], v[80:95]\n\t"
    "s_waitcnt lgkmcnt(4)\n\t"
    "v_mfma_f32_32x32x16_bf16 v[64:79], a[156:159], v[220:223], v[64:79]\n\t"
    "ds_read_b128 a[144:147], v44 offset:9216\n\t"
    "global_load_lds_dwordx4 v8, s[44:45] offset:2048\n\t"
    "v_mfma_f32_32x32x16_bf16 v[80:95], a[156:159], v[252:255], v[80:95]\n\t"
    "s_waitcnt lgkmcnt(4)\n\t"
    "v_mfma_f32_32x32x16_bf16 v[96:111], a[128:131], v[192:195], 0\n\t"
    "ds_read_b128 a[148:151], v45 offset:9216\n\t"
    "global_load_lds_dwordx4 v9, s[44:45] offset:3072\n\t"
    "v_mfma_f32_32x32x16_bf16 v[112:127], a[128:131], v[224:227], 0\n\t"
    "s_nop 3\n\t"
    "v_max3_f32 v32, v64, v65, v66\n\t"
    "v_max3_f32 v32, v32, v67, v68\n\t"
    "v_max3_f32 v32, v32, v69, v70\n\t"
    "v_max3_f32 v32, v32, v71, v72\n\t"
    "v_max3_f32 v32, v32, v73, v74\n\t"
    "v_max3_f32 v32, v32, v75, v76\n\t"
    "s_waitcnt lgkmcnt(4)\n\t"
    "v_mfma_f32_32x32x16_bf16 v[96:111], a[132:135], v[196:199], v[96:111]\n\t"
    "ds_read_b128 a[152:155], v44 offset:9728\n\t"
    "s_waitcnt vmcnt(15)\n\t"
    "ds_read_b64_tr_b16 a[160:161], v63 offset:0\n\t"
    "ds_read_b64_tr_b16 a[162:163], v63 offset:1280\n\t"
    "v_max3_f32 v32, v32, v77, v78\n\t"
    "v_max3_f32 v32, v32, v79, v28\n\t"
    "v_max3_f32 v33, v80, v81, v82\n\t"
    "v_max3_f32 v33, v33, v83, v84\n\t"
    "v_max3_f32 v33, v33, v85, v86\n\t"
    "v_max3_f32 v33, v33, v87, v88\n\t"
    "v_mfma_f32_32x32x16_bf16 v[112:127], a[132:135], v[228:231], v[112:127]\n\t"
    "ds_read_b64_tr_b16 a[164:165], v63 offset:32\n\t"
    "ds_read_b64_tr_b16 a[166:167], v63 offset:1312\n\t"
    "v_max3_f32 v33, v33, v89, v90\n\t"
    "v_max3_f32 v33, v33, v91, v92\n\t"
    "v_max3_f32 v33, v33, v93, v94\n\t"
    "v_max3_f32 v33, v33, v95, v29\n\t"
    "v_mov_b32 v34, v32\n\t"
    "v_mov_b32 v35, v33\n\t"
    "s_waitcnt lgkmcnt(8)\n\t"
    "v_mfma_f32_32x32x16_bf16 v[96:111], a[136:139], v[200:203], v[96:111]\n\t"
    "ds_read_b128 a[156:159], v45 offset:9728\n\t"
    "ds_read_b64_tr_b16 a[168:169], v63 offset:64\n\t"
    "ds_read_b64_tr_b16 a[170:171], v63 offset:1344\n\t"
    "v_permlane32_swap_b32 v32, v34\n\t"
    "v_permlane32_swap_b32 v33, v35\n\t"
    "v_max_f32 v28, v32, v34\n\t"
    "v_max_f32 v29, v33, v35\n\t"
    "v_sub_f32 v64, v64, v28\n\t"
    "v_sub_f32 v65, v65, v28\n\t"
    "v_mfma_f32_32x32x16_bf16 v[112:127], a[136:139], v[232:235], v[112:127]\n\t"
    "ds_read_b64_tr_b16 a[172:173], v63 offset:96\n\t"
    "ds_read_b64_tr_b16 a[174:175], v63 offset:1376\n\t"
    "v_sub_f32 v66, v66, v28\n\t"
    "v_sub_f32 v67, v67, v28\n\t"
    "v_sub_f32 v68, v68, v28\n\t"
    "v_sub_f32 v69, v69, v28\n\t"
    "v_sub_f32 v70, v70, v28\n\t"
    "v_sub_f32 v71, v71, v28\n\t"
    "s_waitcnt lgkmcnt(12)\n\t"
    "v_mfma_f32_32x32x16_bf16 v[96:111], a[140:143], v[204:207], v[96:111]\n\t"
    "ds_read_b64_tr_b16 a[128:129], v20 offset:0\n\t"
    "ds_read_b64_tr_b16 a[176:177], v63 offset:128\n\t"
    "ds_read_b64_tr_b16 a[178:179], v63 offset:1408\n\t"
    "v_sub_f32 v72, v72, v28\n\t"
    "v_sub_f32 v73, v73, v28\n\t"
    "v_sub_f32 v74, v74, v28\n\t"
    "v_sub_f32 v75, v75, v28\n\t"
    "v_sub_f32 v76, v76, v28\n\t"
    "v_sub_f32 v77, v77, v28\n\t"
    "v_mfma_f32_32x32x16_bf16 v[112:127], a[140:143], v[236:239], v[112:127]\n\t"
    "s_waitcnt lgkmcnt(14)\n\t"
    "ds_read_b64_tr_b16 a[130:131], v21 offset:2048\n\t"
    "v_sub_f32 v78, v78, v28\n\t"
    "v_sub_f32 v79, v79, v28\n\t"
    "v_sub_f32 v80, v80, v29\n\t"
    "v_sub_f32 v81, v81, v29\n\t"
    "v_sub_f32 v82, v82, v29\n\t"
    "v_sub_f32 v83, v83, v29\n\t"
    "v_mfma_f32_32x32x16_bf16 v[96:111], a[144:147], v[208:211], v[96:111]\n\t"
    "s_waitcnt lgkmcnt(14)\n\t"
    "ds_read_b64_tr_b16 a[132:133], v20 offset:512\n\t"
    "s_waitcnt vmcnt(14)\n\t"
    "buffer_store_dwordx4 a[180:183], v1, s[92:95], 0 offen offset:0 sc1\n\t"
    "v_sub_f32 v84, v84, v29\n\t"
    "v_sub_f32 v85, v85, v29\n\t"
    "v_sub_f32 v86, v86, v29\n\t"
    "v_sub_f32 v87, v87, v29\n\t"
    "v_sub_f32 v88, v88, v29\n\t"
    "v_sub_f32 v89, v89, v29\n\t"
    "v_mfma_f32_32x32x16_bf16 v[112:127], a[144:147], v[240:243], v[112:127]\n\t"
    "s_waitcnt lgkmcnt(14)\n\t"
    "ds_read_b64_tr_b16 a[134:135], v21 offset:2560\n\t"
    "v_sub_f32 v90, v90, v29\n\t"
    "v_sub_f32 v91, v91, v29\n\t"
    "v_sub_f32 v92, v92, v29\n\t"
    "v_sub_f32 v93, v93, v29\n\t"
    "v_sub_f32 v94, v94, v29\n\t"
    "v_sub_f32 v95, v95, v29\n\t"
    "v_mfma_f32_32x32x16_bf16 v[96:111], a[148:151], v[212:215], v[96:111]\n\t"
    "s_waitcnt lgkmcnt(14)\n\t"
    "ds_read_b64_tr_b16 a[136:137], v20 offset:1024\n\t"
    "s_waitcnt vmcnt(14)\n\t"
    "buffer_store_dwordx4 a[184:187], v1, s[92:95], 0 offen offset:1024 sc1\n\t"
    "v_sub_f32 v128, 0, v28\n\t"
    "v_sub_f32 v144, 0, v29\n\t"
    "v_mov_b32 v129, v128\n\t"
    "v_mov_b32 v145, v144\n\t"
    "v_mov_b32 v130, v128\n\t"
    "v_mov_b32 v146, v144\n\t"
    "v_mfma_f32_32x32x16_bf16 v[112:127], a[148:151], v[244:247], v[112:127]\n\t"
    "s_waitcnt lgkmcnt(14)\n\t"
    "ds_read_b64_tr_b16 a[138:139], v21 offset:3072\n\t"
    "v_mov_b32 v131, v128\n\t"
    "v_mov_b32 v147, v144\n\t"
    "v_mov_b32 v132, v128\n\t"
    "v_mov_b32 v148, v144\n\t"
    "v_mov_b32 v133, v128\n\t"
    "v_mov_b32 v149, v144\n\t"
    "v_mfma_f32_32x32x16_bf16 v[96:111], a[152:155], v[216:219], v[96:111]\n\t"
    "s_waitcnt lgkmcnt(14)\n\t"
    "ds_read_b64_tr_b16 a[140:141], v20 offset:1536\n\t"
    "s_waitcnt vmcnt(14)\n\t"
    "buffer_store_dwordx4 a[188:191], v1, s[92:95], 0 offen offset:2048 sc1\n\t"
    "v_mov_b32 v134, v128\n\t"
    "v_mov_b32 v150, v144\n\t"
    "v_mov_b32 v135, v128\n\t"
    "v_mov_b32 v151, v144\n\t"
    "v_mov_b32 v136, v128\n\t"
    "v_mov_b32 v152, v144\n\t"
    "v_mfma_f32_32x32x16_bf16 v[112:127], a[152:155], v[248:251], v[112:127]\n\t"
    "s_waitcnt lgkmcnt(14)\n\t"
    "ds_read_b64_tr_b16 a[142:143], v21 offset:3584\n\t"
    "v_mov_b32 v137, v128\n\t"
    "v_mov_b32 v153, v144\n\t"
    "v_mov_b32 v138, v128\n\t"
    "v_mov_b32 v154, v144\n\t"
    "v_mov_b32 v139, v128\n\t"
    "v_mov_b32 v155, v144\n\t"
    "s_waitcnt lgkmcnt(14)\n\t"
    "v_mfma_f32_32x32x16_bf16 v[96:111], a[156:159], v[220:223], v[96:111]\n\t"
    "ds_read_b64_tr_b16 a[144:145], v20 offset:4096\n\t"
    "s_waitcnt vmcnt(14)\n\t"
    "buffer_store_dwordx4 a[192:195], v1, s[92:95], 0 offen offset:3072 sc1\n\t"
    "v_mov_b32 v140, v128\n\t"
    "v_mov_b32 v156, v144\n\t"
    "v_mov_b32 v141, v128\n\t"
    "v_mov_b32 v157, v144\n\t"
    "v_mov_b32 v142, v128\n\t"
    "v_mov_b32 v158, v144\n\t"
    "v_add_u32 v44, 49152, v14\n\t"
    "v_add_u32 v45, 49152, v15\n\t"
    "v_mfma_f32_32x32x16_bf16 v[112:127], a[156:159], v[252:255], v[112:127]\n\t"
    "s_waitcnt lgkmcnt(14)\n\t"
    "ds_read_b64_tr_b16 a[146:147], v21 offset:6144\n\t"
    "v_mov_b32 v143, v128\n\t"
    "v_mov_b32 v159, v144\n\t"
    "v_exp_f32 v64, v64\n\t"
    "v_exp_f32 v65, v65\n\t"
    "v_add_f32 v24, v24, v64\n\t"
    "v_exp_f32 v66, v66\n\t"
    "v_add_f32 v25, v25, v65\n\t"
    "v_cvt_pk_bf16_f32 v160, v64, v65\n\t"
    "v_exp_f32 v67, v67\n\t"
    "v_add_f32 v24, v24, v66\n\t"
    "v_exp_f32 v68, v68\n\t"
    "v_add_f32 v25, v25, v67\n\t"
    "v_cvt_pk_bf16_f32 v161, v66, v67\n\t"
    "v_exp_f32 v69, v69\n\t"
    "v_add_f32 v24, v24, v68\n\t"
    "v_exp_f32 v70, v70\n\t"
    "v_add_f32 v25, v25, v69\n\t"
    "v_cvt_pk_bf16_f32 v162, v68, v69\n\t"
    "v_exp_f32 v71, v71\n\t"
    "v_add_f32 v24, v24, v70\n\t"
    "v_exp_f32 v80, v80\n\t"
    "v_add_f32 v25, v25, v71\n\t"
    "v_cvt_pk_bf16_f32 v163, v70, v71\n\t"
    "v_exp_f32 v81, v81\n\t"
    "v_add_f32 v26, v26, v80\n\t"
    "v_exp_f32 v82, v82\n\t"
    "v_add_f32 v27, v27, v81\n\t"
    "v_cvt_pk_bf16_f32 v168, v80, v81\n\t"
    "v_exp_f32 v83, v83\n\t"
    "v_add_f32 v26, v26, v82\n\t"
    "v_exp_f32 v84, v84\n\t"
    "v_add_f32 v27, v27, v83\n\t"
    "v_cvt_pk_bf16_f32 v169, v82, v83\n\t"
    "v_exp_f32 v85, v85\n\t"
    "v_add_f32 v26, v26, v84\n\t"
    "v_exp_f32 v86, v86\n\t"
    "v_add_f32 v27, v27, v85\n\t"
    "v_cvt_pk_bf16_f32 v170, v84, v85\n\t"
    "v_exp_f32 v87, v87\n\t"
    "v_add_f32 v26, v26, v86\n\t"
    "s_nop 0\n\t"
    "v_add_f32 v27, v27, v87\n\t"
    "v_cvt_pk_bf16_f32 v171, v86, v87\n\t"
    "s_waitcnt lgkmcnt(8)\n\t"
    "v_mfma_f32_32x32x16_bf16 a[0:15], a[128:131], v[160:163], 0\n\t"
    "ds_read_b64_tr_b16 a[148:149], v20 offset:4608\n\t"
    "s_waitcnt vmcnt(14)\n\t"
    "buffer_store_dwordx4 a[196:199], v1, s[92:95], s98 offen offset:0 sc1\n\t"
    "v_exp_f32 v72, v72\n\t"
    "v_exp_f32 v73, v73\n\t"
    "v_add_f32 v24, v24, v72\n\t"
    "v_mfma_f32_32x32x16_bf16 a[16:31], a[128:131], v[168:171], 0\n\t"
    "ds_read_b64_tr_b16 a[150:151], v21 offset:6656\n\t"
    "v_exp_f32 v74, v74\n\t"
    "v_add_f32 v25, v25, v73\n\t"
    "v_cvt_pk_bf16_f32 v164, v72, v73\n\t"
    "v_exp_f32 v75, v75\n\t"
    "v_add_f32 v24, v24, v74\n\t"
    "v_sub_f32 v96, v96, v28\n\t"
    "v_sub_f32 v97, v97, v28\n\t"
    "v_sub_f32 v98, v98, v28\n\t"
    "v_sub_f32 v99, v99, v28\n\t"
    "v_sub_f32 v100, v100, v28\n\t"
    "s_waitcnt lgkmcnt(8)\n\t"
    "v_mfma_f32_32x32x16_bf16 a[32:47], a[132:135], v[160:163], 0\n\t"
    "ds_read_b64_tr_b16 a[152:153], v20 offset:5120\n\t"
    "s_waitcnt vmcnt(14)\n\t"
    "buffer_store_dwordx4 a[200:203], v1, s[92:95], s98 offen offset:1024 sc1\n\t"
    "v_exp_f32 v76, v76\n\t"
    "v_add_f32 v25, v25, v75\n\t"
    "v_cvt_pk_bf16_f32 v165, v74, v75\n\t"
    "v_exp_f32 v77, v77\n\t"
    "v_add_f32 v24, v24, v76\n\t"
    "v_sub_f32 v101, v101, v28\n\t"
    "v_sub_f32 v102, v102, v28\n\t"
    "v_sub_f32 v103, v103, v28\n\t"
    "v_sub_f32 v112, v112, v29\n\t"
    "v_sub_f32 v113, v113, v29\n\t"
    "v_mfma_f32_32x32x16_bf16 a[48:63], a[132:135], v[168:171], 0\n\t"
    "ds_read_b64_tr_b16 a[154:155], v21 offset:7168\n\t"
    "v_exp_f32 v78, v78\n\t"
    "v_add_f32 v25, v25, v77\n\t"
    "v_cvt_pk_bf16_f32 v166, v76, v77\n\t"
    "v_exp_f32 v79, v79\n\t"
    "v_add_f32 v24, v24, v78\n\t"
    "v_sub_f32 v114, v114, v29\n\t"
    "v_sub_f32 v115, v115, v29\n\t"
    "v_sub_f32 v116, v116, v29\n\t"
    "v_sub_f32 v117, v117, v29\n\t"
    "v_sub_f32 v118, v118, v29\n\t"
    "s_waitcnt lgkmcnt(8)\n\t"
    "v_mfma_f32_32x32x16_bf16 a[64:79], a[136:139], v[160:163], 0\n\t"
    "ds_read_b64_tr_b16 a[156:157], v20 offset:5632\n\t"
    "s_waitcnt vmcnt(14)\n\t"
    "buffer_store_dwordx4 a[204:207], v1, s[92:95], s98 offen offset:2048 sc1\n\t"
    "v_exp_f32 v88, v88\n\t"
    "v_add_f32 v25, v25, v79\n\t"
    "v_cvt_pk_bf16_f32 v167, v78, v79\n\t"
    "v_exp_f32 v89, v89\n\t"
    "v_add_f32 v26, v26, v88\n\t"
    "v_sub_f32 v119, v119, v29\n\t"
    "v_sub_f32 v104, v104, v28\n\t"
    "v_sub_f32 v105, v105, v28\n\t"
    "v_sub_f32 v106, v106, v28\n\t"
    "v_sub_f32 v107, v107, v28\n\t"
    "v_mfma_f32_32x32x16_bf16 a[80:95], a[136:139], v[168:171], 0\n\t"
    "ds_read_b64_tr_b16 a[158:159], v21 offset:7680\n\t"
    "v_exp_f32 v90, v90\n\t"
    "v_add_f32 v27, v27, v89\n\t"
    "v_cvt_pk_bf16_f32 v172, v88, v89\n\t"
    "v_exp_f32 v91, v91\n\t"
    "v_add_f32 v26, v26, v90\n\t"
    "v_sub_f32 v108, v108, v28\n\t"
    "v_sub_f32 v109, v109, v28\n\t"
    "v_sub_f32 v110, v110, v28\n\t"
    "v_sub_f32 v111, v111, v28\n\t"
    "v_sub_f32 v120, v120, v29\n\t"
    "s_barrier\n\t"
    "s_waitcnt lgkmcnt(8)\n\t"
    "v_mfma_f32_32x32x16_bf16 a[96:111], a[140:143], v[160:163], 0\n\t"
    "ds_read_b128 a[128:131], v44 offset:0\n\t"
    "global_store_dwordx4 v1, a[160:163], s[60:61] offset:-2048 sc1\n\t"
    "v_exp_f32 v92, v92\n\t"
    "v_add_f32 v27, v27, v91\n\t"
    "v_cvt_pk_bf16_f32 v173, v90, v91\n\t"
    "v_exp_f32 v93, v93\n\t"
    "v_add_f32 v26, v26, v92\n\t"
    "v_sub_f32 v121, v121, v29\n\t"
    "v_sub_f32 v122, v122, v29\n\t"
    "v_sub_f32 v123, v123, v29\n\t"
    "v_sub_f32 v124, v124, v29\n\t"
    "v_sub_f32 v125, v125, v29\n\t"
    "v_mfma_f32_32x32x16_bf16 a[112:127], a[140:143], v[168:171], 0\n\t"
    "v_exp_f32 v94, v94\n\t"
    "v_add_f32 v27, v27, v93\n\t"
    "v_cvt_pk_bf16_f32 v174, v92, v93\n\t"
    "v_exp_f32 v95, v95\n\t"
    "v_add_f32 v26, v26, v94\n\t"
    "s_nop 0\n\t"
    "v_add_f32 v27, v27, v95\n\t"
    "v_cvt_pk_bf16_f32 v175, v94, v95\n\t"
    "v_sub_f32 v126, v126, v29\n\t"
    "v_sub_f32 v127, v127, v29\n\t"
    "s_waitcnt lgkmcnt(7)\n\t"
    "v_mfma_f32_32x32x16_bf16 a[0:15], a[144:147], v[164:167], a[0:15]\n\t"
    "ds_read_b128 a[132:135], v45 offset:0\n\t"
    "global_store_dwordx4 v1, a[164:167], s[60:61] offset:-1024 sc1\n\t"
    "v_exp_f32 v96, v96\n\t"
    "v_exp_f32 v97, v97\n\t"
    "v_add_f32 v24, v24, v96\n\t"
    "v_mfma_f32_32x32x16_bf16 a[16:31], a[144:147], v[172:175], a[16:31]\n\t"
    "v_exp_f32 v98, v98\n\t"
    "v_add_f32 v25, v25, v97\n\t"
    "v_cvt_pk_bf16_f32 v176, v96, v97\n\t"
    "v_exp_f32 v99, v99\n\t"
    "v_add_f32 v24, v24, v98\n\t"
    "s_waitcnt lgkmcnt(6)\n\t"
    "v_mfma_f32_32x32x16_bf16 a[32:47], a[148:151], v[164:167], a[32:47]\n\t"
    "ds_read_b128 a[136:139], v44 offset:512\n\t"
    "global_store_dwordx4 v1, a[168:171], s[60:61] offset:0 sc1\n\t"
    "v_exp_f32 v100, v100\n\t"
    "v_add_f32 v25, v25, v99\n\t"
    "v_cvt_pk_bf16_f32 v177, v98, v99\n\t"
    "v_exp_f32 v101, v101\n\t"
    "v_add_f32 v24, v24, v100\n\t"
    "v_mfma_f32_32x32x16_bf16 a[48:63], a[148:151], v[172:175], a[48:63]\n\t"
    "v_exp_f32 v102, v102\n\t"
    "v_add_f32 v25, v25, v101\n\t"
    "v_cvt_pk_bf16_f32 v178, v100, v101\n\t"
    "v_exp_f32 v103, v103\n\t"
    "v_add_f32 v24, v24, v102\n\t"
    "s_waitcnt lgkmcnt(5)\n\t"
    "v_mfma_f32_32x32x16_bf16 a[64:79], a[152:155], v[164:167], a[64:79]\n\t"
    "ds_read_b128 a[140:143], v45 offset:512\n\t"
    "global_store_dwordx4 v1, a[172:175], s[60:61] offset:1024 sc1\n\t"
    "v_exp_f32 v112, v112\n\t"
    "v_add_f32 v25, v25, v103\n\t"
    "v_cvt_pk_bf16_f32 v179, v102, v103\n\t"
    "v_exp_f32 v113, v113\n\t"
    "v_add_f32 v26, v26, v112\n\t"
    "v_mfma_f32_32x32x16_bf16 a[80:95], a[152:155], v[172:175], a[80:95]\n\t"
    "v_exp_f32 v114, v114\n\t"
    "v_add_f32 v27, v27, v113\n\t"
    "v_cvt_pk_bf16_f32 v184, v112, v113\n\t"
    "v_exp_f32 v115, v115\n\t"
    "v_add_f32 v26, v26, v114\n\t"
    "s_waitcnt lgkmcnt(4)\n\t"
    "v_mfma_f32_32x32x16_bf16 a[96:111], a[156:159], v[164:167], a[96:111]\n\t"
    "ds_read_b128 a[144:147], v44 offset:1024\n\t"
    "global_store_dwordx4 v1, a[176:179], s[60:61] offset:2048 sc1\n\t"
    "v_exp_f32 v116, v116\n\t"
    "v_add_f32 v27, v27, v115\n\t"
    "v_cvt_pk_bf16_f32 v185, v114, v115\n\t"
    "v_exp_f32 v117, v117\n\t"
    "v_mfma_f32_32x32x16_bf16 a[112:127], a[156:159], v[172:175], a[112:127]\n\t"
    "s_mov_b32 s49, 7\n\t"
    "LM_5_%=:\n\t"
    "s_waitcnt lgkmcnt(4)\n\t"
    "v_mfma_f32_32x32x16_bf16 v[64:79], a[128:131], v[192:195], v[128:143]\n\t"
    "ds_read_b128 a[148:151], v45 offset:1024\n\t"
    "s_cmp_gt_u32 s49, 2\n\t"
    "s_cselect_b32 s54, 16384, 0\n\t"
    "v_exp_f32 v118, v118\n\t"
    "v_add_f32 v26, v26, v116\n\t"
    "v_mfma_f32_32x32x16_bf16 v[80:95], a[128:131], v[224:227], v[144:159]\n\t"
    "s_waitcnt lgkmcnt(3)\n\t"
    "s_add_u32 s42, s42, s54\n\t"
    "s_addc_u32 s43, s43, 0\n\t"
    "v_exp_f32 v119, v119\n\t"
    "v_add_f32 v27, v27, v117\n\t"
    "v_cvt_pk_bf16_f32 v186, v116, v117\n\t"
    "v_mfma_f32_32x32x16_bf16 v[64:79], a[132:135], v[196:199], v[64:79]\n\t"
    "ds_read_b128 a[152:155], v44 offset:1536\n\t"
    "s_add_u32 s44, s44, s54\n\t"
    "s_addc_u32 s45, s45, 0\n\t"
    "v_exp_f32 v104, v104\n\t"
    "v_add_f32 v26, v26, v118\n\t"
    "v_mfma_f32_32x32x16_bf16 v[80:95], a[132:135], v[228:231], v[80:95]\n\t"
    "s_add_u32 m0, s51, s53\n\t"
    "v_exp_f32 v105, v105\n\t"
    "v_add_f32 v27, v27, v119\n\t"
    "v_cvt_pk_bf16_f32 v187, v118, v119\n\t"
    "v_mfma_f32_32x32x16_bf16 v[64:79], a[136:139], v[200:203], v[64:79]\n\t"
    "ds_read_b128 a[156:159], v45 offset:1536\n\t"
    "global_load_lds_dwordx4 v6, s[42:43] offset:0\n\t"
    "v_exp_f32 v106, v106\n\t"
    "v_add_f32 v24, v24, v104\n\t"
    "v_mfma_f32_32x32x16_bf16 v[80:95], a[136:139], v[232:235], v[80:95]\n\t"
    "s_waitcnt lgkmcnt(3)\n\t"
    "v_exp_f32 v107, v107\n\t"
    "v_add_f32 v25, v25, v105\n\t"
    "v_cvt_pk_bf16_f32 v180, v104, v105\n\t"
    "v_add_u32 v20, s53, v16\n\t"
    "v_mfma_f32_32x32x16_bf16 v[64:79], a[140:143], v[204:207], v[64:79]\n\t"
    "ds_read_b64_tr_b16 a[128:129], v22 offset:8192\n\t"
    "v_exp_f32 v108, v108\n\t"
    "v_add_f32 v24, v24, v106\n\t"
    "v_add_u32 v21, s53, v17\n\t"
    "v_mfma_f32_32x32x16_bf16 v[80:95], a[140:143], v[236:239], v[80:95]\n\t"
    "ds_read_b64_tr_b16 a[130:131], v23 offset:10240\n\t"
    "v_exp_f32 v109, v109\n\t"
    "v_add_f32 v25, v25, v107\n\t"
    "v_cvt_pk_bf16_f32 v181, v106, v107\n\t"
    "v_mfma_f32_32x32x16_bf16 v[64:79], a[144:147], v[208:211], v[64:79]\n\t"
    "ds_read_b64_tr_b16 a[132:133], v22 offset:8704\n\t"
    "global_load_lds_dwordx4 v7, s[42:43] offset:1024\n\t"
    "v_exp_f32 v110, v110\n\t"
    "v_add_f32 v24, v24, v108\n\t"
    "v_mfma_f32_32x32x16_bf16 v[80:95], a[144:147], v[240:243], v[80:95]\n\t"
    "s_waitcnt lgkmcnt(4)\n\t"
    "ds_read_b64_tr_b16 a[134:135], v23 offset:10752\n\t"
    "v_exp_f32 v111, v111\n\t"
    "v_add_f32 v25, v25, v109\n\t"
    "v_cvt_pk_bf16_f32 v182, v108, v109\n\t"
    "v_mfma_f32_32x32x16_bf16 v[64:79], a[148:151], v[212:215], v[64:79]\n\t"
    "ds_read_b64_tr_b16 a[136:137], v22 offset:9216\n\t"
    "v_exp_f32 v120, v120\n\t"
    "v_add_f32 v24, v24, v110\n\t"
    "v_mfma_f32_32x32x16_bf16 v[80:95], a[148:151], v[244:247], v[80:95]\n\t"
    "ds_read_b64_tr_b16 a[138:139], v23 offset:11264\n\t"
    "v_exp_f32 v121, v121\n\t"
    "v_add_f32 v25, v25, v111\n\t"
    "v_cvt_pk_bf16_f32 v183, v110, v111\n\t"
    "v_mfma_f32_32x32x16_bf16 v[64:79], a[152:155], v[216:219], v[64:79]\n\t"
    "ds_read_b64_tr_b16 a[140:141], v22 offset:9728\n\t"
    "global_load_lds_dwordx4 v8, s[42:43] offset:2048\n\t"
    "v_exp_f32 v122, v122\n\t"
    "v_add_f32 v26, v26, v120\n\t"
    "v_mfma_f32_32x32x16_bf16 v[80:95], a[152:155], v[248:251], v[80:95]\n\t"
    "s_waitcnt lgkmcnt(5)\n\t"
    "ds_read_b64_tr_b16 a[142:143], v23 offset:11776\n\t"
    "v_exp_f32 v123, v123\n\t"
    "v_add_f32 v27, v27, v121\n\t"
    "v_cvt_pk_bf16_f32 v188, v120, v121\n\t"
    "v_mfma_f32_32x32x16_bf16 v[64:79], a[156:159], v[220:223], v[64:79]\n\t"
    "ds_read_b64_tr_b16 a[144:145], v22 offset:12288\n\t"
    "v_exp_f32 v124, v124\n\t"
    "v_add_f32 v26, v26, v122\n\t"
    "v_mfma_f32_32x32x16_bf16 v[80:95], a[156:159], v[252:255], v[80:95]\n\t"
    "ds_read_b64_tr_b16 a[146:147], v23 offset:14336\n\t"
    "v_exp_f32 v125, v125\n\t"
    "v_add_f32 v27, v27, v123\n\t"
    "v_cvt_pk_bf16_f32 v189, v122, v123\n\t"
    "v_mfma_f32_32x32x16_bf16 a[0:15], a[128:131], v[176:179], a[0:15]\n\t"
    "ds_read_b64_tr_b16 a[148:149], v22 offset:12800\n\t"
    "global_load_lds_dwordx4 v9, s[42:43] offset:3072\n\t"
    "v_exp_f32 v126, v126\n\t"
    "v_add_f32 v26, v26, v124\n\t"
    "v_mfma_f32_32x32x16_bf16 a[16:31], a[128:131], v[184:187], a[16:31]\n\t"
    "s_waitcnt lgkmcnt(5)\n\t"
    "ds_read_b64_tr_b16 a[150:151], v23 offset:14848\n\t"
    "s_xor_b32 s55, s53, 32768\n\t"
    "v_exp_f32 v127, v127\n\t"
    "v_add_f32 v27, v27, v125\n\t"
    "v_cvt_pk_bf16_f32 v190, v124, v125\n\t"
    "v_mfma_f32_32x32x16_bf16 a[32:47], a[132:135], v[176:179], a[32:47]\n\t"
    "ds_read_b64_tr_b16 a[152:153], v22 offset:13312\n\t"
    "s_add_u32 m0, s52, s55\n\t"
    "v_exp_f32 v64, v64\n\t"
    "v_add_f32 v26, v26, v126\n\t"
    "v_mfma_f32_32x32x16_bf16 a[48:63], a[132:135], v[184:187], a[48:63]\n\t"
    "ds_read_b64_tr_b16 a[154:155], v23 offset:15360\n\t"
    "v_exp_f32 v65, v65\n\t"
    "v_add_f32 v27, v27, v127\n\t"
    "v_cvt_pk_bf16_f32 v191, v126, v127\n\t"
    "v_mfma_f32_32x32x16_bf16 a[64:79], a[136:139], v[176:179], a[64:79]\n\t"
    "ds_read_b64_tr_b16 a[156:157], v22 offset:13824\n\t"
    "global_load_lds_dwordx4 v6, s[44:45] offset:0\n\t"
    "v_exp_f32 v66, v66\n\t"
    "v_add_f32 v24, v24, v64\n\t"
    "v_mfma_f32_32x32x16_bf16 a[80:95], a[136:139], v[184:187], a[80:95]\n\t"
    "s_waitcnt lgkmcnt(5)\n\t"
    "ds_read_b64_tr_b16 a[158:159], v23 offset:15872\n\t"
    "v_exp_f32 v67, v67\n\t"
    "v_add_f32 v25, v25, v65\n\t"
    "v_cvt_pk_bf16_f32 v160, v64, v65\n\t"
    "v_mfma_f32_32x32x16_bf16 a[96:111], a[140:143], v[176:179], a[96:111]\n\t"
    "ds_read_b128 a[128:131], v44 offset:8192\n\t"
    "v_exp_f32 v68, v68\n\t"
    "v_add_f32 v24, v24, v66\n\t"
    "v_mfma_f32_32x32x16_bf16 a[112:127], a[140:143], v[184:187], a[112:127]\n\t"
    "v_exp_f32 v69, v69\n\t"
    "v_add_f32 v25, v25, v67\n\t"
    "v_cvt_pk_bf16_f32 v161, v66, v67\n\t"
    "v_mfma_f32_32x32x16_bf16 a[0:15], a[144:147], v[180:183], a[0:15]\n\t"
    "ds_read_b128 a[132:135], v45 offset:8192\n\t"
    "global_load_lds_dwordx4 v7, s[44:45] offset:1024\n\t"
    "v_exp_f32 v70, v70\n\t"
    "v_add_f32 v24, v24, v68\n\t"
    "v_mfma_f32_32x32x16_bf16 a[16:31], a[144:147], v[188:191], a[16:31]\n\t"
    "s_waitcnt lgkmcnt(4)\n\t"
    "v_exp_f32 v71, v71\n\t"
    "v_add_f32 v25, v25, v69\n\t"
    "v_cvt_pk_bf16_f32 v162, v68, v69\n\t"
    "v_mfma_f32_32x32x16_bf16 a[32:47], a[148:151], v[180:183], a[32:47]\n\t"
    "ds_read_b128 a[136:139], v44 offset:8704\n\t"
    "v_exp_f32 v80, v80\n\t"
    "v_add_f32 v24, v24, v70\n\t"
    "v_mfma_f32_32x32x16_bf16 a[48:63], a[148:151], v[188:191], a[48:63]\n\t"
    "v_exp_f32 v81, v81\n\t"
    "v_add_f32 v25, v25, v71\n\t"
    "v_cvt_pk_bf16_f32 v163, v70, v71\n\t"
    "v_mfma_f32_32x32x16_bf16 a[64:79], a[152:155], v[180:183], a[64:79]\n\t"
    "ds_read_b128 a[140:143], v45 offset:8704\n\t"
    "global_load_lds_dwordx4 v8, s[44:45] offset:2048\n\t"
    "v_exp_f32 v82, v82\n\t"
    "v_add_f32 v26, v26, v80\n\t"
    "v_mfma_f32_32x32x16_bf16 a[80:95], a[152:155], v[188:191], a[80:95]\n\t"
    "s_waitcnt lgkmcnt(3)\n\t"
    "v_exp_f32 v83, v83\n\t"
    "v_add_f32 v27, v27, v81\n\t"
    "v_cvt_pk_bf16_f32 v168, v80, v81\n\t"
    "v_mfma_f32_32x32x16_bf16 a[96:111], a[156:159], v[180:183], a[96:111]\n\t"
    "ds_read_b128 a[144:147], v44 offset:9216\n\t"
    "v_exp_f32 v84, v84\n\t"
    "v_add_f32 v26, v26, v82\n\t"
    "v_mfma_f32_32x32x16_bf16 a[112:127], a[156:159], v[188:191], a[112:127]\n\t"
    "v_exp_f32 v85, v85\n\t"
    "v_add_f32 v27, v27, v83\n\t"
    "v_cvt_pk_bf16_f32 v169, v82, v83\n\t"
    "v_mfma_f32_32x32x16_bf16 v[96:111], a[128:131], v[192:195], v[128:143]\n\t"
    "ds_read_b128 a[148:151], v45 offset:9216\n\t"
    "global_load_lds_dwordx4 v9, s[44:45] offset:3072\n\t"
    "v_exp_f32 v86, v86\n\t"
    "v_add_f32 v26, v26, v84\n\t"
    "v_mfma_f32_32x32x16_bf16 v[112:127], a[128:131], v[224:227], v[144:159]\n\t"
    "s_waitcnt lgkmcnt(3)\n\t"
    "v_exp_f32 v87, v87\n\t"
    "v_add_f32 v27, v27, v85\n\t"
    "v_cvt_pk_bf16_f32 v170, v84, v85\n\t"
    "v_mfma_f32_32x32x16_bf16 v[96:111], a[132:135], v[196:199], v[96:111]\n\t"
    "ds_read_b128 a[152:155], v44 offset:9728\n\t"
    "v_exp_f32 v72, v72\n\t"
    "v_add_f32 v26, v26, v86\n\t"
    "v_mfma_f32_32x32x16_bf16 v[112:127], a[132:135], v[228:231], v[112:127]\n\t"
    "v_exp_f32 v73, v73\n\t"
    "v_add_f32 v27, v27, v87\n\t"
    "v_cvt_pk_bf16_f32 v171, v86, v87\n\t"
    "v_mfma_f32_32x32x16_bf16 v[96:111], a[136:139], v[200:203], v[96:111]\n\t"
    "ds_read_b128 a[156:159], v45 offset:9728\n\t"
    "v_exp_f32 v74, v74\n\t"
    "v_add_f32 v24, v24, v72\n\t"
    "v_mfma_f32_32x32x16_bf16 v[112:127], a[136:139], v[232:235], v[112:127]\n\t"
    "s_waitcnt lgkmcnt(3)\n\t"
    "s_add_u32 s53, s53, 16384\n\t"
    "s_and_b32 s53, s53, 0xffff\n\t"
    "v_exp_f32 v75, v75\n\t"
    "v_add_f32 v25, v25, v73\n\t"
    "v_cvt_pk_bf16_f32 v164, v72, v73\n\t"
    "v_mfma_f32_32x32x16_bf16 v[96:111], a[140:143], v[204:207], v[96:111]\n\t"
    "ds_read_b64_tr_b16 a[128:129], v20 offset:0\n\t"
    "s_xor_b32 s55, s53, 32768\n\t"
    "v_exp_f32 v76, v76\n\t"
    "v_add_f32 v24, v24, v74\n\t"
    "v_mfma_f32_32x32x16_bf16 v[112:127], a[140:143], v[236:239], v[112:127]\n\t"
    "ds_read_b64_tr_b16 a[130:131], v21 offset:2048\n\t"
    "v_exp_f32 v77, v77\n\t"
    "v_add_f32 v25, v25, v75\n\t"
    "v_cvt_pk_bf16_f32 v165, v74, v75\n\t"
    "v_mfma_f32_32x32x16_bf16 v[96:111], a[144:147], v[208:211], v[96:111]\n\t"
    "ds_read_b64_tr_b16 a[132:133], v20 offset:512\n\t"
    "v_exp_f32 v78, v78\n\t"
    "v_add_f32 v24, v24, v76\n\t"
    "v_add_u32 v44, s55, v14\n\t"
    "v_mfma_f32_32x32x16_bf16 v[112:127], a[144:147], v[240:243], v[112:127]\n\t"
    "s_waitcnt lgkmcnt(4)\n\t"
    "ds_read_b64_tr_b16 a[134:135], v21 offset:2560\n\t"
    "v_exp_f32 v79, v79\n\t"
    "v_add_f32 v25, v25, v77\n\t"
    "v_cvt_pk_bf16_f32 v166, v76, v77\n\t"
    "v_mfma_f32_32x32x16_bf16 v[96:111], a[148:151], v[212:215], v[96:111]\n\t"
    "ds_read_b64_tr_b16 a[136:137], v20 offset:1024\n\t"
    "v_exp_f32 v88, v88\n\t"
    "v_add_f32 v24, v24, v78\n\t"
    "v_add_u32 v45, s55, v15\n\t"
    "v_mfma_f32_32x32x16_bf16 v[112:127], a[148:151], v[244:247], v[112:127]\n\t"
    "ds_read_b64_tr_b16 a[138:139], v21 offset:3072\n\t"
    "v_exp_f32 v89, v89\n\t"
    "v_add_f32 v25, v25, v79\n\t"
    "v_cvt_pk_bf16_f32 v167, v78, v79\n\t"
    "v_mfma_f32_32x32x16_bf16 v[96:111], a[152:155], v[216:219], v[96:111]\n\t"
    "ds_read_b64_tr_b16 a[140:141], v20 offset:1536\n\t"
    "v_exp_f32 v90, v90\n\t"
    "v_add_f32 v26, v26, v88\n\t"
    "v_mfma_f32_32x32x16_bf16 v[112:127], a[152:155], v[248:251], v[112:127]\n\t"
    "s_waitcnt lgkmcnt(5)\n\t"
    "ds_read_b64_tr_b16 a[142:143], v21 offset:3584\n\t"
    "v_exp_f32 v91, v91\n\t"
    "v_add_f32 v27, v27, v89\n\t"
    "v_cvt_pk_bf16_f32 v172, v88, v89\n\t"
    "v_mfma_f32_32x32x16_bf16 v[96:111], a[156:159], v[220:223], v[96:111]\n\t"
    "ds_read_b64_tr_b16 a[144:145], v20 offset:4096\n\t"
    "v_exp_f32 v92, v92\n\t"
    "v_add_f32 v26, v26, v90\n\t"
    "v_mfma_f32_32x32x16_bf16 v[112:127], a[156:159], v[252:255], v[112:127]\n\t"
    "ds_read_b64_tr_b16 a[146:147], v21 offset:6144\n\t"
    "v_exp_f32 v93, v93\n\t"
    "v_add_f32 v27, v27, v91\n\t"
    "v_cvt_pk_bf16_f32 v173, v90, v91\n\t"
    "v_mfma_f32_32x32x16_bf16 a[0:15], a[128:131], v[160:163], a[0:15]\n\t"
    "ds_read_b64_tr_b16 a[148:149], v20 offset:4608\n\t"
    "v_exp_f32 v94, v94\n\t"
    "v_add_f32 v26, v26, v92\n\t"
    "v_mfma_f32_32x32x16_bf16 a[16:31], a[128:131], v[168:171], a[16:31]\n\t"
    "s_waitcnt lgkmcnt(5)\n\t"
    "ds_read_b64_tr_b16 a[150:151], v21 offset:6656\n\t"
    "v_exp_f32 v95, v95\n\t"
    "v_add_f32 v27, v27, v93\n\t"
    "v_cvt_pk_bf16_f32 v174, v92, v93\n\t"
    "v_mfma_f32_32x32x16_bf16 a[32:47], a[132:135], v[160:163], a[32:47]\n\t"
    "ds_read_b64_tr_b16 a[152:153], v20 offset:5120\n\t"
    "v_exp_f32 v96, v96\n\t"
    "v_add_f32 v26, v26, v94\n\t"
    "v_mfma_f32_32x32x16_bf16 a[48:63], a[132:135], v[168:171], a[48:63]\n\t"
    "ds_read_b64_tr_b16 a[154:155], v21 offset:7168\n\t"
    "v_exp_f32 v97, v97\n\t"
    "v_add_f32 v27, v27, v95\n\t"
    "v_cvt_pk_bf16_f32 v175, v94, v95\n\t"
    "v_mfma_f32_32x32x16_bf16 a[64:79], a[136:139], v[160:163], a[64:79]\n\t"
    "ds_read_b64_tr_b16 a[156:157], v20 offset:5632\n\t"
    "v_exp_f32 v98, v98\n\t"
    "v_add_f32 v24, v24, v96\n\t"
    "v_mfma_f32_32x32x16_bf16 a[80:95], a[136:139], v[168:171], a[80:95]\n\t"
    "s_waitcnt lgkmcnt(5)\n\t"
    "ds_read_b64_tr_b16 a[158:159], v21 offset:7680\n\t"
    "v_exp_f32 v99, v99\n\t"
    "v_add_f32 v25, v25, v97\n\t"
    "v_cvt_pk_bf16_f32 v176, v96, v97\n\t"
    "s_waitcnt vmcnt(8)\n\t"
    "s_cmp_lg_u32 s49, 6\n\t"
    "s_cbranch_scc1 LM_drain6_%=\n\t"
    "s_waitcnt vmcnt(0)\n\t"
    "LM_drain6_%=:\n\t"
    "s_barrier\n\t"
    "v_mfma_f32_32x32x16_bf16 a[96:111], a[140:143], v[160:163], a[96:111]\n\t"
    "ds_read_b128 a[128:131], v44 offset:0\n\t"
    "v_exp_f32 v100, v100\n\t"
    "v_add_f32 v24, v24, v98\n\t"
    "v_mfma_f32_32x32x16_bf16 a[112:127], a[140:143], v[168:171], a[112:127]\n\t"
    "v_exp_f32 v101, v101\n\t"
    "v_add_f32 v25, v25, v99\n\t"
    "v_cvt_pk_bf16_f32 v177, v98, v99\n\t"
    "s_cmp_lg_u32 s49, 6\n\t"
    "s_cbranch_scc1 LM_pubonce7_%=\n\t"
    "s_cmp_lg_u32 s68, 0\n\t"
    "s_cbranch_scc1 LM_pub8_%=\n\t"
    "v_mov_b32 v30, s64\n\t"
    "v_mov_b32 v31, s65\n\t"
    "v_mov_b32 v32, s70\n\t"
    "s_mov_b64 exec, 1\n\t"
    "global_store_dwordx2 v32, v[30:31], s[66:67] sc1\n\t"
    "s_mov_b64 exec, -1\n\t"
    "LM_pub8_%=:\n\t"
    "LM_pubonce7_%=:\n\t"
    "v_mfma_f32_32x32x16_bf16 a[0:15], a[144:147], v[164:167], a[0:15]\n\t"
    "ds_read_b128 a[132:135], v45 offset:0\n\t"
    "v_exp_f32 v102, v102\n\t"
    "v_add_f32 v24, v24, v100\n\t"
    "v_mov_b32 v22, v20\n\t"
    "v_mfma_f32_32x32x16_bf16 a[16:31], a[144:147], v[172:175], a[16:31]\n\t"
    "s_waitcnt lgkmcnt(4)\n\t"
    "v_exp_f32 v103, v103\n\t"
    "v_add_f32 v25, v25, v101\n\t"
    "v_cvt_pk_bf16_f32 v178, v100, v101\n\t"
    "v_mfma_f32_32x32x16_bf16 a[32:47], a[148:151], v[164:167], a[32:47]\n\t"
    "ds_read_b128 a[136:139], v44 offset:512\n\t"
    "v_exp_f32 v112, v112\n\t"
    "v_add_f32 v24, v24, v102\n\t"
    "v_mov_b32 v23, v21\n\t"
    "v_mfma_f32_32x32x16_bf16 a[48:63], a[148:151], v[172:175], a[48:63]\n\t"
    "v_exp_f32 v113, v113\n\t"
    "v_add_f32 v25, v25, v103\n\t"
    "v_cvt_pk_bf16_f32 v179, v102, v103\n\t"
    "v_mfma_f32_32x32x16_bf16 a[64:79], a[152:155], v[164:167], a[64:79]\n\t"
    "ds_read_b128 a[140:143], v45 offset:512\n\t"
    "v_exp_f32 v114, v114\n\t"
    "v_add_f32 v26, v26, v112\n\t"
    "v_mfma_f32_32x32x16_bf16 a[80:95], a[152:155], v[172:175], a[80:95]\n\t"
    "s_waitcnt lgkmcnt(3)\n\t"
    "v_exp_f32 v115, v115\n\t"
    "v_add_f32 v27, v27, v113\n\t"
    "v_cvt_pk_bf16_f32 v184, v112, v113\n\t"
    "v_mfma_f32_32x32x16_bf16 a[96:111], a[156:159], v[164:167], a[96:111]\n\t"
    "ds_read_b128 a[144:147], v44 offset:1024\n\t"
    "v_exp_f32 v116, v116\n\t"
    "v_add_f32 v26, v26, v114\n\t"
    "v_mfma_f32_32x32x16_bf16 a[112:127], a[156:159], v[172:175], a[112:127]\n\t"
    "v_exp_f32 v117, v117\n\t"
    "v_add_f32 v27, v27, v115\n\t"
    "v_cvt_pk_bf16_f32 v185, v114, v115\n\t"
    "s_sub_u32 s49, s49, 1\n\t"
    "s_cmp_lg_u32 s49, 0\n\t"
    "s_cbranch_scc1 LM_5_%=\n\t"
    "global_load_dwordx4 v[40:43], v62, s[66:67] sc1\n\t"
    "global_load_dwordx4 v[44:47], v62, s[66:67] offset:16 sc1\n\t"
    "ds_read_b64_tr_b16 a[128:129], v22 offset:8192\n\t"
    "ds_read_b64_tr_b16 a[130:131], v23 offset:10240\n\t"
    "ds_read_b64_tr_b16 a[132:133], v22 offset:8704\n\t"
    "ds_read_b64_tr_b16 a[134:135], v23 offset:10752\n\t"
    "ds_read_b64_tr_b16 a[136:137], v22 offset:9216\n\t"
    "ds_read_b64_tr_b16 a[138:139], v23 offset:11264\n\t"
    "ds_read_b64_tr_b16 a[140:141], v22 offset:9728\n\t"
    "ds_read_b64_tr_b16 a[142:143], v23 offset:11776\n\t"
    "ds_read_b64_tr_b16 a[144:145], v22 offset:12288\n\t"
    "ds_read_b64_tr_b16 a[146:147], v23 offset:14336\n\t"
    "ds_read_b64_tr_b16 a[148:149], v22 offset:12800\n\t"
    "s_waitcnt lgkmcnt(14)\n\t"
    "ds_read_b64_tr_b16 a[150:151], v23 offset:14848\n\t"
    "s_waitcnt lgkmcnt(14)\n\t"
    "ds_read_b64_tr_b16 a[152:153], v22 offset:13312\n\t"
    "s_waitcnt lgkmcnt(14)\n\t"
    "ds_read_b64_tr_b16 a[154:155], v23 offset:15360\n\t"
    "s_waitcnt lgkmcnt(14)\n\t"
    "ds_read_b64_tr_b16 a[156:157], v22 offset:13824\n\t"
    "s_waitcnt lgkmcnt(14)\n\t"
    "ds_read_b64_tr_b16 a[158:159], v23 offset:15872\n\t"
    "v_add_f32 v26, v26, v116\n\t"
    "v_add_f32 v27, v27, v117\n\t"
    "v_cvt_pk_bf16_f32 v186, v116, v117\n\t"
    "v_exp_f32 v118, v118\n\t"
    "v_exp_f32 v119, v119\n\t"
    "s_nop 0\n\t"
    "v_add_f32 v26, v26, v118\n\t"
    "v_add_f32 v27, v27, v119\n\t"
    "v_cvt_pk_bf16_f32 v187, v118, v119\n\t"
    "s_waitcnt lgkmcnt(14)\n\t"
    "v_mfma_f32_32x32x16_bf16 a[0:15], a[128:131], v[176:179], a[0:15]\n\t"
    "v_exp_f32 v104, v104\n\t"
    "v_exp_f32 v105, v105\n\t"
    "v_add_f32 v24, v24, v104\n\t"
    "v_mfma_f32_32x32x16_bf16 a[16:31], a[128:131], v[184:187], a[16:31]\n\t"
    "v_exp_f32 v106, v106\n\t"
    "v_add_f32 v25, v25, v105\n\t"
    "v_cvt_pk_bf16_f32 v180, v104, v105\n\t"
    "v_exp_f32 v107, v107\n\t"
    "v_add_f32 v24, v24, v106\n\t"
    "s_waitcnt lgkmcnt(12)\n\t"
    "v_mfma_f32_32x32x16_bf16 a[32:47], a[132:135], v[176:179], a[32:47]\n\t"
    "v_exp_f32 v108, v108\n\t"
    "v_add_f32 v25, v25, v107\n\t"
    "v_cvt_pk_bf16_f32 v181, v106, v107\n\t"
    "v_exp_f32 v109, v109\n\t"
    "v_add_f32 v24, v24, v108\n\t"
    "v_mfma_f32_32x32x16_bf16 a[48:63], a[132:135], v[184:187], a[48:63]\n\t"
    "v_exp_f32 v110, v110\n\t"
    "v_add_f32 v25, v25, v109\n\t"
    "v_cvt_pk_bf16_f32 v182, v108, v109\n\t"
    "v_exp_f32 v111, v111\n\t"
    "v_add_f32 v24, v24, v110\n\t"
    "s_waitcnt lgkmcnt(10)\n\t"
    "v_mfma_f32_32x32x16_bf16 a[64:79], a[136:139], v[176:179], a[64:79]\n\t"
    "v_exp_f32 v120, v120\n\t"
    "v_add_f32 v25, v25, v111\n\t"
    "v_cvt_pk_bf16_f32 v183, v110, v111\n\t"
    "v_exp_f32 v121, v121\n\t"
    "v_add_f32 v26, v26, v120\n\t"
    "v_mfma_f32_32x32x16_bf16 a[80:95], a[136:139], v[184:187], a[80:95]\n\t"
    "v_exp_f32 v122, v122\n\t"
    "v_add_f32 v27, v27, v121\n\t"
    "v_cvt_pk_bf16_f32 v188, v120, v121\n\t"
    "v_exp_f32 v123, v123\n\t"
    "v_add_f32 v26, v26, v122\n\t"
    "s_waitcnt lgkmcnt(8)\n\t"
    "v_mfma_f32_32x32x16_bf16 a[96:111], a[140:143], v[176:179], a[96:111]\n\t"
    "v_exp_f32 v124, v124\n\t"
    "v_add_f32 v27, v27, v123\n\t"
    "v_cvt_pk_bf16_f32 v189, v122, v123\n\t"
    "v_exp_f32 v125, v125\n\t"
    "v_add_f32 v26, v26, v124\n\t"
    "v_mfma_f32_32x32x16_bf16 a[112:127], a[140:143], v[184:187], a[112:127]\n\t"
    "v_exp_f32 v126, v126\n\t"
    "v_add_f32 v27, v27, v125\n\t"
    "v_cvt_pk_bf16_f32 v190, v124, v125\n\t"
    "v_exp_f32 v127, v127\n\t"
    "v_add_f32 v26, v26, v126\n\t"
    "s_nop 0\n\t"
    "v_add_f32 v27, v27, v127\n\t"
    "v_cvt_pk_bf16_f32 v191, v126, v127\n\t"
    "s_waitcnt lgkmcnt(6)\n\t"
    "v_mfma_f32_32x32x16_bf16 a[0:15], a[144:147], v[180:183], a[0:15]\n\t"
    "v_mfma_f32_32x32x16_bf16 a[16:31], a[144:147], v[188:191], a[16:31]\n\t"
    "s_waitcnt lgkmcnt(4)\n\t"
    "v_mfma_f32_32x32x16_bf16 a[32:47], a[148:151], v[180:183], a[32:47]\n\t"
    "v_mfma_f32_32x32x16_bf16 a[48:63], a[148:151], v[188:191], a[48:63]\n\t"
    "s_waitcnt lgkmcnt(2)\n\t"
    "v_mfma_f32_32x32x16_bf16 a[64:79], a[152:155], v[180:183], a[64:79]\n\t"
    "v_mfma_f32_32x32x16_bf16 a[80:95], a[152:155], v[188:191], a[80:95]\n\t"
    "s_waitcnt lgkmcnt(0)\n\t"
    "v_mfma_f32_32x32x16_bf16 a[96:111], a[156:159], v[180:183], a[96:111]\n\t"
    "v_mfma_f32_32x32x16_bf16 a[112:127], a[156:159], v[188:191], a[112:127]\n\t"
    "v_add_f32 v30, v24, v25\n\t"
    "v_mov_b32 v31, v30\n\t"
    "s_nop 1\n\t"
    "v_permlane32_swap_b32 v30, v31\n\t"
    "v_add_f32 v24, v30, v31\n\t"
    "v_add_f32 v30, v26, v27\n\t"
    "v_mov_b32 v31, v30\n\t"
    "s_nop 1\n\t"
    "v_permlane32_swap_b32 v30, v31\n\t"
    "v_add_f32 v26, v30, v31\n\t"
    "v_mov_b32 v30, 0x67800000\n\t"
    "v_cmp_nle_f32 vcc, v24, v30\n\t"
    "s_mov_b64 s[58:59], vcc\n\t"
    "v_cmp_nle_f32 vcc, v26, v30\n\t"
    "s_or_b64 s[58:59], s[58:59], vcc\n\t"
    "s_cmp_lg_u64 s[58:59], 0\n\t"
    "s_cselect_b32 s55, 1, 0\n\t"
    "v_mov_b32 v30, s55\n\t"
    "v_lshrrev_b32 v31, 10, s48\n\t"
    "v_add_u32 v31, 163824, v31\n\t"
    "ds_write_b32 v31, v30\n\t"
    "s_waitcnt lgkmcnt(0)\n\t"
    "s_waitcnt vmcnt(2)\n\t"
    "s_barrier\n\t"
    "v_mov_b32 v31, 163824\n\t"
    "ds_read_b128 v[32:35], v31\n\t"
    "s_waitcnt lgkmcnt(0)\n\t"
    "v_or_b32 v32, v32, v33\n\t"
    "v_or3_b32 v32, v32, v34, v35\n\t"
    "s_nop 0\n\t"
    "v_readfirstlane_b32 s55, v32\n\t"
    "s_andn2_b32 s55, s55, s56\n\t"
    "s_cmp_eq_u32 s55, 0\n\t"
    "s_cbranch_scc1 LM_ok9_%=\n\t"
    "s_sub_u32 s42, s42, 114688\n\t"
    "s_subb_u32 s43, s43, 0\n\t"
    "s_sub_u32 s44, s44, 114688\n\t"
    "s_subb_u32 s45, s45, 0\n\t"
    "s_waitcnt vmcnt(0)\n\t"
    "v_add_u32 v44, 32768, v14\n\t"
    "v_add_u32 v45, 32768, v15\n\t"
    "s_mov_b32 s49, 8\n\t"
    "LM_10_%=:\n\t"
    "s_barrier\n\t"
    "s_add_u32 m0, s51, 32768\n\t"
    "s_nop 0\n\t"
    "global_load_lds_dwordx4 v6, s[42:43] offset:0\n\t"
    "global_load_lds_dwordx4 v7, s[42:43] offset:1024\n\t"
    "global_load_lds_dwordx4 v8, s[42:43] offset:2048\n\t"
    "global_load_lds_dwordx4 v9, s[42:43] offset:3072\n\t"
    "s_add_u32 s42, s42, 16384\n\t"
    "s_addc_u32 s43, s43, 0\n\t"
    "s_waitcnt vmcnt(0)\n\t"
    "s_barrier\n\t"
    "ds_read_b128 a[128:131], v44 offset:0\n\t"
    "ds_read_b128 a[132:135], v45 offset:0\n\t"
    "ds_read_b128 a[136:139], v44 offset:512\n\t"
    "ds_read_b128 a[140:143], v45 offset:512\n\t"
    "ds_read_b128 a[144:147], v44 offset:1024\n\t"
    "ds_read_b128 a[148:151], v45 offset:1024\n\t"
    "ds_read_b128 a[152:155], v44 offset:1536\n\t"
    "ds_read_b128 a[156:159], v45 offset:1536\n\t"
    "s_waitcnt lgkmcnt(7)\n\t"
    "v_mfma_f32_32x32x16_bf16 v[64:79], a[128:131], v[192:195], 0\n\t"
    "v_mfma_f32_32x32x16_bf16 v[80:95], a[128:131], v[224:227], 0\n\t"
    "s_waitcnt lgkmcnt(6)\n\t"
    "v_mfma_f32_32x32x16_bf16 v[64:79], a[132:135], v[196:199], v[64:79]\n\t"
    "v_mfma_f32_32x32x16_bf16 v[80:95], a[132:135], v[228:231], v[80:95]\n\t"
    "s_waitcnt lgkmcnt(5)\n\t"
    "v_mfma_f32_32x32x16_bf16 v[64:79], a[136:139], v[200:203], v[64:79]\n\t"
    "v_mfma_f32_32x32x16_bf16 v[80:95], a[136:139], v[232:235], v[80:95]\n\t"
    "s_waitcnt lgkmcnt(4)\n\t"
    "v_mfma_f32_32x32x16_bf16 v[64:79], a[140:143], v[204:207], v[64:79]\n\t"
    "v_mfma_f32_32x32x16_bf16 v[80:95], a[140:143], v[236:239], v[80:95]\n\t"
    "s_waitcnt lgkmcnt(3)\n\t"
    "v_mfma_f32_32x32x16_bf16 v[64:79], a[144:147], v[208:211], v[64:79]\n\t"
    "v_mfma_f32_32x32x16_bf16 v[80:95], a[144:147], v[240:243], v[80:95]\n\t"
    "s_waitcnt lgkmcnt(2)\n\t"
    "v_mfma_f32_32x32x16_bf16 v[64:79], a[148:151], v[212:215], v[64:79]\n\t"
    "v_mfma_f32_32x32x16_bf16 v[80:95], a[148:151], v[244:247], v[80:95]\n\t"
    "s_waitcnt lgkmcnt(1)\n\t"
    "v_mfma_f32_32x32x16_bf16 v[64:79], a[152:155], v[216:219], v[64:79]\n\t"
    "v_mfma_f32_32x32x16_bf16 v[80:95], a[152:155], v[248:251], v[80:95]\n\t"
    "s_waitcnt lgkmcnt(0)\n\t"
    "v_mfma_f32_32x32x16_bf16 v[64:79], a[156:159], v[220:223], v[64:79]\n\t"
    "v_mfma_f32_32x32x16_bf16 v[80:95], a[156:159], v[252:255], v[80:95]\n\t"
    "ds_read_b128 a[128:131], v44 offset:8192\n\t"
    "ds_read_b128 a[132:135], v45 offset:8192\n\t"
    "ds_read_b128 a[136:139], v44 offset:8704\n\t"
    "ds_read_b128 a[140:143], v45 offset:8704\n\t"
    "ds_read_b128 a[144:147], v44 offset:9216\n\t"
    "ds_read_b128 a[148:151], v45 offset:9216\n\t"
    "ds_read_b128 a[152:155], v44 offset:9728\n\t"
    "ds_read_b128 a[156:159], v45 offset:9728\n\t"
    "s_waitcnt lgkmcnt(7)\n\t"
    "v_mfma_f32_32x32x16_bf16 v[96:111], a[128:131], v[192:195], 0\n\t"
    "v_mfma_f32_32x32x16_bf16 v[112:127], a[128:131], v[224:227], 0\n\t"
    "s_waitcnt lgkmcnt(6)\n\t"
    "v_mfma_f32_32x32x16_bf16 v[96:111], a[132:135], v[196:199], v[96:111]\n\t"
    "v_mfma_f32_32x32x16_bf16 v[112:127], a[132:135], v[228:231], v[112:127]\n\t"
    "s_waitcnt lgkmcnt(5)\n\t"
    "v_mfma_f32_32x32x16_bf16 v[96:111], a[136:139], v[200:203], v[96:111]\n\t"
    "v_mfma_f32_32x32x16_bf16 v[112:127], a[136:139], v[232:235], v[112:127]\n\t"
    "s_waitcnt lgkmcnt(4)\n\t"
    "v_mfma_f32_32x32x16_bf16 v[96:111], a[140:143], v[204:207], v[96:111]\n\t"
    "v_mfma_f32_32x32x16_bf16 v[112:127], a[140:143], v[236:239], v[112:127]\n\t"
    "s_waitcnt lgkmcnt(3)\n\t"
    "v_mfma_f32_32x32x16_bf16 v[96:111], a[144:147], v[208:211], v[96:111]\n\t"
    "v_mfma_f32_32x32x16_bf16 v[112:127], a[144:147], v[240:243], v[112:127]\n\t"
    "s_waitcnt lgkmcnt(2)\n\t"
    "v_mfma_f32_32x32x16_bf16 v[96:111], a[148:151], v[212:215], v[96:111]\n\t"
    "v_mfma_f32_32x32x16_bf16 v[112:127], a[148:151], v[244:247], v[112:127]\n\t"
    "s_waitcnt lgkmcnt(1)\n\t"
    "v_mfma_f32_32x32x16_bf16 v[96:111], a[152:155], v[216:219], v[96:111]\n\t"
    "v_mfma_f32_32x32x16_bf16 v[112:127], a[152:155], v[248:251], v[112:127]\n\t"
    "s_waitcnt lgkmcnt(0)\n\t"
    "v_mfma_f32_32x32x16_bf16 v[96:111], a[156:159], v[220:223], v[96:111]\n\t"
    "v_mfma_f32_32x32x16_bf16 v[112:127], a[156:159], v[252:255], v[112:127]\n\t"
    "v_max3_f32 v28, v28, v64, v65\n\t"
    "v_max3_f32 v28, v28, v66, v67\n\t"
    "v_max3_f32 v28, v28, v68, v69\n\t"
    "v_max3_f32 v28, v28, v70, v71\n\t"
    "v_max3_f32 v28, v28, v72, v73\n\t"
    "v_max3_f32 v28, v28, v74, v75\n\t"
    "v_max3_f32 v28, v28, v76, v77\n\t"
    "v_max3_f32 v28, v28, v78, v79\n\t"
    "v_max3_f32 v29, v29, v80, v81\n\t"
    "v_max3_f32 v29, v29, v82, v83\n\t"
    "v_max3_f32 v29, v29, v84, v85\n\t"
    "v_max3_f32 v29, v29, v86, v87\n\t"
    "v_max3_f32 v29, v29, v88, v89\n\t"
    "v_max3_f32 v29, v29, v90, v91\n\t"
    "v_max3_f32 v29, v29, v92, v93\n\t"
    "v_max3_f32 v29, v29, v94, v95\n\t"
    "v_max3_f32 v28, v28, v96, v97\n\t"
    "v_max3_f32 v28, v28, v98, v99\n\t"
    "v_max3_f32 v28, v28, v100, v101\n\t"
    "v_max3_f32 v28, v28, v102, v103\n\t"
    "v_max3_f32 v28, v28, v104, v105\n\t"
    "v_max3_f32 v28, v28, v106, v107\n\t"
    "v_max3_f32 v28, v28, v108, v109\n\t"
    "v_max3_f32 v28, v28, v110, v111\n\t"
    "v_max3_f32 v29, v29, v112, v113\n\t"
    "v_max3_f32 v29, v29, v114, v115\n\t"
    "v_max3_f32 v29, v29, v116, v117\n\t"
    "v_max3_f32 v29, v29, v118, v119\n\t"
    "v_max3_f32 v29, v29, v120, v121\n\t"
    "v_max3_f32 v29, v29, v122, v123\n\t"
    "v_max3_f32 v29, v29, v124, v125\n\t"
    "v_max3_f32 v29, v29, v126, v127\n\t"
    "s_sub_u32 s49, s49, 1\n\t"
    "s_cmp_lg_u32 s49, 0\n\t"
    "s_cbranch_scc1 LM_10_%=\n\t"
    "s_sub_u32 s42, s42, 131072\n\t"
    "s_subb_u32 s43, s43, 0\n\t"
    "s_barrier\n\t"
    "s_add_u32 m0, s51, 32768\n\t"
    "s_nop 0\n\t"
    "global_load_lds_dwordx4 v6, s[42:43] offset:0\n\t"
    "global_load_lds_dwordx4 v7, s[42:43] offset:1024\n\t"
    "global_load_lds_dwordx4 v8, s[42:43] offset:2048\n\t"
    "global_load_lds_dwordx4 v9, s[42:43] offset:3072\n\t"
    "s_add_u32 s42, s42, 16384\n\t"
    "s_addc_u32 s43, s43, 0\n\t"
    "s_add_u32 m0, s51, 49152\n\t"
    "s_nop 0\n\t"
    "global_load_lds_dwordx4 v6, s[42:43] offset:0\n\t"
    "global_load_lds_dwordx4 v7, s[42:43] offset:1024\n\t"
    "global_load_lds_dwordx4 v8, s[42:43] offset:2048\n\t"
    "global_load_lds_dwordx4 v9, s[42:43] offset:3072\n\t"
    "s_mov_b32 m0, s52\n\t"
    "s_nop 0\n\t"
    "global_load_lds_dwordx4 v6, s[44:45] offset:0\n\t"
    "global_load_lds_dwordx4 v7, s[44:45] offset:1024\n\t"
    "global_load_lds_dwordx4 v8, s[44:45] offset:2048\n\t"
    "global_load_lds_dwordx4 v9, s[44:45] offset:3072\n\t"
    "s_add_u32 s44, s44, 16384\n\t"
    "s_addc_u32 s45, s45, 0\n\t"
    "s_add_u32 m0, s52, 16384\n\t"
    "s_nop 0\n\t"
    "global_load_lds_dwordx4 v6, s[44:45] offset:0\n\t"
    "global_load_lds_dwordx4 v7, s[44:45] offset:1024\n\t"
    "global_load_lds_dwordx4 v8, s[44:45] offset:2048\n\t"
    "global_load_lds_dwordx4 v9, s[44:45] offset:3072\n\t"
    "s_waitcnt vmcnt(0) lgkmcnt(0)\n\t"
    "s_mov_b32 s56, 1\n\t"
    "s_branch LM_restart4_%=\n\t"
    "LM_ok9_%=:\n\t"
    "s_mov_b32 s69, 100000\n\t"
    "s_waitcnt vmcnt(0)\n\t"
    "LM_poll11_%=:\n\t"
    "v_cmp_eq_u64 vcc, s[64:65], v[40:41]\n\t"
    "s_mov_b64 s[72:73], vcc\n\t"
    "v_cmp_eq_u64 vcc, s[64:65], v[42:43]\n\t"
    "s_and_b64 s[72:73], s[72:73], vcc\n\t"
    "v_cmp_eq_u64 vcc, s[64:65], v[44:45]\n\t"
    "s_and_b64 s[72:73], s[72:73], vcc\n\t"
    "v_cmp_eq_u64 vcc, s[64:65], v[46:47]\n\t"
    "s_and_b64 s[72:73], s[72:73], vcc\n\t"
    "s_cmp_eq_u64 s[72:73], -1\n\t"
    "s_cbranch_scc1 LM_polld12_%=\n\t"
    "s_sleep 1\n\t"
    "global_load_dwordx4 v[40:43], v62, s[66:67] sc1\n\t"
    "global_load_dwordx4 v[44:47], v62, s[66:67] offset:16 sc1\n\t"
    "s_waitcnt vmcnt(0)\n\t"
    "s_sub_u32 s69, s69, 1\n\t"
    "s_cmp_lg_u32 s69, 0\n\t"
    "s_cbranch_scc1 LM_poll11_%=\n\t"
    "LM_polld12_%=:\n\t"
    "s_cmp_lg_u32 s71, 0\n\t"
    "s_cbranch_scc1 LM_gen13_%=\n\t"
    "s_cmp_lg_u32 s68, 0\n\t"
    "s_cbranch_scc1 LM_pub14_%=\n\t"
    "v_mov_b32 v40, s64\n\t"
    "v_mov_b32 v41, s65\n\t"
    "v_mov_b32 v42, 0\n\t"
    "s_mov_b64 exec, 1\n\t"
    "global_store_dwordx2 v42, v[40:41], s[66:67] sc1\n\t"
    "s_mov_b64 exec, -1\n\t"
    "LM_pub14_%=:\n\t"
    "LM_gen13_%=:\n\t"
    "v_rcp_f32 v36, v24\n\t"
    "v_rcp_f32 v38, v26\n\t"
    "s_nop 0\n\t"
    "v_mov_b32 v37, v36\n\t"
    "v_mov_b32 v39, v38\n\t"
    "v_accvgpr_read_b32 v40, a0\n\t"
    "v_accvgpr_read_b32 v41, a1\n\t"
    "v_accvgpr_read_b32 v42, a2\n\t"
    "v_accvgpr_read_b32 v43, a3\n\t"
    "v_accvgpr_read_b32 v44, a4\n\t"
    "v_accvgpr_read_b32 v45, a5\n\t"
    "v_accvgpr_read_b32 v46, a6\n\t"
    "v_accvgpr_read_b32 v47, a7\n\t"
    "v_pk_mul_f32 v[40:41], v[40:41], v[36:37]\n\t"
    "v_pk_mul_f32 v[42:43], v[42:43], v[36:37]\n\t"
    "v_pk_mul_f32 v[44:45], v[44:45], v[36:37]\n\t"
    "v_pk_mul_f32 v[46:47], v[46:47], v[36:37]\n\t"
    "v_cvt_pk_bf16_f32 v40, v40, v41\n\t"
    "v_cvt_pk_bf16_f32 v41, v42, v43\n\t"
    "v_cvt_pk_bf16_f32 v42, v44, v45\n\t"
    "v_cvt_pk_bf16_f32 v43, v46, v47\n\t"
    "global_store_dwordx4 v18, v[40:43], s[46:47] offset:0 sc1\n\t"
    "v_accvgpr_read_b32 v48, a8\n\t"
    "v_accvgpr_read_b32 v49, a9\n\t"
    "v_accvgpr_read_b32 v50, a10\n\t"
    "v_accvgpr_read_b32 v51, a11\n\t"
    "v_accvgpr_read_b32 v52, a12\n\t"
    "v_accvgpr_read_b32 v53, a13\n\t"
    "v_accvgpr_read_b32 v54, a14\n\t"
    "v_accvgpr_read_b32 v55, a15\n\t"
    "v_pk_mul_f32 v[48:49], v[48:49], v[36:37]\n\t"
    "v_pk_mul_f32 v[50:51], v[50:51], v[36:37]\n\t"
    "v_pk_mul_f32 v[52:53], v[52:53], v[36:37]\n\t"
    "v_pk_mul_f32 v[54:55], v[54:55], v[36:37]\n\t"
    "v_cvt_pk_bf16_f32 v48, v48, v49\n\t"
    "v_cvt_pk_bf16_f32 v49, v50, v51\n\t"
    "v_cvt_pk_bf16_f32 v50, v52, v53\n\t"
    "v_cvt_pk_bf16_f32 v51, v54, v55\n\t"
    "global_store_dwordx4 v18, v[48:51], s[46:47] offset:512 sc1\n\t"
    "v_accvgpr_read_b32 v56, a16\n\t"
    "v_accvgpr_read_b32 v57, a17\n\t"
    "v_accvgpr_read_b32 v58, a18\n\t"
    "v_accvgpr_read_b32 v59, a19\n\t"
    "v_accvgpr_read_b32 v60, a20\n\t"
    "v_accvgpr_read_b32 v61, a21\n\t"
    "v_accvgpr_read_b32 v62, a22\n\t"
    "v_accvgpr_read_b32 v63, a23\n\t"
    "v_pk_mul_f32 v[56:57], v[56:57], v[38:39]\n\t"
    "v_pk_mul_f32 v[58:59], v[58:59], v[38:39]\n\t"
    "v_pk_mul_f32 v[60:61], v[60:61], v[38:39]\n\t"
    "v_pk_mul_f32 v[62:63], v[62:63], v[38:39]\n\t"
    "v_cvt_pk_bf16_f32 v56, v56, v57\n\t"
    "v_cvt_pk_bf16_f32 v57, v58, v59\n\t"
    "v_cvt_pk_bf16_f32 v58, v60, v61\n\t"
    "v_cvt_pk_bf16_f32 v59, v62, v63\n\t"
    "global_store_dwordx4 v18, v[56:59], s[46:47] offset:2048 sc1\n\t"
    "v_accvgpr_read_b32 v40, a24\n\t"
    "v_accvgpr_read_b32 v41, a25\n\t"
    "v_accvgpr_read_b32 v42, a26\n\t"
    "v_accvgpr_read_b32 v43, a27\n\t"
    "v_accvgpr_read_b32 v44, a28\n\t"
    "v_accvgpr_read_b32 v45, a29\n\t"
    "v_accvgpr_read_b32 v46, a30\n\t"
    "v_accvgpr_read_b32 v47, a31\n\t"
    "v_pk_mul_f32 v[40:41], v[40:41], v[38:39]\n\t"
    "v_pk_mul_f32 v[42:43], v[42:43], v[38:39]\n\t"
    "v_pk_mul_f32 v[44:45], v[44:45], v[38:39]\n\t"
    "v_pk_mul_f32 v[46:47], v[46:47], v[38:39]\n\t"
    "v_cvt_pk_bf16_f32 v40, v40, v41\n\t"
    "v_cvt_pk_bf16_f32 v41, v42, v43\n\t"
    "v_cvt_pk_bf16_f32 v42, v44, v45\n\t"
    "v_cvt_pk_bf16_f32 v43, v46, v47\n\t"
    "global_store_dwordx4 v18, v[40:43], s[46:47] offset:2560 sc1\n\t"
    "s_add_u32 s46, s46, 4096\n\t"
    "s_addc_u32 s47, s47, 0\n\t"
    "v_accvgpr_read_b32 v48, a32\n\t"
    "v_accvgpr_read_b32 v49, a33\n\t"
    "v_accvgpr_read_b32 v50, a34\n\t"
    "v_accvgpr_read_b32 v51, a35\n\t"
    "v_accvgpr_read_b32 v52, a36\n\t"
    "v_accvgpr_read_b32 v53, a37\n\t"
    "v_accvgpr_read_b32 v54, a38\n\t"
    "v_accvgpr_read_b32 v55, a39\n\t"
    "v_pk_mul_f32 v[48:49], v[48:49], v[36:37]\n\t"
    "v_pk_mul_f32 v[50:51], v[50:51], v[36:37]\n\t"
    "v_pk_mul_f32 v[52:53], v[52:53], v[36:37]\n\t"
    "v_pk_mul_f32 v[54:55], v[54:55], v[36:37]\n\t"
    "v_cvt_pk_bf16_f32 v48, v48, v49\n\t"
    "v_cvt_pk_bf16_f32 v49, v50, v51\n\t"
    "v_cvt_pk_bf16_f32 v50, v52, v53\n\t"
    "v_cvt_pk_bf16_f32 v51, v54, v55\n\t"
    "global_store_dwordx4 v18, v[48:51], s[46:47] offset:0 sc1\n\t"
    "v_accvgpr_read_b32 v56, a40\n\t"
    "v_accvgpr_read_b32 v57, a41\n\t"
    "v_accvgpr_read_b32 v58, a42\n\t"
    "v_accvgpr_read_b32 v59, a43\n\t"
    "v_accvgpr_read_b32 v60, a44\n\t"
    "v_accvgpr_read_b32 v61, a45\n\t"
    "v_accvgpr_read_b32 v62, a46\n\t"
    "v_accvgpr_read_b32 v63, a47\n\t"
    "v_pk_mul_f32 v[56:57], v[56:57], v[36:37]\n\t"
    "v_pk_mul_f32 v[58:59], v[58:59], v[36:37]\n\t"
    "v_pk_mul_f32 v[60:61], v[60:61], v[36:37]\n\t"
    "v_pk_mul_f32 v[62:63], v[62:63], v[36:37]\n\t"
    "v_cvt_pk_bf16_f32 v56, v56, v57\n\t"
    "v_cvt_pk_bf16_f32 v57, v58, v59\n\t"
    "v_cvt_pk_bf16_f32 v58, v60, v61\n\t"
    "v_cvt_pk_bf16_f32 v59, v62, v63\n\t"
    "global_store_dwordx4 v18, v[56:59], s[46:47] offset:512 sc1\n\t"
    "v_accvgpr_read_b32 v40, a48\n\t"
    "v_accvgpr_read_b32 v41, a49\n\t"
    "v_accvgpr_read_b32 v42, a50\n\t"
    "v_accvgpr_read_b32 v43, a51\n\t"
    "v_accvgpr_read_b32 v44, a52\n\t"
    "v_accvgpr_read_b32 v45, a53\n\t"
    "v_accvgpr_read_b32 v46, a54\n\t"
    "v_accvgpr_read_b32 v47, a55\n\t"
    "v_pk_mul_f32 v[40:41], v[40:41], v[38:39]\n\t"
    "v_pk_mul_f32 v[42:43], v[42:43], v[38:39]\n\t"
    "v_pk_mul_f32 v[44:45], v[44:45], v[38:39]\n\t"
    "v_pk_mul_f32 v[46:47], v[46:47], v[38:39]\n\t"
    "v_cvt_pk_bf16_f32 v40, v40, v41\n\t"
    "v_cvt_pk_bf16_f32 v41, v42, v43\n\t"
    "v_cvt_pk_bf16_f32 v42, v44, v45\n\t"
    "v_cvt_pk_bf16_f32 v43, v46, v47\n\t"
    "global_store_dwordx4 v18, v[40:43], s[46:47] offset:2048 sc1\n\t"
    "v_accvgpr_read_b32 v48, a56\n\t"
    "v_accvgpr_read_b32 v49, a57\n\t"
    "v_accvgpr_read_b32 v50, a58\n\t"
    "v_accvgpr_read_b32 v51, a59\n\t"
    "v_accvgpr_read_b32 v52, a60\n\t"
    "v_accvgpr_read_b32 v53, a61\n\t"
    "v_accvgpr_read_b32 v54, a62\n\t"
    "v_accvgpr_read_b32 v55, a63\n\t"
    "v_pk_mul_f32 v[48:49], v[48:49], v[38:39]\n\t"
    "v_pk_mul_f32 v[50:51], v[50:51], v[38:39]\n\t"
    "v_pk_mul_f32 v[52:53], v[52:53], v[38:39]\n\t"
    "v_pk_mul_f32 v[54:55], v[54:55], v[38:39]\n\t"
    "v_cvt_pk_bf16_f32 v48, v48, v49\n\t"
    "v_cvt_pk_bf16_f32 v49, v50, v51\n\t"
    "v_cvt_pk_bf16_f32 v50, v52, v53\n\t"
    "v_cvt_pk_bf16_f32 v51, v54, v55\n\t"
    "global_store_dwordx4 v18, v[48:51], s[46:47] offset:2560 sc1\n\t"
    "s_add_u32 s46, s46, 4096\n\t"
    "s_addc_u32 s47, s47, 0\n\t"
    "v_accvgpr_read_b32 v56, a64\n\t"
    "v_accvgpr_read_b32 v57, a65\n\t"
    "v_accvgpr_read_b32 v58, a66\n\t"
    "v_accvgpr_read_b32 v59, a67\n\t"
    "v_accvgpr_read_b32 v60, a68\n\t"
    "v_accvgpr_read_b32 v61, a69\n\t"
    "v_accvgpr_read_b32 v62, a70\n\t"
    "v_accvgpr_read_b32 v63, a71\n\t"
    "v_pk_mul_f32 v[56:57], v[56:57], v[36:37]\n\t"
    "v_pk_mul_f32 v[58:59], v[58:59], v[36:37]\n\t"
    "v_pk_mul_f32 v[60:61], v[60:61], v[36:37]\n\t"
    "v_pk_mul_f32 v[62:63], v[62:63], v[36:37]\n\t"
    "v_cvt_pk_bf16_f32 v56, v56, v57\n\t"
    "v_cvt_pk_bf16_f32 v57, v58, v59\n\t"
    "v_cvt_pk_bf16_f32 v58, v60, v61\n\t"
    "v_cvt_pk_bf16_f32 v59, v62, v63\n\t"
    "global_store_dwordx4 v18, v[56:59], s[46:47] offset:0 sc1\n\t"
    "v_accvgpr_read_b32 v40, a72\n\t"
    "v_accvgpr_read_b32 v41, a73\n\t"
    "v_accvgpr_read_b32 v42, a74\n\t"
    "v_accvgpr_read_b32 v43, a75\n\t"
    "v_accvgpr_read_b32 v44, a76\n\t"
    "v_accvgpr_read_b32 v45, a77\n\t"
    "v_accvgpr_read_b32 v46, a78\n\t"
    "v_accvgpr_read_b32 v47, a79\n\t"
    "v_pk_mul_f32 v[40:41], v[40:41], v[36:37]\n\t"
    "v_pk_mul_f32 v[42:43], v[42:43], v[36:37]\n\t"
    "v_pk_mul_f32 v[44:45], v[44:45], v[36:37]\n\t"
    "v_pk_mul_f32 v[46:47], v[46:47], v[36:37]\n\t"
    "v_cvt_pk_bf16_f32 v40, v40, v41\n\t"
    "v_cvt_pk_bf16_f32 v41, v42, v43\n\t"
    "v_cvt_pk_bf16_f32 v42, v44, v45\n\t"
    "v_cvt_pk_bf16_f32 v43, v46, v47\n\t"
    "global_store_dwordx4 v18, v[40:43], s[46:47] offset:512 sc1\n\t"
    "v_accvgpr_read_b32 v48, a80\n\t"
    "v_accvgpr_read_b32 v49, a81\n\t"
    "v_accvgpr_read_b32 v50, a82\n\t"
    "v_accvgpr_read_b32 v51, a83\n\t"
    "v_accvgpr_read_b32 v52, a84\n\t"
    "v_accvgpr_read_b32 v53, a85\n\t"
    "v_accvgpr_read_b32 v54, a86\n\t"
    "v_accvgpr_read_b32 v55, a87\n\t"
    "v_pk_mul_f32 v[48:49], v[48:49], v[38:39]\n\t"
    "v_pk_mul_f32 v[50:51], v[50:51], v[38:39]\n\t"
    "v_pk_mul_f32 v[52:53], v[52:53], v[38:39]\n\t"
    "v_pk_mul_f32 v[54:55], v[54:55], v[38:39]\n\t"
    "v_cvt_pk_bf16_f32 v48, v48, v49\n\t"
    "v_cvt_pk_bf16_f32 v49, v50, v51\n\t"
    "v_cvt_pk_bf16_f32 v50, v52, v53\n\t"
    "v_cvt_pk_bf16_f32 v51, v54, v55\n\t"
    "global_store_dwordx4 v18, v[48:51], s[46:47] offset:2048 sc1\n\t"
    "v_accvgpr_read_b32 v56, a88\n\t"
    "v_accvgpr_read_b32 v57, a89\n\t"
    "v_accvgpr_read_b32 v58, a90\n\t"
    "v_accvgpr_read_b32 v59, a91\n\t"
    "v_accvgpr_read_b32 v60, a92\n\t"
    "v_accvgpr_read_b32 v61, a93\n\t"
    "v_accvgpr_read_b32 v62, a94\n\t"
    "v_accvgpr_read_b32 v63, a95\n\t"
    "v_pk_mul_f32 v[56:57], v[56:57], v[38:39]\n\t"
    "v_pk_mul_f32 v[58:59], v[58:59], v[38:39]\n\t"
    "v_pk_mul_f32 v[60:61], v[60:61], v[38:39]\n\t"
    "v_pk_mul_f32 v[62:63], v[62:63], v[38:39]\n\t"
    "v_cvt_pk_bf16_f32 v56, v56, v57\n\t"
    "v_cvt_pk_bf16_f32 v57, v58, v59\n\t"
    "v_cvt_pk_bf16_f32 v58, v60, v61\n\t"
    "v_cvt_pk_bf16_f32 v59, v62, v63\n\t"
    "global_store_dwordx4 v18, v[56:59], s[46:47] offset:2560 sc1\n\t"
    "s_add_u32 s46, s46, 4096\n\t"
    "s_addc_u32 s47, s47, 0\n\t"
    "v_accvgpr_read_b32 v40, a96\n\t"
    "v_accvgpr_read_b32 v41, a97\n\t"
    "v_accvgpr_read_b32 v42, a98\n\t"
    "v_accvgpr_read_b32 v43, a99\n\t"
    "v_accvgpr_read_b32 v44, a100\n\t"
    "v_accvgpr_read_b32 v45, a101\n\t"
    "v_accvgpr_read_b32 v46, a102\n\t"
    "v_accvgpr_read_b32 v47, a103\n\t"
    "v_pk_mul_f32 v[40:41], v[40:41], v[36:37]\n\t"
    "v_pk_mul_f32 v[42:43], v[42:43], v[36:37]\n\t"
    "v_pk_mul_f32 v[44:45], v[44:45], v[36:37]\n\t"
    "v_pk_mul_f32 v[46:47], v[46:47], v[36:37]\n\t"
    "v_cvt_pk_bf16_f32 v40, v40, v41\n\t"
    "v_cvt_pk_bf16_f32 v41, v42, v43\n\t"
    "v_cvt_pk_bf16_f32 v42, v44, v45\n\t"
    "v_cvt_pk_bf16_f32 v43, v46, v47\n\t"
    "global_store_dwordx4 v18, v[40:43], s[46:47] offset:0 sc1\n\t"
    "v_accvgpr_read_b32 v48, a104\n\t"
    "v_accvgpr_read_b32 v49, a105\n\t"
    "v_accvgpr_read_b32 v50, a106\n\t"
    "v_accvgpr_read_b32 v51, a107\n\t"
    "v_accvgpr_read_b32 v52, a108\n\t"
    "v_accvgpr_read_b32 v53, a109\n\t"
    "v_accvgpr_read_b32 v54, a110\n\t"
    "v_accvgpr_read_b32 v55, a111\n\t"
    "v_pk_mul_f32 v[48:49], v[48:49], v[36:37]\n\t"
    "v_pk_mul_f32 v[50:51], v[50:51], v[36:37]\n\t"
    "v_pk_mul_f32 v[52:53], v[52:53], v[36:37]\n\t"
    "v_pk_mul_f32 v[54:55], v[54:55], v[36:37]\n\t"
    "v_cvt_pk_bf16_f32 v48, v48, v49\n\t"
    "v_cvt_pk_bf16_f32 v49, v50, v51\n\t"
    "v_cvt_pk_bf16_f32 v50, v52, v53\n\t"
    "v_cvt_pk_bf16_f32 v51, v54, v55\n\t"
    "global_store_dwordx4 v18, v[48:51], s[46:47] offset:512 sc1\n\t"
    "v_accvgpr_read_b32 v56, a112\n\t"
    "v_accvgpr_read_b32 v57, a113\n\t"
    "v_accvgpr_read_b32 v58, a114\n\t"
    "v_accvgpr_read_b32 v59, a115\n\t"
    "v_accvgpr_read_b32 v60, a116\n\t"
    "v_accvgpr_read_b32 v61, a117\n\t"
    "v_accvgpr_read_b32 v62, a118\n\t"
    "v_accvgpr_read_b32 v63, a119\n\t"
    "v_pk_mul_f32 v[56:57], v[56:57], v[38:39]\n\t"
    "v_pk_mul_f32 v[58:59], v[58:59], v[38:39]\n\t"
    "v_pk_mul_f32 v[60:61], v[60:61], v[38:39]\n\t"
    "v_pk_mul_f32 v[62:63], v[62:63], v[38:39]\n\t"
    "v_cvt_pk_bf16_f32 v56, v56, v57\n\t"
    "v_cvt_pk_bf16_f32 v57, v58, v59\n\t"
    "v_cvt_pk_bf16_f32 v58, v60, v61\n\t"
    "v_cvt_pk_bf16_f32 v59, v62, v63\n\t"
    "global_store_dwordx4 v18, v[56:59], s[46:47] offset:2048 sc1\n\t"
    "v_accvgpr_read_b32 v40, a120\n\t"
    "v_accvgpr_read_b32 v41, a121\n\t"
    "v_accvgpr_read_b32 v42, a122\n\t"
    "v_accvgpr_read_b32 v43, a123\n\t"
    "v_accvgpr_read_b32 v44, a124\n\t"
    "v_accvgpr_read_b32 v45, a125\n\t"
    "v_accvgpr_read_b32 v46, a126\n\t"
    "v_accvgpr_read_b32 v47, a127\n\t"
    "v_pk_mul_f32 v[40:41], v[40:41], v[38:39]\n\t"
    "v_pk_mul_f32 v[42:43], v[42:43], v[38:39]\n\t"
    "v_pk_mul_f32 v[44:45], v[44:45], v[38:39]\n\t"
    "v_pk_mul_f32 v[46:47], v[46:47], v[38:39]\n\t"
    "v_cvt_pk_bf16_f32 v40, v40, v41\n\t"
    "v_cvt_pk_bf16_f32 v41, v42, v43\n\t"
    "v_cvt_pk_bf16_f32 v42, v44, v45\n\t"
    "v_cvt_pk_bf16_f32 v43, v46, v47\n\t"
    "global_store_dwordx4 v18, v[40:43], s[46:47] offset:2560 sc1\n\t"
    "s_nop 1\n\t"
      : "+{v10}"(Awa[0]), "+{v11}"(Awa[1]), "+{v12}"(Awa[2]), "+{v13}"(Awa[3]), "+{v61}"(Apl1), "+{v62}"(Apl2), "+{v63}"(Act),
        "+{v192}"(Ac1[0]), "+{v193}"(Ac1[1]), "+{v194}"(Ac1[2]), "+{v195}"(Ac1[3]), "+{v196}"(Ac1[4]),
        "+{v197}"(Ac2[0]), "+{v198}"(Ac2[1]), "+{v199}"(Ac2[2]), "+{v200}"(Ac2[3]), "+{v201}"(Ac2[4]), "+{v202}"(Ac2[5]), "+{v203}"(Ac2[6]),
        "+{s[36:37]}"(sXH), "+{s[38:39]}"(sW), "+{s[42:43]}"(sK), "+{s[44:45]}"(sV), "+{s[46:47]}"(sO), "+{s[100:101]}"(pfk), "+{s[72:73]}"(pfv), "+{s58}"(m0p)
      : "{v1}"(Axo), "{v2}"(Awo[0]), "{v3}"(Awo[1]), "{v4}"(Awo[2]), "{v5}"(Awo[3]), "{v6}"(Ako[0]), "{v7}"(Ako[1]), "{v8}"(Ako[2]), "{v9}"(Ako[3]),
        "{v14}"(Aka[0]), "{v15}"(Aka[1]), "{v16}"(Avb[0]), "{v17}"(Avb[1]), "{v18}"(Aoo),
        "{s[40:41]}"(src1), "{s48}"(woff), "{s[60:61]}"(dst1), "{s[62:63]}"(src2), "{s[96:97]}"(dst2), "{s57}"(rem2), "{s99}"(m0c),
        "{s[64:65]}"(tag), "{s[66:67]}"(ws), "{s68}"(wv), "{s70}"(myf), "{s71}"(b)
      : "v20", "v21", "v22", "v23", "v24", "v25", "v26", "v27", "v28", "v29", "v30", "v31", "v32", "v33", "v34", "v35", "v36", "v37", "v38", "v39", "v40", "v41", "v42", "v43", "v44", "v45", "v46", "v47", "v48", "v49", "v50", "v51", "v52", "v53", "v54", "v55", "v56", "v57", "v58", "v59", "v60", "v64", "v65", "v66", "v67", "v68", "v69", "v70", "v71", "v72", "v73", "v74", "v75", "v76", "v77", "v78", "v79", "v80", "v81", "v82", "v83", "v84", "v85", "v86", "v87", "v88", "v89", "v90", "v91", "v92", "v93", "v94", "v95", "v96", "v97", "v98", "v99", "v100", "v101", "v102", "v103", "v104", "v105", "v106", "v107", "v108", "v109", "v110", "v111", "v112", "v113", "v114", "v115", "v116", "v117", "v118", "v119", "v120", "v121", "v122", "v123", "v124", "v125", "v126", "v127", "v128", "v129", "v130", "v131", "v132", "v133", "v134", "v135", "v136", "v137", "v138", "v139", "v140", "v141", "v142", "v143", "v144", "v145", "v146", "v147", "v148", "v149", "v150", "v151", "v152", "v153", "v154", "v155", "v156", "v157", "v158", "v159", "v160", "v161", "v162", "v163", "v164", "v165", "v166", "v167", "v168", "v169", "v170", "v171", "v172", "v173", "v174", "v175", "v176", "v177", "v178", "v179", "v180", "v181", "v182", "v183", "v184", "v185", "v186", "v187", "v188", "v189", "v190", "v191", "v204", "v205", "v206", "v207", "v208", "v209", "v210", "v211", "v212", "v213", "v214", "v215", "v216", "v217", "v218", "v219", "v220", "v221", "v222", "v223", "v224", "v225", "v226", "v227", "v228", "v229", "v230", "v231", "v232", "v233", "v234", "v235", "v236", "v237", "v238", "v239", "v240", "v241", "v242", "v243", "v244", "v245", "v246", "v247", "v248", "v249", "v250", "v251", "v252", "v253", "v254", "v255", "a0", "a1", "a2", "a3", "a4", "a5", "a6", "a7", "a8", "a9", "a10", "a11", "a12", "a13", "a14", "a15", "a16", "a17", "a18", "a19", "a20", "a21", "a22", "a23", "a24", "a25", "a26", "a27", "a28", "a29", "a30", "a31", "a32", "a33", "a34", "a35", "a36", "a37", "a38", "a39", "a40", "a41", "a42", "a43", "a44", "a45", "a46", "a47", "a48", "a49", "a50", "a51", "a52", "a53", "a54", "a55", "a56", "a57", "a58", "a59", "a60", "a61", "a62", "a63", "a64", "a65", "a66", "a67", "a68", "a69", "a70", "a71", "a72", "a73", "a74", "a75", "a76", "a77", "a78", "a79", "a80", "a81", "a82", "a83", "a84", "a85", "a86", "a87", "a88", "a89", "a90", "a91", "a92", "a93", "a94", "a95", "a96", "a97", "a98", "a99", "a100", "a101", "a102", "a103", "a104", "a105", "a106", "a107", "a108", "a109", "a110", "a111", "a112", "a113", "a114", "a115", "a116", "a117", "a118", "a119", "a120", "a121", "a122", "a123", "a124", "a125", "a126", "a127", "a128", "a129", "a130", "a131", "a132", "a133", "a134", "a135", "a136", "a137", "a138", "a139", "a140", "a141", "a142", "a143", "a144", "a145", "a146", "a147", "a148", "a149", "a150", "a151", "a152", "a153", "a154", "a155", "a156", "a157", "a158", "a159", "a160", "a161", "a162", "a163", "a164", "a165", "a166", "a167", "a168", "a169", "a170", "a171", "a172", "a173", "a174", "a175", "a176", "a177", "a178", "a179", "a180", "a181", "a182", "a183", "a184", "a185", "a186", "a187", "a188", "a189", "a190", "a191", "a192", "a193", "a194", "a195", "a196", "a197", "a198", "a199", "a200", "a201", "a202", "a203", "a204", "a205", "a206", "a207", "a208", "a209", "a210", "a211", "a212", "a213", "a214", "a215", "a216", "a217", "a218", "a219", "a220", "a221", "a222", "a223", "a224", "a225", "a226", "a227", "a228", "a229", "a230", "a231", "a232", "a233", "a234", "a235", "a236", "a237", "a238", "a239", "a240", "a241", "a242", "a243", "a244", "a245", "a246", "a247", "a248", "a249", "a250", "a251", "a252", "a253", "a254", "a255", "s49", "s50", "s51", "s52", "s53", "s54", "s55", "s56", "s59", "s69", "s88", "s89", "s90", "s91", "s92", "s93", "s94", "s95", "s98", "vcc", "memory");
    STAMP_OUT(0)
  }
#define BD_GLUE(isB)                                                                                                                        \
    unsigned lane = __builtin_amdgcn_mbcnt_hi(-1, __builtin_amdgcn_mbcnt_lo(-1, 0));                                                        \
    asm volatile("" : "+v"(lane));                                                                                                          \
    constexpr unsigned KS = isB ? 128u : 56u, KSW = isB ? 32u : 14u;                                                                        \
    const unsigned w = wv, x = b & 7u, jj = b >> 3;                                                                                         \
    const unsigned nt = 4u * (x & 1u) + (jj & 3u), qt = 8u * (x >> 1) + (jj >> 2);                                                          \
    const unsigned odd = nt & 1u;                                                                                                           \
    const unsigned nb0 = 5u * nt + odd, nb1 = 5u * nt + (odd ? 0u : 4u);                                                                    \
    const char* sF = ws + (isB ? WS_OF : WS_HH) + ((size_t)qt * KS + w * KSW) * 4096u;                                                      \
    const char* sW = ws + (isB ? WS_WOP : WS_WDP) + ((size_t)nt * KS + w * KSW) * 5120u + 2048u;                                            \
    unsigned fo0 = 0u, wo0 = 0u;                                                                                                            \
    const unsigned g = lane >> 4, c16 = lane & 15u;                                                                                         \
    const unsigned rw = lb + 81920u + w * 20480u + lane * 16u;                                                                              \
    const unsigned rr = lb + 81920u + w * 1024u + lane * 16u;                                                                               \
    const unsigned qb16 = qt * 4u + w, row = qb16 * 16u + c16;                                                                              \
    const unsigned fp = (qb16 * 20u + (nb0 >> 1)) * 1024u + lane * 16u, f1 = (qb16 * 20u + (nb1 >> 1)) * 1024u + lane * 16u + (nb1 & 1u) * 8u; \
    unsigned rs, rs1, st, st1;                                                                                                              \
    if (isB) { rs = row * 1280u + (nb0 * 16u + 4u * g) * 2u; rs1 = row * 1280u + (nb1 * 16u + 4u * g) * 2u; st = fp; st1 = f1; }           \
    else { rs = fp; rs1 = f1; st = row * 1280u + ((nb0 + (g & 1u)) * 16u + 8u * (g >> 1)) * 2u; st1 = row * 1280u + (nb1 * 16u + 4u * g) * 2u; } \
    const unsigned go = (nb0 * 16u + 4u * g) * 4u, go1 = (nb1 * 16u + 4u * g) * 4u;                                                         \
    const unsigned co = go + 2u * 2560u, co1 = go1 + 2u * 2560u;                                                                            \
    const unsigned ss = row * 64u + nt * 8u;                                                                                                \
    const unsigned fo = lane * 16u;
  {
    BD_GLUE(true)
    const unsigned ja = b >> 3, ha = 4u * (b & 7u) + (ja & 3u), rba = ja >> 2;
    const unsigned myf = __builtin_amdgcn_readfirstlane(F3 + 8u * (32u * rba + ha));
    const unsigned pl = F3 + 8u * (32u * (qt >> 2) + (lane & 31u));
    asm volatile(
    "v_add_u32 v5, s59, v1\n\t"
    "v_add_u32 v4, s58, v1\n\t"
    "s_waitcnt vmcnt(0)\n\t"
    "s_barrier\n\t"
    "s_cmp_lg_u32 s68, 0\n\t"
    "s_cbranch_scc1 LM_pub15_%=\n\t"
    "v_mov_b32 v100, s64\n\t"
    "v_mov_b32 v101, s65\n\t"
    "v_mov_b32 v102, s70\n\t"
    "s_mov_b64 exec, 1\n\t"
    "global_store_dwordx2 v102, v[100:101], s[66:67] sc1\n\t"
    "s_mov_b64 exec, -1\n\t"
    "buffer_inv sc1\n\t"
    "LM_pub15_%=:\n\t"
    "global_load_dwordx4 a[0:3], v5, s[38:39] offset:-2048 sc1\n\t"
    "global_load_dwordx4 a[4:7], v5, s[38:39] offset:-1024 sc1\n\t"
    "global_load_dwordx4 a[8:11], v5, s[38:39] offset:0 sc1\n\t"
    "global_load_dwordx4 a[12:15], v5, s[38:39] offset:1024 sc1\n\t"
    "global_load_dwordx4 a[16:19], v5, s[38:39] offset:2048 sc1\n\t"
    "s_add_u32 s59, s59, 5120\n\t"
    "s_cmp_eq_u32 s59, 163840\n\t"
    "s_cselect_b32 s59, 0, s59\n\t"
    "v_add_u32 v5, s59, v1\n\t"
    "global_load_dwordx4 a[36:39], v5, s[38:39] offset:-2048 sc1\n\t"
    "global_load_dwordx4 a[40:43], v5, s[38:39] offset:-1024 sc1\n\t"
    "global_load_dwordx4 a[44:47], v5, s[38:39] offset:0 sc1\n\t"
    "global_load_dwordx4 a[48:51], v5, s[38:39] offset:1024 sc1\n\t"
    "global_load_dwordx4 a[52:55], v5, s[38:39] offset:2048 sc1\n\t"
    "s_add_u32 s59, s59, 5120\n\t"
    "s_cmp_eq_u32 s59, 163840\n\t"
    "s_cselect_b32 s59, 0, s59\n\t"
    "v_add_u32 v5, s59, v1\n\t"
    "global_load_dwordx4 a[72:75], v5, s[38:39] offset:-2048 sc1\n\t"
    "global_load_dwordx4 a[76:79], v5, s[38:39] offset:-1024 sc1\n\t"
    "global_load_dwordx4 a[80:83], v5, s[38:39] offset:0 sc1\n\t"
    "global_load_dwordx4 a[84:87], v5, s[38:39] offset:1024 sc1\n\t"
    "global_load_dwordx4 a[88:91], v5, s[38:39] offset:2048 sc1\n\t"
    "s_add_u32 s59, s59, 5120\n\t"
    "s_cmp_eq_u32 s59, 163840\n\t"
    "s_cselect_b32 s59, 0, s59\n\t"
    "v_add_u32 v5, s59, v1\n\t"
    "global_load_dwordx4 a[108:111], v5, s[38:39] offset:-2048 sc1\n\t"
    "global_load_dwordx4 a[112:115], v5, s[38:39] offset:-1024 sc1\n\t"
    "global_load_dwordx4 a[116:119], v5, s[38:39] offset:0 sc1\n\t"
    "global_load_dwordx4 a[120:123], v5, s[38:39] offset:1024 sc1\n\t"
    "global_load_dwordx4 a[124:127], v5, s[38:39] offset:2048 sc1\n\t"
    "s_add_u32 s59, s59, 5120\n\t"
    "s_cmp_eq_u32 s59, 163840\n\t"
    "s_cselect_b32 s59, 0, s59\n\t"
    "v_add_u32 v5, s59, v1\n\t"
    "global_load_dwordx4 a[144:147], v5, s[38:39] offset:-2048 sc1\n\t"
    "global_load_dwordx4 a[148:151], v5, s[38:39] offset:-1024 sc1\n\t"
    "global_load_dwordx4 a[152:155], v5, s[38:39] offset:0 sc1\n\t"
    "global_load_dwordx4 a[156:159], v5, s[38:39] offset:1024 sc1\n\t"
    "global_load_dwordx4 a[160:163], v5, s[38:39] offset:2048 sc1\n\t"
    "s_add_u32 s59, s59, 5120\n\t"
    "s_cmp_eq_u32 s59, 163840\n\t"
    "s_cselect_b32 s59, 0, s59\n\t"
    "v_add_u32 v5, s59, v1\n\t"
    "v_mov_b32 v20, 0\n\t"
    "v_mov_b32 v21, 0\n\t"
    "v_mov_b32 v22, 0\n\t"
    "v_mov_b32 v23, 0\n\t"
    "v_mov_b32 v24, 0\n\t"
    "v_mov_b32 v25, 0\n\t"
    "v_mov_b32 v26, 0\n\t"
    "v_mov_b32 v27, 0\n\t"
    "v_mov_b32 v28, 0\n\t"
    "v_mov_b32 v29, 0\n\t"
    "v_mov_b32 v30, 0\n\t"
    "v_mov_b32 v31, 0\n\t"
    "v_mov_b32 v32, 0\n\t"
    "v_mov_b32 v33, 0\n\t"
    "v_mov_b32 v34, 0\n\t"
    "v_mov_b32 v35, 0\n\t"
    "v_mov_b32 v36, 0\n\t"
    "v_mov_b32 v37, 0\n\t"
    "v_mov_b32 v38, 0\n\t"
    "v_mov_b32 v39, 0\n\t"
    "v_mov_b32 v40, 0\n\t"
    "v_mov_b32 v41, 0\n\t"
    "v_mov_b32 v42, 0\n\t"
    "v_mov_b32 v43, 0\n\t"
    "v_mov_b32 v44, 0\n\t"
    "v_mov_b32 v45, 0\n\t"
    "v_mov_b32 v46, 0\n\t"
    "v_mov_b32 v47, 0\n\t"
    "v_mov_b32 v48, 0\n\t"
    "v_mov_b32 v49, 0\n\t"
    "v_mov_b32 v50, 0\n\t"
    "v_mov_b32 v51, 0\n\t"
    "v_mov_b32 v52, 0\n\t"
    "v_mov_b32 v53, 0\n\t"
    "v_mov_b32 v54, 0\n\t"
    "v_mov_b32 v55, 0\n\t"
    "v_mov_b32 v56, 0\n\t"
    "v_mov_b32 v57, 0\n\t"
    "v_mov_b32 v58, 0\n\t"
    "v_mov_b32 v59, 0\n\t"
    "v_mov_b32 v60, 0\n\t"
    "v_mov_b32 v61, 0\n\t"
    "v_mov_b32 v62, 0\n\t"
    "v_mov_b32 v63, 0\n\t"
    "v_mov_b32 v64, 0\n\t"
    "v_mov_b32 v65, 0\n\t"
    "v_mov_b32 v66, 0\n\t"
    "v_mov_b32 v67, 0\n\t"
    "v_mov_b32 v68, 0\n\t"
    "v_mov_b32 v69, 0\n\t"
    "v_mov_b32 v70, 0\n\t"
    "v_mov_b32 v71, 0\n\t"
    "v_mov_b32 v72, 0\n\t"
    "v_mov_b32 v73, 0\n\t"
    "v_mov_b32 v74, 0\n\t"
    "v_mov_b32 v75, 0\n\t"
    "v_mov_b32 v76, 0\n\t"
    "v_mov_b32 v77, 0\n\t"
    "v_mov_b32 v78, 0\n\t"
    "v_mov_b32 v79, 0\n\t"
    "v_mov_b32 v80, 0\n\t"
    "v_mov_b32 v81, 0\n\t"
    "v_mov_b32 v82, 0\n\t"
    "v_mov_b32 v83, 0\n\t"
    "v_mov_b32 v84, 0\n\t"
    "v_mov_b32 v85, 0\n\t"
    "v_mov_b32 v86, 0\n\t"
    "v_mov_b32 v87, 0\n\t"
    "v_mov_b32 v88, 0\n\t"
    "v_mov_b32 v89, 0\n\t"
    "v_mov_b32 v90, 0\n\t"
    "v_mov_b32 v91, 0\n\t"
    "v_mov_b32 v92, 0\n\t"
    "v_mov_b32 v93, 0\n\t"
    "v_mov_b32 v94, 0\n\t"
    "v_mov_b32 v95, 0\n\t"
    "v_mov_b32 v96, 0\n\t"
    "v_mov_b32 v97, 0\n\t"
    "v_mov_b32 v98, 0\n\t"
    "v_mov_b32 v99, 0\n\t"
    "s_mov_b32 s69, 100000\n\t"
    "global_load_dwordx2 v[100:101], v18, s[66:67] sc1\n\t"
    "s_sleep 2\n\t"
    "LM_poll16_%=:\n\t"
    "global_load_dwordx2 v[102:103], v18, s[66:67] sc1\n\t"
    "s_waitcnt vmcnt(1)\n\t"
    "v_cmp_eq_u64 vcc, s[64:65], v[100:101]\n\t"
    "s_cmp_eq_u64 vcc, -1\n\t"
    "s_cbranch_scc1 LM_polld17_%=\n\t"
    "global_load_dwordx2 v[100:101], v18, s[66:67] sc1\n\t"
    "s_waitcnt vmcnt(1)\n\t"
    "v_cmp_eq_u64 vcc, s[64:65], v[102:103]\n\t"
    "s_cmp_eq_u64 vcc, -1\n\t"
    "s_cbranch_scc1 LM_polld17_%=\n\t"
    "s_sub_u32 s69, s69, 1\n\t"
    "s_cmp_lg_u32 s69, 0\n\t"
    "s_cbranch_scc1 LM_poll16_%=\n\t"
    "LM_polld17_%=:\n\t"
    "global_load_dwordx4 a[20:23], v4, s[36:37] offset:0 sc1\n\t"
    "global_load_dwordx4 a[24:27], v4, s[36:37] offset:1024 sc1\n\t"
    "global_load_dwordx4 a[28:31], v4, s[36:37] offset:2048 sc1\n\t"
    "global_load_dwordx4 a[32:35], v4, s[36:37] offset:3072 sc1\n\t"
    "s_add_u32 s58, s58, 4096\n\t"
    "s_cmp_eq_u32 s58, 131072\n\t"
    "s_cselect_b32 s58, 0, s58\n\t"
    "v_add_u32 v4, s58, v1\n\t"
    "global_load_dwordx4 a[56:59], v4, s[36:37] offset:0 sc1\n\t"
    "global_load_dwordx4 a[60:63], v4, s[36:37] offset:1024 sc1\n\t"
    "global_load_dwordx4 a[64:67], v4, s[36:37] offset:2048 sc1\n\t"
    "global_load_dwordx4 a[68:71], v4, s[36:37] offset:3072 sc1\n\t"
    "s_add_u32 s58, s58, 4096\n\t"
    "s_cmp_eq_u32 s58, 131072\n\t"
    "s_cselect_b32 s58, 0, s58\n\t"
    "v_add_u32 v4, s58, v1\n\t"
    "global_load_dwordx4 a[92:95], v4, s[36:37] offset:0 sc1\n\t"
    "global_load_dwordx4 a[96:99], v4, s[36:37] offset:1024 sc1\n\t"
    "global_load_dwordx4 a[100:103], v4, s[36:37] offset:2048 sc1\n\t"
    "global_load_dwordx4 a[104:107], v4, s[36:37] offset:3072 sc1\n\t"
    "s_add_u32 s58, s58, 4096\n\t"
    "s_cmp_eq_u32 s58, 131072\n\t"
    "s_cselect_b32 s58, 0, s58\n\t"
    "v_add_u32 v4, s58, v1\n\t"
    "global_load_dwordx4 a[128:131], v4, s[36:37] offset:0 sc1\n\t"
    "global_load_dwordx4 a[132:135], v4, s[36:37] offset:1024 sc1\n\t"
    "global_load_dwordx4 a[136:139], v4, s[36:37] offset:2048 sc1\n\t"
    "global_load_dwordx4 a[140:143], v4, s[36:37] offset:3072 sc1\n\t"
    "s_add_u32 s58, s58, 4096\n\t"
    "s_cmp_eq_u32 s58, 131072\n\t"
    "s_cselect_b32 s58, 0, s58\n\t"
    "v_add_u32 v4, s58, v1\n\t"
    "global_load_dwordx4 a[164:167], v4, s[36:37] offset:0 sc1\n\t"
    "global_load_dwordx4 a[168:171], v4, s[36:37] offset:1024 sc1\n\t"
    "global_load_dwordx4 a[172:175], v4, s[36:37] offset:2048 sc1\n\t"
    "global_load_dwordx4 a[176:179], v4, s[36:37] offset:3072 sc1\n\t"
    "s_add_u32 s58, s58, 4096\n\t"
    "s_cmp_eq_u32 s58, 131072\n\t"
    "s_cselect_b32 s58, 0, s58\n\t"
    "v_add_u32 v4, s58, v1\n\t"
    "s_mov_b32 s52, 4\n\t"
    "LM_18_%=:\n\t"
    "s_waitcnt vmcnt(16)\n\t"
    "v_mfma_f32_16x16x32_bf16 v[20:23], a[0:3], a[20:23], v[20:23]\n\t"
    "global_load_dwordx4 a[180:183], v5, s[38:39] offset:-2048 sc1\n\t"
    "v_mfma_f32_16x16x32_bf16 v[24:27], a[0:3], a[24:27], v[24:27]\n\t"
    "v_mfma_f32_16x16x32_bf16 v[28:31], a[0:3], a[28:31], v[28:31]\n\t"
    "global_load_dwordx4 a[200:203], v4, s[36:37] offset:0 sc1\n\t"
    "v_mfma_f32_16x16x32_bf16 v[32:35], a[0:3], a[32:35], v[32:35]\n\t"
    "v_mfma_f32_16x16x32_bf16 v[36:39], a[4:7], a[20:23], v[36:39]\n\t"
    "global_load_dwordx4 a[204:207], v4, s[36:37] offset:1024 sc1\n\t"
    "v_mfma_f32_16x16x32_bf16 v[40:43], a[4:7], a[24:27], v[40:43]\n\t"
    "v_mfma_f32_16x16x32_bf16 v[44:47], a[4:7], a[28:31], v[44:47]\n\t"
    "global_load_dwordx4 a[208:211], v4, s[36:37] offset:2048 sc1\n\t"
    "v_mfma_f32_16x16x32_bf16 v[48:51], a[4:7], a[32:35], v[48:51]\n\t"
    "v_mfma_f32_16x16x32_bf16 v[52:55], a[8:11], a[20:23], v[52:55]\n\t"
    "global_load_dwordx4 a[212:215], v4, s[36:37] offset:3072 sc1\n\t"
    "v_mfma_f32_16x16x32_bf16 v[56:59], a[8:11], a[24:27], v[56:59]\n\t"
    "v_mfma_f32_16x16x32_bf16 v[60:63], a[8:11], a[28:31], v[60:63]\n\t"
    "global_load_dwordx4 a[184:187], v5, s[38:39] offset:-1024 sc1\n\t"
    "v_mfma_f32_16x16x32_bf16 v[64:67], a[8:11], a[32:35], v[64:67]\n\t"
    "v_mfma_f32_16x16x32_bf16 v[68:71], a[12:15], a[20:23], v[68:71]\n\t"
    "global_load_dwordx4 a[188:191], v5, s[38:39] offset:0 sc1\n\t"
    "v_mfma_f32_16x16x32_bf16 v[72:75], a[12:15], a[24:27], v[72:75]\n\t"
    "v_mfma_f32_16x16x32_bf16 v[76:79], a[12:15], a[28:31], v[76:79]\n\t"
    "global_load_dwordx4 a[192:195], v5, s[38:39] offset:1024 sc1\n\t"
    "v_mfma_f32_16x16x32_bf16 v[80:83], a[12:15], a[32:35], v[80:83]\n\t"
    "v_mfma_f32_16x16x32_bf16 v[84:87], a[16:19], a[20:23], v[84:87]\n\t"
    "global_load_dwordx4 a[196:199], v5, s[38:39] offset:2048 sc1\n\t"
    "v_mfma_f32_16x16x32_bf16 v[88:91], a[16:19], a[24:27], v[88:91]\n\t"
    "v_mfma_f32_16x16x32_bf16 v[92:95], a[16:19], a[28:31], v[92:95]\n\t"
    "v_mfma_f32_16x16x32_bf16 v[96:99], a[16:19], a[32:35], v[96:99]\n\t"
    "s_add_u32 s58, s58, 4096\n\t"
    "s_cmp_eq_u32 s58, 131072\n\t"
    "s_cselect_b32 s58, 0, s58\n\t"
    "v_add_u32 v4, s58, v1\n\t"
    "s_add_u32 s59, s59, 5120\n\t"
    "s_cmp_eq_u32 s59, 163840\n\t"
    "s_cselect_b32 s59, 0, s59\n\t"
    "v_add_u32 v5, s59, v1\n\t"
    "s_waitcnt vmcnt(21)\n\t"
    "v_mfma_f32_16x16x32_bf16 v[20:23], a[36:39], a[56:59], v[20:23]\n\t"
    "global_load_dwordx4 a[0:3], v5, s[38:39] offset:-2048 sc1\n\t"
    "v_mfma_f32_16x16x32_bf16 v[24:27], a[36:39], a[60:63], v[24:27]\n\t"
    "v_mfma_f32_16x16x32_bf16 v[28:31], a[36:39], a[64:67], v[28:31]\n\t"
    "global_load_dwordx4 a[20:23], v4, s[36:37] offset:0 sc1\n\t"
    "v_mfma_f32_16x16x32_bf16 v[32:35], a[36:39], a[68:71], v[32:35]\n\t"
    "v_mfma_f32_16x16x32_bf16 v[36:39], a[40:43], a[56:59], v[36:39]\n\t"
    "global_load_dwordx4 a[24:27], v4, s[36:37] offset:1024 sc1\n\t"
    "v_mfma_f32_16x16x32_bf16 v[40:43], a[40:43], a[60:63], v[40:43]\n\t"
    "v_mfma_f32_16x16x32_bf16 v[44:47], a[40:43], a[64:67], v[44:47]\n\t"
    "global_load_dwordx4 a[28:31], v4, s[36:37] offset:2048 sc1\n\t"
    "v_mfma_f32_16x16x32_bf16 v[48:51], a[40:43], a[68:71], v[48:51]\n\t"
    "v_mfma_f32_16x16x32_bf16 v[52:55], a[44:47], a[56:59], v[52:55]\n\t"
    "global_load_dwordx4 a[32:35], v4, s[36:37] offset:3072 sc1\n\t"
    "v_mfma_f32_16x16x32_bf16 v[56:59], a[44:47], a[60:63], v[56:59]\n\t"
    "v_mfma_f32_16x16x32_bf16 v[60:63], a[44:47], a[64:67], v[60:63]\n\t"
    "global_load_dwordx4 a[4:7], v5, s[38:39] offset:-1024 sc1\n\t"
    "v_mfma_f32_16x16x32_bf16 v[64:67], a[44:47], a[68:71], v[64:67]\n\t"
    "v_mfma_f32_16x16x32_bf16 v[68:71], a[48:51], a[56:59], v[68:71]\n\t"
    "global_load_dwordx4 a[8:11], v5, s[38:39] offset:0 sc1\n\t"
    "v_mfma_f32_16x16x32_bf16 v[72:75], a[48:51], a[60:63], v[72:75]\n\t"
    "v_mfma_f32_16x16x32_bf16 v[76:79], a[48:51], a[64:67], v[76:79]\n\t"
    "global_load_dwordx4 a[12:15], v5, s[38:39] offset:1024 sc1\n\t"
    "v_mfma_f32_16x16x32_bf16 v[80:83], a[48:51], a[68:71], v[80:83]\n\t"
    "v_mfma_f32_16x16x32_bf16 v[84:87], a[52:55], a[56:59], v[84:87]\n\t"
    "global_load_dwordx4 a[16:19], v5, s[38:39] offset:2048 sc1\n\t"
    "v_mfma_f32_16x16x32_bf16 v[88:91], a[52:55], a[60:63], v[88:91]\n\t"
    "v_mfma_f32_16x16x32_bf16 v[92:95], a[52:55], a[64:67], v[92:95]\n\t"
    "v_mfma_f32_16x16x32_bf16 v[96:99], a[52:55], a[68:71], v[96:99]\n\t"
    "s_add_u32 s58, s58, 4096\n\t"
    "s_cmp_eq_u32 s58, 131072\n\t"
    "s_cselect_b32 s58, 0, s58\n\t"
    "v_add_u32 v4, s58, v1\n\t"
    "s_add_u32 s59, s59, 5120\n\t"
    "s_cmp_eq_u32 s59, 163840\n\t"
    "s_cselect_b32 s59, 0, s59\n\t"
    "v_add_u32 v5, s59, v1\n\t"
    "s_waitcnt vmcnt(26)\n\t"
    "v_mfma_f32_16x16x32_bf16 v[20:23], a[72:75], a[92:95], v[20:23]\n\t"
    "global_load_dwordx4 a[36:39], v5, s[38:39] offset:-2048 sc1\n\t"
    "v_mfma_f32_16x16x32_bf16 v[24:27], a[72:75], a[96:99], v[24:27]\n\t"
    "v_mfma_f32_16x16x32_bf16 v[28:31], a[72:75], a[100:103], v[28:31]\n\t"
    "global_load_dwordx4 a[56:59], v4, s[36:37] offset:0 sc1\n\t"
    "v_mfma_f32_16x16x32_bf16 v[32:35], a[72:75], a[104:107], v[32:35]\n\t"
    "v_mfma_f32_16x16x32_bf16 v[36:39], a[76:79], a[92:95], v[36:39]\n\t"
    "global_load_dwordx4 a[60:63], v4, s[36:37] offset:1024 sc1\n\t"
    "v_mfma_f32_16x16x32_bf16 v[40:43], a[76:79], a[96:99], v[40:43]\n\t"
    "v_mfma_f32_16x16x32_bf16 v[44:47], a[76:79], a[100:103], v[44:47]\n\t"
    "global_load_dwordx4 a[64:67], v4, s[36:37] offset:2048 sc1\n\t"
    "v_mfma_f32_16x16x32_bf16 v[48:51], a[76:79], a[104:107], v[48:51]\n\t"
    "v_mfma_f32_16x16x32_bf16 v[52:55], a[80:83], a[92:95], v[52:55]\n\t"
    "global_load_dwordx4 a[68:71], v4, s[36:37] offset:3072 sc1\n\t"
    "v_mfma_f32_16x16x32_bf16 v[56:59], a[80:83], a[96:99], v[56:59]\n\t"
    "v_mfma_f32_16x16x32_bf16 v[60:63], a[80:83], a[100:103], v[60:63]\n\t"
    "global_load_dwordx4 a[40:43], v5, s[38:39] offset:-1024 sc1\n\t"
    "v_mfma_f32_16x16x32_bf16 v[64:67], a[80:83], a[104:107], v[64:67]\n\t"
    "v_mfma_f32_16x16x32_bf16 v[68:71], a[84:87], a[92:95], v[68:71]\n\t"
    "global_load_dwordx4 a[44:47], v5, s[38:39] offset:0 sc1\n\t"
    "v_mfma_f32_16x16x32_bf16 v[72:75], a[84:87], a[96:99], v[72:75]\n\t"
    "v_mfma_f32_16x16x32_bf16 v[76:79], a[84:87], a[100:103], v[76:79]\n\t"
    "global_load_dwordx4 a[48:51], v5, s[38:39] offset:1024 sc1\n\t"
    "v_mfma_f32_16x16x32_bf16 v[80:83], a[84:87], a[104:107], v[80:83]\n\t"
    "v_mfma_f32_16x16x32_bf16 v[84:87], a[88:91], a[92:95], v[84:87]\n\t"
    "global_load_dwordx4 a[52:55], v5, s[38:39] offset:2048 sc1\n\t"
    "v_mfma_f32_16x16x32_bf16 v[88:91], a[88:91], a[96:99], v[88:91]\n\t"
    "v_mfma_f32_16x16x32_bf16 v[92:95], a[88:91], a[100:103], v[92:95]\n\t"
    "v_mfma_f32_16x16x32_bf16 v[96:99], a[88:91], a[104:107], v[96:99]\n\t"
    "s_add_u32 s58, s58, 4096\n\t"
    "s_cmp_eq_u32 s58, 131072\n\t"
    "s_cselect_b32 s58, 0, s58\n\t"
    "v_add_u32 v4, s58, v1\n\t"
    "s_add_u32 s59, s59, 5120\n\t"
    "s_cmp_eq_u32 s59, 163840\n\t"
    "s_cselect_b32 s59, 0, s59\n\t"
    "v_add_u32 v5, s59, v1\n\t"
    "s_waitcnt vmcnt(31)\n\t"
    "v_mfma_f32_16x16x32_bf16 v[20:23], a[108:111], a[128:131], v[20:23]\n\t"
    "global_load_dwordx4 a[72:75], v5, s[38:39] offset:-2048 sc1\n\t"
    "v_mfma_f32_16x16x32_bf16 v[24:27], a[108:111], a[132:135], v[24:27]\n\t"
    "v_mfma_f32_16x16x32_bf16 v[28:31], a[108:111], a[136:139], v[28:31]\n\t"
    "global_load_dwordx4 a[92:95], v4, s[36:37] offset:0 sc1\n\t"
    "v_mfma_f32_16x16x32_bf16 v[32:35], a[108:111], a[140:143], v[32:35]\n\t"
    "v_mfma_f32_16x16x32_bf16 v[36:39], a[112:115], a[128:131], v[36:39]\n\t"
    "global_load_dwordx4 a[96:99], v4, s[36:37] offset:1024 sc1\n\t"
    "v_mfma_f32_16x16x32_bf16 v[40:43], a[112:115], a[132:135], v[40:43]\n\t"
    "v_mfma_f32_16x16x32_bf16 v[44:47], a[112:115], a[136:139], v[44:47]\n\t"
    "global_load_dwordx4 a[100:103], v4, s[36:37] offset:2048 sc1\n\t"
    "v_mfma_f32_16x16x32_bf16 v[48:51], a[112:115], a[140:143], v[48:51]\n\t"
    "v_mfma_f32_16x16x32_bf16 v[52:55], a[116:119], a[128:131], v[52:55]\n\t"
    "global_load_dwordx4 a[104:107], v4, s[36:37] offset:3072 sc1\n\t"
    "v_mfma_f32_16x16x32_bf16 v[56:59], a[116:119], a[132:135], v[56:59]\n\t"
    "v_mfma_f32_16x16x32_bf16 v[60:63], a[116:119], a[136:139], v[60:63]\n\t"
    "global_load_dwordx4 a[76:79], v5, s[38:39] offset:-1024 sc1\n\t"
    "v_mfma_f32_16x16x32_bf16 v[64:67], a[116:119], a[140:143], v[64:67]\n\t"
    "v_mfma_f32_16x16x32_bf16 v[68:71], a[120:123], a[128:131], v[68:71]\n\t"
    "global_load_dwordx4 a[80:83], v5, s[38:39] offset:0 sc1\n\t"
    "v_mfma_f32_16x16x32_bf16 v[72:75], a[120:123], a[132:135], v[72:75]\n\t"
    "v_mfma_f32_16x16x32_bf16 v[76:79], a[120:123], a[136:139], v[76:79]\n\t"
    "global_load_dwordx4 a[84:87], v5, s[38:39] offset:1024 sc1\n\t"
    "v_mfma_f32_16x16x32_bf16 v[80:83], a[120:123], a[140:143], v[80:83]\n\t"
    "v_mfma_f32_16x16x32_bf16 v[84:87], a[124:127], a[128:131], v[84:87]\n\t"
    "global_load_dwordx4 a[88:91], v5, s[38:39] offset:2048 sc1\n\t"
    "v_mfma_f32_16x16x32_bf16 v[88:91], a[124:127], a[132:135], v[88:91]\n\t"
    "v_mfma_f32_16x16x32_bf16 v[92:95], a[124:127], a[136:139], v[92:95]\n\t"
    "v_mfma_f32_16x16x32_bf16 v[96:99], a[124:127], a[140:143], v[96:99]\n\t"
    "s_add_u32 s58, s58, 4096\n\t"
    "s_cmp_eq_u32 s58, 131072\n\t"
    "s_cselect_b32 s58, 0, s58\n\t"
    "v_add_u32 v4, s58, v1\n\t"
    "s_add_u32 s59, s59, 5120\n\t"
    "s_cmp_eq_u32 s59, 163840\n\t"
    "s_cselect_b32 s59, 0, s59\n\t"
    "v_add_u32 v5, s59, v1\n\t"
    "s_waitcnt vmcnt(36)\n\t"
    "v_mfma_f32_16x16x32_bf16 v[20:23], a[144:147], a[164:167], v[20:23]\n\t"
    "global_load_dwordx4 a[108:111], v5, s[38:39] offset:-2048 sc1\n\t"
    "v_mfma_f32_16x16x32_bf16 v[24:27], a[144:147], a[168:171], v[24:27]\n\t"
    "v_mfma_f32_16x16x32_bf16 v[28:31], a[144:147], a[172:175], v[28:31]\n\t"
    "global_load_dwordx4 a[128:131], v4, s[36:37] offset:0 sc1\n\t"
    "v_mfma_f32_16x16x32_bf16 v[32:35], a[144:147], a[176:179], v[32:35]\n\t"
    "v_mfma_f32_16x16x32_bf16 v[36:39], a[148:151], a[164:167], v[36:39]\n\t"
    "global_load_dwordx4 a[132:135], v4, s[36:37] offset:1024 sc1\n\t"
    "v_mfma_f32_16x16x32_bf16 v[40:43], a[148:151], a[168:171], v[40:43]\n\t"
    "v_mfma_f32_16x16x32_bf16 v[44:47], a[148:151], a[172:175], v[44:47]\n\t"
    "global_load_dwordx4 a[136:139], v4, s[36:37] offset:2048 sc1\n\t"
    "v_mfma_f32_16x16x32_bf16 v[48:51], a[148:151], a[176:179], v[48:51]\n\t"
    "v_mfma_f32_16x16x32_bf16 v[52:55], a[152:155], a[164:167], v[52:55]\n\t"
    "global_load_dwordx4 a[140:143], v4, s[36:37] offset:3072 sc1\n\t"
    "v_mfma_f32_16x16x32_bf16 v[56:59], a[152:155], a[168:171], v[56:59]\n\t"
    "v_mfma_f32_16x16x32_bf16 v[60:63], a[152:155], a[172:175], v[60:63]\n\t"
    "global_load_dwordx4 a[112:115], v5, s[38:39] offset:-1024 sc1\n\t"
    "v_mfma_f32_16x16x32_bf16 v[64:67], a[152:155], a[176:179], v[64:67]\n\t"
    "v_mfma_f32_16x16x32_bf16 v[68:71], a[156:159], a[164:167], v[68:71]\n\t"
    "global_load_dwordx4 a[116:119], v5, s[38:39] offset:0 sc1\n\t"
    "v_mfma_f32_16x16x32_bf16 v[72:75], a[156:159], a[168:171], v[72:75]\n\t"
    "v_mfma_f32_16x16x32_bf16 v[76:79], a[156:159], a[172:175], v[76:79]\n\t"
    "global_load_dwordx4 a[120:123], v5, s[38:39] offset:1024 sc1\n\t"
    "v_mfma_f32_16x16x32_bf16 v[80:83], a[156:159], a[176:179], v[80:83]\n\t"
    "v_mfma_f32_16x16x32_bf16 v[84:87], a[160:163], a[164:167], v[84:87]\n\t"
    "global_load_dwordx4 a[124:127], v5, s[38:39] offset:2048 sc1\n\t"
    "v_mfma_f32_16x16x32_bf16 v[88:91], a[160:163], a[168:171], v[88:91]\n\t"
    "v_mfma_f32_16x16x32_bf16 v[92:95], a[160:163], a[172:175], v[92:95]\n\t"
    "v_mfma_f32_16x16x32_bf16 v[96:99], a[160:163], a[176:179], v[96:99]\n\t"
    "s_add_u32 s58, s58, 4096\n\t"
    "s_cmp_eq_u32 s58, 131072\n\t"
    "s_cselect_b32 s58, 0, s58\n\t"
    "v_add_u32 v4, s58, v1\n\t"
    "s_add_u32 s59, s59, 5120\n\t"
    "s_cmp_eq_u32 s59, 163840\n\t"
    "s_cselect_b32 s59, 0, s59\n\t"
    "v_add_u32 v5, s59, v1\n\t"
    "s_waitcnt vmcnt(40)\n\t"
    "v_mfma_f32_16x16x32_bf16 v[20:23], a[180:183], a[200:203], v[20:23]\n\t"
    "global_load_dwordx4 a[144:147], v5, s[38:39] offset:-2048 sc1\n\t"
    "v_mfma_f32_16x16x32_bf16 v[24:27], a[180:183], a[204:207], v[24:27]\n\t"
    "v_mfma_f32_16x16x32_bf16 v[28:31], a[180:183], a[208:211], v[28:31]\n\t"
    "global_load_dwordx4 a[164:167], v4, s[36:37] offset:0 sc1\n\t"
    "v_mfma_f32_16x16x32_bf16 v[32:35], a[180:183], a[212:215], v[32:35]\n\t"
    "s_waitcnt vmcnt(38)\n\t"
    "v_mfma_f32_16x16x32_bf16 v[36:39], a[184:187], a[200:203], v[36:39]\n\t"
    "global_load_dwordx4 a[168:171], v4, s[36:37] offset:1024 sc1\n\t"
    "v_mfma_f32_16x16x32_bf16 v[40:43], a[184:187], a[204:207], v[40:43]\n\t"
    "v_mfma_f32_16x16x32_bf16 v[44:47], a[184:187], a[208:211], v[44:47]\n\t"
    "global_load_dwordx4 a[172:175], v4, s[36:37] offset:2048 sc1\n\t"
    "v_mfma_f32_16x16x32_bf16 v[48:51], a[184:187], a[212:215], v[48:51]\n\t"
    "v_mfma_f32_16x16x32_bf16 v[52:55], a[188:191], a[200:203], v[52:55]\n\t"
    "global_load_dwordx4 a[176:179], v4, s[36:37] offset:3072 sc1\n\t"
    "v_mfma_f32_16x16x32_bf16 v[56:59], a[188:191], a[204:207], v[56:59]\n\t"
    "v_mfma_f32_16x16x32_bf16 v[60:63], a[188:191], a[208:211], v[60:63]\n\t"
    "global_load_dwordx4 a[148:151], v5, s[38:39] offset:-1024 sc1\n\t"
    "v_mfma_f32_16x16x32_bf16 v[64:67], a[188:191], a[212:215], v[64:67]\n\t"
    "v_mfma_f32_16x16x32_bf16 v[68:71], a[192:195], a[200:203], v[68:71]\n\t"
    "global_load_dwordx4 a[152:155], v5, s[38:39] offset:0 sc1\n\t"
    "v_mfma_f32_16x16x32_bf16 v[72:75], a[192:195], a[204:207], v[72:75]\n\t"
    "v_mfma_f32_16x16x32_bf16 v[76:79], a[192:195], a[208:211], v[76:79]\n\t"
    "global_load_dwordx4 a[156:159], v5, s[38:39] offset:1024 sc1\n\t"
    "v_mfma_f32_16x16x32_bf16 v[80:83], a[192:195], a[212:215], v[80:83]\n\t"
    "v_mfma_f32_16x16x32_bf16 v[84:87], a[196:199], a[200:203], v[84:87]\n\t"
    "global_load_dwordx4 a[160:163], v5, s[38:39] offset:2048 sc1\n\t"
    "v_mfma_f32_16x16x32_bf16 v[88:91], a[196:199], a[204:207], v[88:91]\n\t"
    "v_mfma_f32_16x16x32_bf16 v[92:95], a[196:199], a[208:211], v[92:95]\n\t"
    "v_mfma_f32_16x16x32_bf16 v[96:99], a[196:199], a[212:215], v[96:99]\n\t"
    "s_add_u32 s58, s58, 4096\n\t"
    "s_cmp_eq_u32 s58, 131072\n\t"
    "s_cselect_b32 s58, 0, s58\n\t"
    "v_add_u32 v4, s58, v1\n\t"
    "s_add_u32 s59, s59, 5120\n\t"
    "s_cmp_eq_u32 s59, 163840\n\t"
    "s_cselect_b32 s59, 0, s59\n\t"
    "v_add_u32 v5, s59, v1\n\t"
    "s_sub_u32 s52, s52, 1\n\t"
    "s_cmp_lg_u32 s52, 0\n\t"
    "s_cbranch_scc1 LM_18_%=\n\t"
    "s_waitcnt vmcnt(40)\n\t"
    "v_mfma_f32_16x16x32_bf16 v[20:23], a[0:3], a[20:23], v[20:23]\n\t"
    "global_load_dwordx4 a[180:183], v5, s[38:39] offset:-2048 sc1\n\t"
    "v_mfma_f32_16x16x32_bf16 v[24:27], a[0:3], a[24:27], v[24:27]\n\t"
    "v_mfma_f32_16x16x32_bf16 v[28:31], a[0:3], a[28:31], v[28:31]\n\t"
    "global_load_dwordx4 a[200:203], v4, s[36:37] offset:0 sc1\n\t"
    "v_mfma_f32_16x16x32_bf16 v[32:35], a[0:3], a[32:35], v[32:35]\n\t"
    "s_waitcnt vmcnt(38)\n\t"
    "v_mfma_f32_16x16x32_bf16 v[36:39], a[4:7], a[20:23], v[36:39]\n\t"
    "global_load_dwordx4 a[204:207], v4, s[36:37] offset:1024 sc1\n\t"
    "v_mfma_f32_16x16x32_bf16 v[40:43], a[4:7], a[24:27], v[40:43]\n\t"
    "v_mfma_f32_16x16x32_bf16 v[44:47], a[4:7], a[28:31], v[44:47]\n\t"
    "global_load_dwordx4 a[208:211], v4, s[36:37] offset:2048 sc1\n\t"
    "v_mfma_f32_16x16x32_bf16 v[48:51], a[4:7], a[32:35], v[48:51]\n\t"
    "v_mfma_f32_16x16x32_bf16 v[52:55], a[8:11], a[20:23], v[52:55]\n\t"
    "global_load_dwordx4 a[212:215], v4, s[36:37] offset:3072 sc1\n\t"
    "v_mfma_f32_16x16x32_bf16 v[56:59], a[8:11], a[24:27], v[56:59]\n\t"
    "v_mfma_f32_16x16x32_bf16 v[60:63], a[8:11], a[28:31], v[60:63]\n\t"
    "global_load_dwordx4 a[184:187], v5, s[38:39] offset:-1024 sc1\n\t"
    "v_mfma_f32_16x16x32_bf16 v[64:67], a[8:11], a[32:35], v[64:67]\n\t"
    "v_mfma_f32_16x16x32_bf16 v[68:71], a[12:15], a[20:23], v[68:71]\n\t"
    "global_load_dwordx4 a[188:191], v5, s[38:39] offset:0 sc1\n\t"
    "v_mfma_f32_16x16x32_bf16 v[72:75], a[12:15], a[24:27], v[72:75]\n\t"
    "v_mfma_f32_16x16x32_bf16 v[76:79], a[12:15], a[28:31], v[76:79]\n\t"
    "global_load_dwordx4 a[192:195], v5, s[38:39] offset:1024 sc1\n\t"
    "v_mfma_f32_16x16x32_bf16 v[80:83], a[12:15], a[32:35], v[80:83]\n\t"
    "v_mfma_f32_16x16x32_bf16 v[84:87], a[16:19], a[20:23], v[84:87]\n\t"
    "global_load_dwordx4 a[196:199], v5, s[38:39] offset:2048 sc1\n\t"
    "v_mfma_f32_16x16x32_bf16 v[88:91], a[16:19], a[24:27], v[88:91]\n\t"
    "v_mfma_f32_16x16x32_bf16 v[92:95], a[16:19], a[28:31], v[92:95]\n\t"
    "v_mfma_f32_16x16x32_bf16 v[96:99], a[16:19], a[32:35], v[96:99]\n\t"
    "s_add_u32 s58, s58, 4096\n\t"
    "s_cmp_eq_u32 s58, 131072\n\t"
    "s_cselect_b32 s58, 0, s58\n\t"
    "v_add_u32 v4, s58, v1\n\t"
    "s_add_u32 s59, s59, 5120\n\t"
    "s_cmp_eq_u32 s59, 163840\n\t"
    "s_cselect_b32 s59, 0, s59\n\t"
    "v_add_u32 v5, s59, v1\n\t"
    "s_waitcnt vmcnt(40)\n\t"
    "v_mfma_f32_16x16x32_bf16 v[20:23], a[36:39], a[56:59], v[20:23]\n\t"
    "global_load_dwordx4 a[0:3], v5, s[38:39] offset:-2048 sc1\n\t"
    "v_mfma_f32_16x16x32_bf16 v[24:27], a[36:39], a[60:63], v[24:27]\n\t"
    "v_mfma_f32_16x16x32_bf16 v[28:31], a[36:39], a[64:67], v[28:31]\n\t"
    "global_load_dwordx4 a[20:23], v4, s[36:37] offset:0 sc1\n\t"
    "v_mfma_f32_16x16x32_bf16 v[32:35], a[36:39], a[68:71], v[32:35]\n\t"
    "s_waitcnt vmcnt(38)\n\t"
    "v_mfma_f32_16x16x32_bf16 v[36:39], a[40:43], a[56:59], v[36:39]\n\t"
    "global_load_dwordx4 a[24:27], v4, s[36:37] offset:1024 sc1\n\t"
    "v_mfma_f32_16x16x32_bf16 v[40:43], a[40:43], a[60:63], v[40:43]\n\t"
    "v_mfma_f32_16x16x32_bf16 v[44:47], a[40:43], a[64:67], v[44:47]\n\t"
    "global_load_dwordx4 a[28:31], v4, s[36:37] offset:2048 sc1\n\t"
    "v_mfma_f32_16x16x32_bf16 v[48:51], a[40:43], a[68:71], v[48:51]\n\t"
    "v_mfma_f32_16x16x32_bf16 v[52:55], a[44:47], a[56:59], v[52:55]\n\t"
    "global_load_dwordx4 a[32:35], v4, s[36:37] offset:3072 sc1\n\t"
    "v_mfma_f32_16x16x32_bf16 v[56:59], a[44:47], a[60:63], v[56:59]\n\t"
    "v_mfma_f32_16x16x32_bf16 v[60:63], a[44:47], a[64:67], v[60:63]\n\t"
    "global_load_dwordx4 a[4:7], v5, s[38:39] offset:-1024 sc1\n\t"
    "v_mfma_f32_16x16x32_bf16 v[64:67], a[44:47], a[68:71], v[64:67]\n\t"
    "v_mfma_f32_16x16x32_bf16 v[68:71], a[48:51], a[56:59], v[68:71]\n\t"
    "global_load_dwordx4 a[8:11], v5, s[38:39] offset:0 sc1\n\t"
    "v_mfma_f32_16x16x32_bf16 v[72:75], a[48:51], a[60:63], v[72:75]\n\t"
    "v_mfma_f32_16x16x32_bf16 v[76:79], a[48:51], a[64:67], v[76:79]\n\t"
    "global_load_dwordx4 a[12:15], v5, s[38:39] offset:1024 sc1\n\t"
    "v_mfma_f32_16x16x32_bf16 v[80:83], a[48:51], a[68:71], v[80:83]\n\t"
    "v_mfma_f32_16x16x32_bf16 v[84:87], a[52:55], a[56:59], v[84:87]\n\t"
    "global_load_dwordx4 a[16:19], v5, s[38:39] offset:2048 sc1\n\t"
    "v_mfma_f32_16x16x32_bf16 v[88:91], a[52:55], a[60:63], v[88:91]\n\t"
    "v_mfma_f32_16x16x32_bf16 v[92:95], a[52:55], a[64:67], v[92:95]\n\t"
    "v_mfma_f32_16x16x32_bf16 v[96:99], a[52:55], a[68:71], v[96:99]\n\t"
    "s_add_u32 s58, s58, 4096\n\t"
    "s_cmp_eq_u32 s58, 131072\n\t"
    "s_cselect_b32 s58, 0, s58\n\t"
    "v_add_u32 v4, s58, v1\n\t"
    "s_add_u32 s59, s59, 5120\n\t"
    "s_cmp_eq_u32 s59, 163840\n\t"
    "s_cselect_b32 s59, 0, s59\n\t"
    "v_add_u32 v5, s59, v1\n\t"
    "s_waitcnt vmcnt(40)\n\t"
    "v_mfma_f32_16x16x32_bf16 v[20:23], a[72:75], a[92:95], v[20:23]\n\t"
    "global_load_dwordx4 a[36:39], v5, s[38:39] offset:-2048 sc1\n\t"
    "v_mfma_f32_16x16x32_bf16 v[24:27], a[72:75], a[96:99], v[24:27]\n\t"
    "v_mfma_f32_16x16x32_bf16 v[28:31], a[72:75], a[100:103], v[28:31]\n\t"
    "global_load_dwordx4 a[56:59], v4, s[36:37] offset:0 sc1\n\t"
    "v_mfma_f32_16x16x32_bf16 v[32:35], a[72:75], a[104:107], v[32:35]\n\t"
    "s_waitcnt vmcnt(38)\n\t"
    "v_mfma_f32_16x16x32_bf16 v[36:39], a[76:79], a[92:95], v[36:39]\n\t"
    "global_load_dwordx4 a[60:63], v4, s[36:37] offset:1024 sc1\n\t"
    "v_mfma_f32_16x16x32_bf16 v[40:43], a[76:79], a[96:99], v[40:43]\n\t"
    "v_mfma_f32_16x16x32_bf16 v[44:47], a[76:79], a[100:103], v[44:47]\n\t"
    "global_load_dwordx4 a[64:67], v4, s[36:37] offset:2048 sc1\n\t"
    "v_mfma_f32_16x16x32_bf16 v[48:51], a[76:79], a[104:107], v[48:51]\n\t"
    "v_mfma_f32_16x16x32_bf16 v[52:55], a[80:83], a[92:95], v[52:55]\n\t"
    "global_load_dwordx4 a[68:71], v4, s[36:37] offset:3072 sc1\n\t"
    "v_mfma_f32_16x16x32_bf16 v[56:59], a[80:83], a[96:99], v[56:59]\n\t"
    "v_mfma_f32_16x16x32_bf16 v[60:63], a[80:83], a[100:103], v[60:63]\n\t"
    "global_load_dwordx4 a[40:43], v5, s[38:39] offset:-1024 sc1\n\t"
    "v_mfma_f32_16x16x32_bf16 v[64:67], a[80:83], a[104:107], v[64:67]\n\t"
    "v_mfma_f32_16x16x32_bf16 v[68:71], a[84:87], a[92:95], v[68:71]\n\t"
    "global_load_dwordx4 a[44:47], v5, s[38:39] offset:0 sc1\n\t"
    "v_mfma_f32_16x16x32_bf16 v[72:75], a[84:87], a[96:99], v[72:75]\n\t"
    "v_mfma_f32_16x16x32_bf16 v[76:79], a[84:87], a[100:103], v[76:79]\n\t"
    "global_load_dwordx4 a[48:51], v5, s[38:39] offset:1024 sc1\n\t"
    "v_mfma_f32_16x16x32_bf16 v[80:83], a[84:87], a[104:107], v[80:83]\n\t"
    "v_mfma_f32_16x16x32_bf16 v[84:87], a[88:91], a[92:95], v[84:87]\n\t"
    "global_load_dwordx4 a[52:55], v5, s[38:39] offset:2048 sc1\n\t"
    "v_mfma_f32_16x16x32_bf16 v[88:91], a[88:91], a[96:99], v[88:91]\n\t"
    "v_mfma_f32_16x16x32_bf16 v[92:95], a[88:91], a[100:103], v[92:95]\n\t"
    "v_mfma_f32_16x16x32_bf16 v[96:99], a[88:91], a[104:107], v[96:99]\n\t"
    "s_add_u32 s58, s58, 4096\n\t"
    "s_cmp_eq_u32 s58, 131072\n\t"
    "s_cselect_b32 s58, 0, s58\n\t"
    "v_add_u32 v4, s58, v1\n\t"
    "s_add_u32 s59, s59, 5120\n\t"
    "s_cmp_eq_u32 s59, 163840\n\t"
    "s_cselect_b32 s59, 0, s59\n\t"
    "v_add_u32 v5, s59, v1\n\t"
    "s_waitcnt vmcnt(40)\n\t"
    "v_mfma_f32_16x16x32_bf16 v[20:23], a[108:111], a[128:131], v[20:23]\n\t"
    "global_load_dwordx2 v[100:101], v10, s[40:41] offset:0 sc1\n\t"
    "v_mfma_f32_16x16x32_bf16 v[24:27], a[108:111], a[132:135], v[24:27]\n\t"
    "v_mfma_f32_16x16x32_bf16 v[28:31], a[108:111], a[136:139], v[28:31]\n\t"
    "global_load_dwordx2 v[102:103], v10, s[40:41] offset:32 sc1\n\t"
    "v_mfma_f32_16x16x32_bf16 v[32:35], a[108:111], a[140:143], v[32:35]\n\t"
    "s_waitcnt vmcnt(38)\n\t"
    "v_mfma_f32_16x16x32_bf16 v[36:39], a[112:115], a[128:131], v[36:39]\n\t"
    "global_load_dwordx4 v[112:115], v12, s[42:43] offset:0 sc1\n\t"
    "v_mfma_f32_16x16x32_bf16 v[40:43], a[112:115], a[132:135], v[40:43]\n\t"
    "v_mfma_f32_16x16x32_bf16 v[44:47], a[112:115], a[136:139], v[44:47]\n\t"
    "global_load_dwordx4 v[116:119], v12, s[42:43] offset:64 sc1\n\t"
    "v_mfma_f32_16x16x32_bf16 v[48:51], a[112:115], a[140:143], v[48:51]\n\t"
    "v_mfma_f32_16x16x32_bf16 v[52:55], a[116:119], a[128:131], v[52:55]\n\t"
    "global_load_dwordx4 v[132:135], v2, s[42:43] offset:0 sc1\n\t"
    "v_mfma_f32_16x16x32_bf16 v[56:59], a[116:119], a[132:135], v[56:59]\n\t"
    "v_mfma_f32_16x16x32_bf16 v[60:63], a[116:119], a[136:139], v[60:63]\n\t"
    "global_load_dwordx4 v[136:139], v2, s[42:43] offset:64 sc1\n\t"
    "v_mfma_f32_16x16x32_bf16 v[64:67], a[116:119], a[140:143], v[64:67]\n\t"
    "v_mfma_f32_16x16x32_bf16 v[68:71], a[120:123], a[128:131], v[68:71]\n\t"
    "global_load_dwordx2 v[104:105], v10, s[40:41] offset:64 sc1\n\t"
    "v_mfma_f32_16x16x32_bf16 v[72:75], a[120:123], a[132:135], v[72:75]\n\t"
    "v_mfma_f32_16x16x32_bf16 v[76:79], a[120:123], a[136:139], v[76:79]\n\t"
    "global_load_dwordx2 v[106:107], v10, s[40:41] offset:96 sc1\n\t"
    "v_mfma_f32_16x16x32_bf16 v[80:83], a[120:123], a[140:143], v[80:83]\n\t"
    "v_mfma_f32_16x16x32_bf16 v[84:87], a[124:127], a[128:131], v[84:87]\n\t"
    "v_mfma_f32_16x16x32_bf16 v[88:91], a[124:127], a[132:135], v[88:91]\n\t"
    "v_mfma_f32_16x16x32_bf16 v[92:95], a[124:127], a[136:139], v[92:95]\n\t"
    "v_mfma_f32_16x16x32_bf16 v[96:99], a[124:127], a[140:143], v[96:99]\n\t"
    "s_waitcnt vmcnt(39)\n\t"
    "v_mfma_f32_16x16x32_bf16 v[20:23], a[144:147], a[164:167], v[20:23]\n\t"
    "global_load_dwordx4 v[120:123], v12, s[42:43] offset:128 sc1\n\t"
    "v_mfma_f32_16x16x32_bf16 v[24:27], a[144:147], a[168:171], v[24:27]\n\t"
    "v_mfma_f32_16x16x32_bf16 v[28:31], a[144:147], a[172:175], v[28:31]\n\t"
    "global_load_dwordx4 v[124:127], v12, s[42:43] offset:192 sc1\n\t"
    "v_mfma_f32_16x16x32_bf16 v[32:35], a[144:147], a[176:179], v[32:35]\n\t"
    "s_waitcnt vmcnt(37)\n\t"
    "v_mfma_f32_16x16x32_bf16 v[36:39], a[148:151], a[164:167], v[36:39]\n\t"
    "global_load_dwordx4 v[140:143], v2, s[42:43] offset:128 sc1\n\t"
    "v_mfma_f32_16x16x32_bf16 v[40:43], a[148:151], a[168:171], v[40:43]\n\t"
    "v_mfma_f32_16x16x32_bf16 v[44:47], a[148:151], a[172:175], v[44:47]\n\t"
    "global_load_dwordx4 v[144:147], v2, s[42:43] offset:192 sc1\n\t"
    "v_mfma_f32_16x16x32_bf16 v[48:51], a[148:151], a[176:179], v[48:51]\n\t"
    "v_mfma_f32_16x16x32_bf16 v[52:55], a[152:155], a[164:167], v[52:55]\n\t"
    "global_load_dwordx2 v[108:109], v11, s[40:41] sc1\n\t"
    "v_mfma_f32_16x16x32_bf16 v[56:59], a[152:155], a[168:171], v[56:59]\n\t"
    "v_mfma_f32_16x16x32_bf16 v[60:63], a[152:155], a[172:175], v[60:63]\n\t"
    "global_load_dwordx4 v[128:131], v13, s[42:43] sc1\n\t"
    "v_mfma_f32_16x16x32_bf16 v[64:67], a[152:155], a[176:179], v[64:67]\n\t"
    "v_mfma_f32_16x16x32_bf16 v[68:71], a[156:159], a[164:167], v[68:71]\n\t"
    "global_load_dwordx4 v[148:151], v3, s[42:43] sc1\n\t"
    "v_mfma_f32_16x16x32_bf16 v[72:75], a[156:159], a[168:171], v[72:75]\n\t"
    "v_mfma_f32_16x16x32_bf16 v[76:79], a[156:159], a[172:175], v[76:79]\n\t"
    "v_mfma_f32_16x16x32_bf16 v[80:83], a[156:159], a[176:179], v[80:83]\n\t"
    "v_mfma_f32_16x16x32_bf16 v[84:87], a[160:163], a[164:167], v[84:87]\n\t"
    "v_mfma_f32_16x16x32_bf16 v[88:91], a[160:163], a[168:171], v[88:91]\n\t"
    "v_mfma_f32_16x16x32_bf16 v[92:95], a[160:163], a[172:175], v[92:95]\n\t"
    "v_mfma_f32_16x16x32_bf16 v[96:99], a[160:163], a[176:179], v[96:99]\n\t"
    "s_waitcnt vmcnt(37)\n\t"
    "v_mfma_f32_16x16x32_bf16 v[20:23], a[180:183], a[200:203], v[20:23]\n\t"
    "v_mfma_f32_16x16x32_bf16 v[24:27], a[180:183], a[204:207], v[24:27]\n\t"
    "v_mfma_f32_16x16x32_bf16 v[28:31], a[180:183], a[208:211], v[28:31]\n\t"
    "v_mfma_f32_16x16x32_bf16 v[32:35], a[180:183], a[212:215], v[32:35]\n\t"
    "s_waitcnt vmcnt(33)\n\t"
    "v_mfma_f32_16x16x32_bf16 v[36:39], a[184:187], a[200:203], v[36:39]\n\t"
    "v_mfma_f32_16x16x32_bf16 v[40:43], a[184:187], a[204:207], v[40:43]\n\t"
    "v_mfma_f32_16x16x32_bf16 v[44:47], a[184:187], a[208:211], v[44:47]\n\t"
    "v_mfma_f32_16x16x32_bf16 v[48:51], a[184:187], a[212:215], v[48:51]\n\t"
    "v_mfma_f32_16x16x32_bf16 v[52:55], a[188:191], a[200:203], v[52:55]\n\t"
    "v_mfma_f32_16x16x32_bf16 v[56:59], a[188:191], a[204:207], v[56:59]\n\t"
    "v_mfma_f32_16x16x32_bf16 v[60:63], a[188:191], a[208:211], v[60:63]\n\t"
    "v_mfma_f32_16x16x32_bf16 v[64:67], a[188:191], a[212:215], v[64:67]\n\t"
    "v_mfma_f32_16x16x32_bf16 v[68:71], a[192:195], a[200:203], v[68:71]\n\t"
    "v_mfma_f32_16x16x32_bf16 v[72:75], a[192:195], a[204:207], v[72:75]\n\t"
    "v_mfma_f32_16x16x32_bf16 v[76:79], a[192:195], a[208:211], v[76:79]\n\t"
    "v_mfma_f32_16x16x32_bf16 v[80:83], a[192:195], a[212:215], v[80:83]\n\t"
    "v_mfma_f32_16x16x32_bf16 v[84:87], a[196:199], a[200:203], v[84:87]\n\t"
    "v_mfma_f32_16x16x32_bf16 v[88:91], a[196:199], a[204:207], v[88:91]\n\t"
    "v_mfma_f32_16x16x32_bf16 v[92:95], a[196:199], a[208:211], v[92:95]\n\t"
    "v_mfma_f32_16x16x32_bf16 v[96:99], a[196:199], a[212:215], v[96:99]\n\t"
    "s_waitcnt vmcnt(28)\n\t"
    "v_mfma_f32_16x16x32_bf16 v[20:23], a[0:3], a[20:23], v[20:23]\n\t"
    "v_mfma_f32_16x16x32_bf16 v[24:27], a[0:3], a[24:27], v[24:27]\n\t"
    "v_mfma_f32_16x16x32_bf16 v[28:31], a[0:3], a[28:31], v[28:31]\n\t"
    "v_mfma_f32_16x16x32_bf16 v[32:35], a[0:3], a[32:35], v[32:35]\n\t"
    "s_waitcnt vmcnt(24)\n\t"
    "v_mfma_f32_16x16x32_bf16 v[36:39], a[4:7], a[20:23], v[36:39]\n\t"
    "v_mfma_f32_16x16x32_bf16 v[40:43], a[4:7], a[24:27], v[40:43]\n\t"
    "v_mfma_f32_16x16x32_bf16 v[44:47], a[4:7], a[28:31], v[44:47]\n\t"
    "v_mfma_f32_16x16x32_bf16 v[48:51], a[4:7], a[32:35], v[48:51]\n\t"
    "v_mfma_f32_16x16x32_bf16 v[52:55], a[8:11], a[20:23], v[52:55]\n\t"
    "v_mfma_f32_16x16x32_bf16 v[56:59], a[8:11], a[24:27], v[56:59]\n\t"
    "v_mfma_f32_16x16x32_bf16 v[60:63], a[8:11], a[28:31], v[60:63]\n\t"
    "v_mfma_f32_16x16x32_bf16 v[64:67], a[8:11], a[32:35], v[64:67]\n\t"
    "v_mfma_f32_16x16x32_bf16 v[68:71], a[12:15], a[20:23], v[68:71]\n\t"
    "v_mfma_f32_16x16x32_bf16 v[72:75], a[12:15], a[24:27], v[72:75]\n\t"
    "v_mfma_f32_16x16x32_bf16 v[76:79], a[12:15], a[28:31], v[76:79]\n\t"
    "v_mfma_f32_16x16x32_bf16 v[80:83], a[12:15], a[32:35], v[80:83]\n\t"
    "v_mfma_f32_16x16x32_bf16 v[84:87], a[16:19], a[20:23], v[84:87]\n\t"
    "v_mfma_f32_16x16x32_bf16 v[88:91], a[16:19], a[24:27], v[88:91]\n\t"
    "v_mfma_f32_16x16x32_bf16 v[92:95], a[16:19], a[28:31], v[92:95]\n\t"
    "v_mfma_f32_16x16x32_bf16 v[96:99], a[16:19], a[32:35], v[96:99]\n\t"
    "s_waitcnt vmcnt(19)\n\t"
    "v_mfma_f32_16x16x32_bf16 v[20:23], a[36:39], a[56:59], v[20:23]\n\t"
    "v_mfma_f32_16x16x32_bf16 v[24:27], a[36:39], a[60:63], v[24:27]\n\t"
    "v_mfma_f32_16x16x32_bf16 v[28:31], a[36:39], a[64:67], v[28:31]\n\t"
    "v_mfma_f32_16x16x32_bf16 v[32:35], a[36:39], a[68:71], v[32:35]\n\t"
    "s_waitcnt vmcnt(15)\n\t"
    "v_mfma_f32_16x16x32_bf16 v[36:39], a[40:43], a[56:59], v[36:39]\n\t"
    "v_mfma_f32_16x16x32_bf16 v[40:43], a[40:43], a[60:63], v[40:43]\n\t"
    "v_mfma_f32_16x16x32_bf16 v[44:47], a[40:43], a[64:67], v[44:47]\n\t"
    "v_mfma_f32_16x16x32_bf16 v[48:51], a[40:43], a[68:71], v[48:51]\n\t"
    "v_mfma_f32_16x16x32_bf16 v[52:55], a[44:47], a[56:59], v[52:55]\n\t"
    "ds_write_b128 v8, v[20:23] offset:0\n\t"
    "v_mfma_f32_16x16x32_bf16 v[56:59], a[44:47], a[60:63], v[56:59]\n\t"
    "ds_write_b128 v8, v[24:27] offset:1024\n\t"
    "v_mfma_f32_16x16x32_bf16 v[60:63], a[44:47], a[64:67], v[60:63]\n\t"
    "ds_write_b128 v8, v[28:31] offset:2048\n\t"
    "v_mfma_f32_16x16x32_bf16 v[64:67], a[44:47], a[68:71], v[64:67]\n\t"
    "ds_write_b128 v8, v[32:35] offset:3072\n\t"
    "v_mfma_f32_16x16x32_bf16 v[68:71], a[48:51], a[56:59], v[68:71]\n\t"
    "ds_write_b128 v8, v[36:39] offset:4096\n\t"
    "v_mfma_f32_16x16x32_bf16 v[72:75], a[48:51], a[60:63], v[72:75]\n\t"
    "ds_write_b128 v8, v[40:43] offset:5120\n\t"
    "v_mfma_f32_16x16x32_bf16 v[76:79], a[48:51], a[64:67], v[76:79]\n\t"
    "ds_write_b128 v8, v[44:47] offset:6144\n\t"
    "v_mfma_f32_16x16x32_bf16 v[80:83], a[48:51], a[68:71], v[80:83]\n\t"
    "ds_write_b128 v8, v[48:51] offset:7168\n\t"
    "v_mfma_f32_16x16x32_bf16 v[84:87], a[52:55], a[56:59], v[84:87]\n\t"
    "ds_write_b128 v8, v[52:55] offset:8192\n\t"
    "v_mfma_f32_16x16x32_bf16 v[88:91], a[52:55], a[60:63], v[88:91]\n\t"
    "ds_write_b128 v8, v[56:59] offset:9216\n\t"
    "v_mfma_f32_16x16x32_bf16 v[92:95], a[52:55], a[64:67], v[92:95]\n\t"
    "ds_write_b128 v8, v[60:63] offset:10240\n\t"
    "v_mfma_f32_16x16x32_bf16 v[96:99], a[52:55], a[68:71], v[96:99]\n\t"
    "ds_write_b128 v8, v[64:67] offset:11264\n\t"
    "ds_write_b128 v8, v[68:71] offset:12288\n\t"
    "ds_write_b128 v8, v[72:75] offset:13312\n\t"
    "ds_write_b128 v8, v[76:79] offset:14336\n\t"
    "s_waitcnt lgkmcnt(14)\n\t"
    "ds_write_b128 v8, v[80:83] offset:15360\n\t"
    "s_waitcnt lgkmcnt(14)\n\t"
    "ds_write_b128 v8, v[84:87] offset:16384\n\t"
    "s_waitcnt lgkmcnt(14)\n\t"
    "ds_write_b128 v8, v[88:91] offset:17408\n\t"
    "s_waitcnt lgkmcnt(14)\n\t"
    "ds_write_b128 v8, v[92:95] offset:18432\n\t"
    "s_waitcnt lgkmcnt(14)\n\t"
    "ds_write_b128 v8, v[96:99] offset:19456\n\t"
    "s_waitcnt lgkmcnt(0)\n\t"
    "s_barrier\n\t"
    "ds_read_b128 v[152:155], v9 offset:0\n\t"
    "ds_read_b128 v[156:159], v9 offset:20480\n\t"
    "ds_read_b128 v[160:163], v9 offset:40960\n\t"
    "ds_read_b128 v[164:167], v17 offset:0\n\t"
    "ds_read_b128 v[168:171], v9 offset:4096\n\t"
    "ds_read_b128 v[172:175], v9 offset:24576\n\t"
    "ds_read_b128 v[176:179], v9 offset:45056\n\t"
    "ds_read_b128 v[180:183], v17 offset:4096\n\t"
    "ds_read_b128 v[184:187], v9 offset:8192\n\t"
    "ds_read_b128 v[188:191], v9 offset:28672\n\t"
    "ds_read_b128 v[192:195], v9 offset:49152\n\t"
    "ds_read_b128 v[196:199], v17 offset:8192\n\t"
    "ds_read_b128 v[200:203], v9 offset:12288\n\t"
    "ds_read_b128 v[204:207], v9 offset:32768\n\t"
    "ds_read_b128 v[208:211], v9 offset:53248\n\t"
    "s_waitcnt lgkmcnt(14)\n\t"
    "ds_read_b128 v[212:215], v17 offset:12288\n\t"
    "s_waitcnt lgkmcnt(14)\n\t"
    "ds_read_b128 v[216:219], v9 offset:16384\n\t"
    "s_waitcnt lgkmcnt(14)\n\t"
    "ds_read_b128 v[220:223], v9 offset:36864\n\t"
    "s_waitcnt lgkmcnt(14)\n\t"
    "ds_read_b128 v[224:227], v9 offset:57344\n\t"
    "s_waitcnt lgkmcnt(14)\n\t"
    "ds_read_b128 v[228:231], v17 offset:16384\n\t"
    "v_pk_add_f32 v[152:153], v[152:153], v[156:157]\n\t"
    "v_pk_add_f32 v[154:155], v[154:155], v[158:159]\n\t"
    "v_pk_add_f32 v[160:161], v[160:161], v[164:165]\n\t"
    "v_pk_add_f32 v[162:163], v[162:163], v[166:167]\n\t"
    "v_pk_add_f32 v[152:153], v[152:153], v[160:161]\n\t"
    "v_pk_add_f32 v[154:155], v[154:155], v[162:163]\n\t"
    "s_waitcnt vmcnt(14)\n\t"
    "v_lshlrev_b32 v156, 16, v100\n\t"
    "v_and_b32 v157, 0xffff0000, v100\n\t"
    "v_lshlrev_b32 v158, 16, v101\n\t"
    "v_and_b32 v159, 0xffff0000, v101\n\t"
    "s_waitcnt vmcnt(12)\n\t"
    "v_pk_fma_f32 v[152:153], v[152:153], v[112:113], v[156:157]\n\t"
    "v_pk_fma_f32 v[154:155], v[154:155], v[114:115], v[158:159]\n\t"
    "v_cvt_pk_bf16_f32 v20, v152, v153\n\t"
    "v_cvt_pk_bf16_f32 v21, v154, v155\n\t"
    "s_waitcnt lgkmcnt(14)\n\t"
    "v_pk_add_f32 v[168:169], v[168:169], v[172:173]\n\t"
    "v_pk_add_f32 v[170:171], v[170:171], v[174:175]\n\t"
    "s_waitcnt lgkmcnt(12)\n\t"
    "v_pk_add_f32 v[176:177], v[176:177], v[180:181]\n\t"
    "v_pk_add_f32 v[178:179], v[178:179], v[182:183]\n\t"
    "v_pk_add_f32 v[168:169], v[168:169], v[176:177]\n\t"
    "v_pk_add_f32 v[170:171], v[170:171], v[178:179]\n\t"
    "v_lshlrev_b32 v172, 16, v102\n\t"
    "v_and_b32 v173, 0xffff0000, v102\n\t"
    "v_lshlrev_b32 v174, 16, v103\n\t"
    "v_and_b32 v175, 0xffff0000, v103\n\t"
    "s_waitcnt vmcnt(11)\n\t"
    "v_pk_fma_f32 v[168:169], v[168:169], v[116:117], v[172:173]\n\t"
    "v_pk_fma_f32 v[170:171], v[170:171], v[118:119], v[174:175]\n\t"
    "v_cvt_pk_bf16_f32 v22, v168, v169\n\t"
    "v_cvt_pk_bf16_f32 v23, v170, v171\n\t"
    "s_waitcnt lgkmcnt(10)\n\t"
    "v_pk_add_f32 v[184:185], v[184:185], v[188:189]\n\t"
    "v_pk_add_f32 v[186:187], v[186:187], v[190:191]\n\t"
    "s_waitcnt lgkmcnt(8)\n\t"
    "v_pk_add_f32 v[192:193], v[192:193], v[196:197]\n\t"
    "v_pk_add_f32 v[194:195], v[194:195], v[198:199]\n\t"
    "v_pk_add_f32 v[184:185], v[184:185], v[192:193]\n\t"
    "v_pk_add_f32 v[186:187], v[186:187], v[194:195]\n\t"
    "s_waitcnt vmcnt(8)\n\t"
    "v_lshlrev_b32 v188, 16, v104\n\t"
    "v_and_b32 v189, 0xffff0000, v104\n\t"
    "v_lshlrev_b32 v190, 16, v105\n\t"
    "v_and_b32 v191, 0xffff0000, v105\n\t"
    "s_waitcnt vmcnt(6)\n\t"
    "v_pk_fma_f32 v[184:185], v[184:185], v[120:121], v[188:189]\n\t"
    "v_pk_fma_f32 v[186:187], v[186:187], v[122:123], v[190:191]\n\t"
    "v_cvt_pk_bf16_f32 v24, v184, v185\n\t"
    "v_cvt_pk_bf16_f32 v25, v186, v187\n\t"
    "s_waitcnt lgkmcnt(6)\n\t"
    "v_pk_add_f32 v[200:201], v[200:201], v[204:205]\n\t"
    "v_pk_add_f32 v[202:203], v[202:203], v[206:207]\n\t"
    "s_waitcnt lgkmcnt(4)\n\t"
    "v_pk_add_f32 v[208:209], v[208:209], v[212:213]\n\t"
    "v_pk_add_f32 v[210:211], v[210:211], v[214:215]\n\t"
    "v_pk_add_f32 v[200:201], v[200:201], v[208:209]\n\t"
    "v_pk_add_f32 v[202:203], v[202:203], v[210:211]\n\t"
    "v_lshlrev_b32 v204, 16, v106\n\t"
    "v_and_b32 v205, 0xffff0000, v106\n\t"
    "v_lshlrev_b32 v206, 16, v107\n\t"
    "v_and_b32 v207, 0xffff0000, v107\n\t"
    "s_waitcnt vmcnt(5)\n\t"
    "v_pk_fma_f32 v[200:201], v[200:201], v[124:125], v[204:205]\n\t"
    "v_pk_fma_f32 v[202:203], v[202:203], v[126:127], v[206:207]\n\t"
    "v_cvt_pk_bf16_f32 v26, v200, v201\n\t"
    "v_cvt_pk_bf16_f32 v27, v202, v203\n\t"
    "s_waitcnt lgkmcnt(2)\n\t"
    "v_pk_add_f32 v[216:217], v[216:217], v[220:221]\n\t"
    "v_pk_add_f32 v[218:219], v[218:219], v[222:223]\n\t"
    "s_waitcnt lgkmcnt(0)\n\t"
    "v_pk_add_f32 v[224:225], v[224:225], v[228:229]\n\t"
    "v_pk_add_f32 v[226:227], v[226:227], v[230:231]\n\t"
    "v_pk_add_f32 v[216:217], v[216:217], v[224:225]\n\t"
    "v_pk_add_f32 v[218:219], v[218:219], v[226:227]\n\t"
    "s_waitcnt vmcnt(2)\n\t"
    "v_lshlrev_b32 v220, 16, v108\n\t"
    "v_and_b32 v221, 0xffff0000, v108\n\t"
    "v_lshlrev_b32 v222, 16, v109\n\t"
    "v_and_b32 v223, 0xffff0000, v109\n\t"
    "s_waitcnt vmcnt(1)\n\t"
    "v_pk_fma_f32 v[216:217], v[216:217], v[128:129], v[220:221]\n\t"
    "v_pk_fma_f32 v[218:219], v[218:219], v[130:131], v[222:223]\n\t"
    "v_cvt_pk_bf16_f32 v28, v216, v217\n\t"
    "v_cvt_pk_bf16_f32 v29, v218, v219\n\t"
    "global_store_dwordx4 v14, v[20:23], s[44:45] offset:0 sc1\n\t"
    "global_store_dwordx4 v14, v[24:27], s[44:45] offset:1024 sc1\n\t"
    "global_store_dwordx2 v15, v[28:29], s[44:45] sc1\n\t"
    "v_lshlrev_b32 v152, 16, v20\n\t"
    "v_and_b32 v153, 0xffff0000, v20\n\t"
    "v_lshlrev_b32 v154, 16, v21\n\t"
    "v_and_b32 v155, 0xffff0000, v21\n\t"
    "v_lshlrev_b32 v156, 16, v22\n\t"
    "v_and_b32 v157, 0xffff0000, v22\n\t"
    "v_lshlrev_b32 v158, 16, v23\n\t"
    "v_and_b32 v159, 0xffff0000, v23\n\t"
    "v_lshlrev_b32 v160, 16, v24\n\t"
    "v_and_b32 v161, 0xffff0000, v24\n\t"
    "v_lshlrev_b32 v162, 16, v25\n\t"
    "v_and_b32 v163, 0xffff0000, v25\n\t"
    "v_lshlrev_b32 v164, 16, v26\n\t"
    "v_and_b32 v165, 0xffff0000, v26\n\t"
    "v_lshlrev_b32 v166, 16, v27\n\t"
    "v_and_b32 v167, 0xffff0000, v27\n\t"
    "v_lshlrev_b32 v168, 16, v28\n\t"
    "v_and_b32 v169, 0xffff0000, v28\n\t"
    "v_lshlrev_b32 v170, 16, v29\n\t"
    "v_and_b32 v171, 0xffff0000, v29\n\t"
    "v_add_f32 v132, 1.0, v132\n\t"
    "v_add_f32 v133, 1.0, v133\n\t"
    "v_cvt_pk_bf16_f32 v133, v132, v133\n\t"
    "v_lshlrev_b32 v132, 16, v133\n\t"
    "v_and_b32 v133, 0xffff0000, v133\n\t"
    "v_pk_mul_f32 v[132:133], v[132:133], v[152:153]\n\t"
    "v_cvt_pk_bf16_f32 v32, v132, v133\n\t"
    "v_add_f32 v134, 1.0, v134\n\t"
    "v_add_f32 v135, 1.0, v135\n\t"
    "v_cvt_pk_bf16_f32 v135, v134, v135\n\t"
    "v_lshlrev_b32 v134, 16, v135\n\t"
    "v_and_b32 v135, 0xffff0000, v135\n\t"
    "v_pk_mul_f32 v[134:135], v[134:135], v[154:155]\n\t"
    "v_cvt_pk_bf16_f32 v33, v134, v135\n\t"
    "v_add_f32 v136, 1.0, v136\n\t"
    "v_add_f32 v137, 1.0, v137\n\t"
    "v_cvt_pk_bf16_f32 v137, v136, v137\n\t"
    "v_lshlrev_b32 v136, 16, v137\n\t"
    "v_and_b32 v137, 0xffff0000, v137\n\t"
    "v_pk_mul_f32 v[136:137], v[136:137], v[156:157]\n\t"
    "v_cvt_pk_bf16_f32 v34, v136, v137\n\t"
    "v_add_f32 v138, 1.0, v138\n\t"
    "v_add_f32 v139, 1.0, v139\n\t"
    "v_cvt_pk_bf16_f32 v139, v138, v139\n\t"
    "v_lshlrev_b32 v138, 16, v139\n\t"
    "v_and_b32 v139, 0xffff0000, v139\n\t"
    "v_pk_mul_f32 v[138:139], v[138:139], v[158:159]\n\t"
    "v_cvt_pk_bf16_f32 v35, v138, v139\n\t"
    "v_add_f32 v140, 1.0, v140\n\t"
    "v_add_f32 v141, 1.0, v141\n\t"
    "v_cvt_pk_bf16_f32 v141, v140, v141\n\t"
    "v_lshlrev_b32 v140, 16, v141\n\t"
    "v_and_b32 v141, 0xffff0000, v141\n\t"
    "v_pk_mul_f32 v[140:141], v[140:141], v[160:161]\n\t"
    "v_cvt_pk_bf16_f32 v36, v140, v141\n\t"
    "v_add_f32 v142, 1.0, v142\n\t"
    "v_add_f32 v143, 1.0, v143\n\t"
    "v_cvt_pk_bf16_f32 v143, v142, v143\n\t"
    "v_lshlrev_b32 v142, 16, v143\n\t"
    "v_and_b32 v143, 0xffff0000, v143\n\t"
    "v_pk_mul_f32 v[142:143], v[142:143], v[162:163]\n\t"
    "v_cvt_pk_bf16_f32 v37, v142, v143\n\t"
    "v_add_f32 v144, 1.0, v144\n\t"
    "v_add_f32 v145, 1.0, v145\n\t"
    "v_cvt_pk_bf16_f32 v145, v144, v145\n\t"
    "v_lshlrev_b32 v144, 16, v145\n\t"
    "v_and_b32 v145, 0xffff0000, v145\n\t"
    "v_pk_mul_f32 v[144:145], v[144:145], v[164:165]\n\t"
    "v_cvt_pk_bf16_f32 v38, v144, v145\n\t"
    "v_add_f32 v146, 1.0, v146\n\t"
    "v_add_f32 v147, 1.0, v147\n\t"
    "v_cvt_pk_bf16_f32 v147, v146, v147\n\t"
    "v_lshlrev_b32 v146, 16, v147\n\t"
    "v_and_b32 v147, 0xffff0000, v147\n\t"
    "v_pk_mul_f32 v[146:147], v[146:147], v[166:167]\n\t"
    "v_cvt_pk_bf16_f32 v39, v146, v147\n\t"
    "s_waitcnt vmcnt(3)\n\t"
    "v_add_f32 v148, 1.0, v148\n\t"
    "v_add_f32 v149, 1.0, v149\n\t"
    "v_cvt_pk_bf16_f32 v149, v148, v149\n\t"
    "v_lshlrev_b32 v148, 16, v149\n\t"
    "v_and_b32 v149, 0xffff0000, v149\n\t"
    "v_pk_mul_f32 v[148:149], v[148:149], v[168:169]\n\t"
    "v_cvt_pk_bf16_f32 v40, v148, v149\n\t"
    "v_add_f32 v150, 1.0, v150\n\t"
    "v_add_f32 v151, 1.0, v151\n\t"
    "v_cvt_pk_bf16_f32 v151, v150, v151\n\t"
    "v_lshlrev_b32 v150, 16, v151\n\t"
    "v_and_b32 v151, 0xffff0000, v151\n\t"
    "v_pk_mul_f32 v[150:151], v[150:151], v[170:171]\n\t"
    "v_cvt_pk_bf16_f32 v41, v150, v151\n\t"
    "global_store_dwordx4 v14, v[32:35], s[56:57] offset:0 sc1\n\t"
    "global_store_dwordx4 v14, v[36:39], s[56:57] offset:1024 sc1\n\t"
    "global_store_dwordx2 v15, v[40:41], s[56:57] sc1\n\t"
    "v_add_f32 v44, v152, v153\n\t"
    "v_add_f32 v46, v154, v155\n\t"
    "v_add_f32 v44, v44, v156\n\t"
    "v_add_f32 v46, v46, v157\n\t"
    "v_add_f32 v44, v44, v158\n\t"
    "v_add_f32 v46, v46, v159\n\t"
    "v_add_f32 v44, v44, v160\n\t"
    "v_add_f32 v46, v46, v161\n\t"
    "v_add_f32 v44, v44, v162\n\t"
    "v_add_f32 v46, v46, v163\n\t"
    "v_add_f32 v44, v44, v164\n\t"
    "v_add_f32 v46, v46, v165\n\t"
    "v_add_f32 v44, v44, v166\n\t"
    "v_add_f32 v46, v46, v167\n\t"
    "v_add_f32 v44, v44, v168\n\t"
    "v_add_f32 v46, v46, v169\n\t"
    "v_add_f32 v44, v44, v170\n\t"
    "v_add_f32 v46, v46, v171\n\t"
    "v_add_f32 v44, v44, v46\n\t"
    "v_mov_b32 v45, v44\n\t"
    "s_nop 1\n\t"
    "v_permlane32_swap_b32 v44, v45\n\t"
    "v_add_f32 v44, v44, v45\n\t"
    "v_mov_b32 v45, v44\n\t"
    "s_nop 1\n\t"
    "v_permlane16_swap_b32 v44, v45\n\t"
    "v_add_f32 v44, v44, v45\n\t"
    "v_mul_f32 v48, 0x3c4ccccd, v44\n\t"
    "v_sub_f32 v152, v152, v48\n\t"
    "v_sub_f32 v153, v153, v48\n\t"
    "v_sub_f32 v154, v154, v48\n\t"
    "v_sub_f32 v155, v155, v48\n\t"
    "v_sub_f32 v156, v156, v48\n\t"
    "v_sub_f32 v157, v157, v48\n\t"
    "v_sub_f32 v158, v158, v48\n\t"
    "v_sub_f32 v159, v159, v48\n\t"
    "v_sub_f32 v160, v160, v48\n\t"
    "v_sub_f32 v161, v161, v48\n\t"
    "v_sub_f32 v162, v162, v48\n\t"
    "v_sub_f32 v163, v163, v48\n\t"
    "v_sub_f32 v164, v164, v48\n\t"
    "v_sub_f32 v165, v165, v48\n\t"
    "v_sub_f32 v166, v166, v48\n\t"
    "v_sub_f32 v167, v167, v48\n\t"
    "v_sub_f32 v168, v168, v48\n\t"
    "v_sub_f32 v169, v169, v48\n\t"
    "v_sub_f32 v170, v170, v48\n\t"
    "v_sub_f32 v171, v171, v48\n\t"
    "v_mul_f32 v49, v152, v152\n\t"
    "v_mul_f32 v46, v153, v153\n\t"
    "v_fmac_f32 v49, v154, v154\n\t"
    "v_fmac_f32 v46, v155, v155\n\t"
    "v_fmac_f32 v49, v156, v156\n\t"
    "v_fmac_f32 v46, v157, v157\n\t"
    "v_fmac_f32 v49, v158, v158\n\t"
    "v_fmac_f32 v46, v159, v159\n\t"
    "v_fmac_f32 v49, v160, v160\n\t"
    "v_fmac_f32 v46, v161, v161\n\t"
    "v_fmac_f32 v49, v162, v162\n\t"
    "v_fmac_f32 v46, v163, v163\n\t"
    "v_fmac_f32 v49, v164, v164\n\t"
    "v_fmac_f32 v46, v165, v165\n\t"
    "v_fmac_f32 v49, v166, v166\n\t"
    "v_fmac_f32 v46, v167, v167\n\t"
    "v_fmac_f32 v49, v168, v168\n\t"
    "v_fmac_f32 v46, v169, v169\n\t"
    "v_fmac_f32 v49, v170, v170\n\t"
    "v_fmac_f32 v46, v171, v171\n\t"
    "v_add_f32 v49, v49, v46\n\t"
    "v_mov_b32 v45, v49\n\t"
    "s_nop 1\n\t"
    "v_permlane32_swap_b32 v49, v45\n\t"
    "v_add_f32 v49, v49, v45\n\t"
    "v_mov_b32 v45, v49\n\t"
    "s_nop 1\n\t"
    "v_permlane16_swap_b32 v49, v45\n\t"
    "v_add_f32 v49, v49, v45\n\t"
    "global_store_dwordx2 v16, v[48:49], s[46:47] sc1\n\t"
    "s_nop 1\n\t"
      : "+{s58}"(fo0), "+{s59}"(wo0)
      : "{v1}"(fo), "{v2}"(co), "{v3}"(co1), "{v8}"(rw), "{v9}"(rr), "{v17}"(rr + 61440u),
        "{v10}"(rs), "{v11}"(rs1), "{v12}"(go), "{v13}"(go1), "{v14}"(st), "{v15}"(st1), "{v16}"(ss), "{v18}"(pl),
        "{s[36:37]}"(sF), "{s[38:39]}"(sW), "{s[40:41]}"(a.x), "{s[42:43]}"(ws + WS_MOD + 2 * 2560), "{s[44:45]}"(ws + WS_X1F), "{s[46:47]}"(ws + WS_ST),
        "{s[56:57]}"(ws + WS_X1C),
        "{s[64:65]}"(tag), "{s[66:67]}"(ws), "{s68}"(wv), "{s70}"(myf)
      : "v4", "v5", "v6", "v7", "v20", "v21", "v22", "v23", "v24", "v25", "v26", "v27", "v28", "v29", "v30", "v31", "v32", "v33", "v34", "v35", "v36", "v37", "v38", "v39", "v40", "v41", "v42", "v43", "v44", "v45", "v46", "v47", "v48", "v49", "v50", "v51", "v52", "v53", "v54", "v55", "v56", "v57", "v58", "v59", "v60", "v61", "v62", "v63", "v64", "v65", "v66", "v67", "v68", "v69", "v70", "v71", "v72", "v73", "v74", "v75", "v76", "v77", "v78", "v79", "v80", "v81", "v82", "v83", "v84", "v85", "v86", "v87", "v88", "v89", "v90", "v91", "v92", "v93", "v94", "v95", "v96", "v97", "v98", "v99", "v100", "v101", "v102", "v103", "v104", "v105", "v106", "v107", "v108", "v109", "v110", "v111", "v112", "v113", "v114", "v115", "v116", "v117", "v118", "v119", "v120", "v121", "v122", "v123", "v124", "v125", "v126", "v127", "v128", "v129", "v130", "v131", "v132", "v133", "v134", "v135", "v136", "v137", "v138", "v139", "v140", "v141", "v142", "v143", "v144", "v145", "v146", "v147", "v148", "v149", "v150", "v151", "v152", "v153", "v154", "v155", "v156", "v157", "v158", "v159", "v160", "v161", "v162", "v163", "v164", "v165", "v166", "v167", "v168", "v169", "v170", "v171", "v172", "v173", "v174", "v175", "v176", "v177", "v178", "v179", "v180", "v181", "v182", "v183", "v184", "v185", "v186", "v187", "v188", "v189", "v190", "v191", "v192", "v193", "v194", "v195", "v196", "v197", "v198", "v199", "v200", "v201", "v202", "v203", "v204", "v205", "v206", "v207", "v208", "v209", "v210", "v211", "v212", "v213", "v214", "v215", "v216", "v217", "v218", "v219", "v220", "v221", "v222", "v223", "v224", "v225", "v226", "v227", "v228", "v229", "v230", "v231", "v232", "v233", "v234", "v235", "v236", "v237", "v238", "v239", "a0", "a1", "a2", "a3", "a4", "a5", "a6", "a7", "a8", "a9", "a10", "a11", "a12", "a13", "a14", "a15", "a16", "a17", "a18", "a19", "a20", "a21", "a22", "a23", "a24", "a25", "a26", "a27", "a28", "a29", "a30", "a31", "a32", "a33", "a34", "a35", "a36", "a37", "a38", "a39", "a40", "a41", "a42", "a43", "a44", "a45", "a46", "a47", "a48", "a49", "a50", "a51", "a52", "a53", "a54", "a55", "a56", "a57", "a58", "a59", "a60", "a61", "a62", "a63", "a64", "a65", "a66", "a67", "a68", "a69", "a70", "a71", "a72", "a73", "a74", "a75", "a76", "a77", "a78", "a79", "a80", "a81", "a82", "a83", "a84", "a85", "a86", "a87", "a88", "a89", "a90", "a91", "a92", "a93", "a94", "a95", "a96", "a97", "a98", "a99", "a100", "a101", "a102", "a103", "a104", "a105", "a106", "a107", "a108", "a109", "a110", "a111", "a112", "a113", "a114", "a115", "a116", "a117", "a118", "a119", "a120", "a121", "a122", "a123", "a124", "a125", "a126", "a127", "a128", "a129", "a130", "a131", "a132", "a133", "a134", "a135", "a136", "a137", "a138", "a139", "a140", "a141", "a142", "a143", "a144", "a145", "a146", "a147", "a148", "a149", "a150", "a151", "a152", "a153", "a154", "a155", "a156", "a157", "a158", "a159", "a160", "a161", "a162", "a163", "a164", "a165", "a166", "a167", "a168", "a169", "a170", "a171", "a172", "a173", "a174", "a175", "a176", "a177", "a178", "a179", "a180", "a181", "a182", "a183", "a184", "a185", "a186", "a187", "a188", "a189", "a190", "a191", "a192", "a193", "a194", "a195", "a196", "a197", "a198", "a199", "a200", "a201", "a202", "a203", "a204", "a205", "a206", "a207", "a208", "a209", "a210", "a211", "a212", "a213", "a214", "a215", "a216", "a217", "a218", "a219", "a220", "a221", "a222", "a223", "a224", "a225", "a226", "a227", "a228", "a229", "a230", "a231", "a232", "a233", "a234", "a235", "a236", "a237", "a238", "a239", "a240", "a241", "a242", "a243", "a244", "a245", "a246", "a247", "a248", "a249", "a250", "a251", "a252", "a253", "a254", "a255", "s48", "s49", "s50", "s51", "s52", "s53", "s54", "s55", "s60", "s61", "s62", "s63", "s69", "s72", "s73", "vcc", "memory");
    STAMP_OUT(1)
  }
  {
    unsigned lane = __builtin_amdgcn_mbcnt_hi(-1, __builtin_amdgcn_mbcnt_lo(-1, 0));
    asm volatile("" : "+v"(lane));
    const unsigned w = wv, x = b & 7u, jj = b >> 3;
    const unsigned ct = 4u * (x & 3u) + (jj & 3u), rt = 8u * (x >> 2) + (jj >> 2), odd = ct & 1u;
    const unsigned qb0 = rt * 8u + 2u * w;
    const char* sW = ws + WS_WFP + (size_t)((w >> 1) * 16u + ct) * (640u * 224u) + (w & 1u) * 7168u + 3072u;
    const unsigned m0b = __builtin_amdgcn_readfirstlane(lb + (w >> 1) * 14336u + (w & 1u) * 7168u + 3072u);
    const unsigned m0v = __builtin_amdgcn_readfirstlane(lb + 114688u + (w & 1u) * 1024u);
    const unsigned lo = lane * 16u;
    const unsigned g = lane >> 4, q = (lane >> 2) & 3u, pp = lane & 3u, c16 = lane & 15u;
    unsigned va = lb + (4u * g + q) * 224u + 8u * pp;
    const char* sX = ws + WS_X1C;
    char* sO = ws + WS_HH;
    const unsigned xo0 = (qb0 * 20u) * 1024u + lane * 16u, xo1 = xo0 + 20u * 1024u;
    const unsigned so0 = (qb0 * 16u + c16) * 64u + g * 16u, so1 = so0 + 16u * 64u;
    const unsigned vo = ct * 2048u + (w & 1u) * 1024u + lane * 16u;
    const unsigned vrd = lb + 114688u + g * 16u;
    const unsigned q64 = 2u * rt + (w >> 1), qbl = 2u * (w & 1u);
    const unsigned k0 = (7u * ct + odd) >> 1, f1 = 7u * ct + (odd ? 0u : 6u);
    const unsigned st0 = ((q64 * 56u + k0) * 4u + qbl) * 1024u + lane * 16u, st1 = st0 + 1024u;
    const unsigned su0 = ((q64 * 56u + (f1 >> 1)) * 4u + qbl) * 1024u + lane * 16u + (f1 & 1u) * 8u, su1 = su0 + 1024u;
    const unsigned ntb = 4u * (x & 1u) + (jj & 3u), qtb = 8u * (x >> 1) + (jj >> 2);
    const unsigned myf = __builtin_amdgcn_readfirstlane(F4 + 8u * (8u * qtb + ntb));
    const unsigned pl = F4 + 8u * (16u * rt + c16);
    asm volatile(
    "s_waitcnt vmcnt(0)\n\t"
    "s_barrier\n\t"
    "s_cmp_lg_u32 s68, 0\n\t"
    "s_cbranch_scc1 LM_pub19_%=\n\t"
    "v_mov_b32 v28, s64\n\t"
    "v_mov_b32 v29, s65\n\t"
    "v_mov_b32 v30, s70\n\t"
    "s_mov_b64 exec, 1\n\t"
    "global_store_dwordx2 v30, v[28:29], s[66:67] sc1\n\t"
    "s_mov_b64 exec, -1\n\t"
    "LM_pub19_%=:\n\t"
    "s_mov_b32 m0, s46\n\t"
    "s_nop 0\n\t"
    "global_load_lds_dwordx4 v3, s[38:39] offset:-3072\n\t"
    "global_load_lds_dwordx4 v3, s[38:39] offset:-2048\n\t"
    "global_load_lds_dwordx4 v3, s[38:39] offset:-1024\n\t"
    "global_load_lds_dwordx4 v3, s[38:39] offset:0\n\t"
    "global_load_lds_dwordx4 v3, s[38:39] offset:1024\n\t"
    "global_load_lds_dwordx4 v3, s[38:39] offset:2048\n\t"
    "global_load_lds_dwordx4 v3, s[38:39] offset:3072\n\t"
    "s_add_u32 s38, s38, 14336\n\t"
    "s_addc_u32 s39, s39, 0\n\t"
    "s_add_u32 m0, s46, 28672\n\t"
    "s_nop 0\n\t"
    "global_load_lds_dwordx4 v3, s[38:39] offset:-3072\n\t"
    "global_load_lds_dwordx4 v3, s[38:39] offset:-2048\n\t"
    "global_load_lds_dwordx4 v3, s[38:39] offset:-1024\n\t"
    "global_load_lds_dwordx4 v3, s[38:39] offset:0\n\t"
    "global_load_lds_dwordx4 v3, s[38:39] offset:1024\n\t"
    "global_load_lds_dwordx4 v3, s[38:39] offset:2048\n\t"
    "global_load_lds_dwordx4 v3, s[38:39] offset:3072\n\t"
    "s_add_u32 s38, s38, 14336\n\t"
    "s_addc_u32 s39, s39, 0\n\t"
    "s_mov_b32 s69, 100000\n\t"
    "global_load_dwordx2 v[28:29], v9, s[66:67] sc1\n\t"
    "s_sleep 2\n\t"
    "LM_poll20_%=:\n\t"
    "global_load_dwordx2 v[30:31], v9, s[66:67] sc1\n\t"
    "s_waitcnt vmcnt(1)\n\t"
    "v_cmp_eq_u64 vcc, s[64:65], v[28:29]\n\t"
    "s_cmp_eq_u64 vcc, -1\n\t"
    "s_cbranch_scc1 LM_polld21_%=\n\t"
    "global_load_dwordx2 v[28:29], v9, s[66:67] sc1\n\t"
    "s_waitcnt vmcnt(1)\n\t"
    "v_cmp_eq_u64 vcc, s[64:65], v[30:31]\n\t"
    "s_cmp_eq_u64 vcc, -1\n\t"
    "s_cbranch_scc1 LM_polld21_%=\n\t"
    "s_sub_u32 s69, s69, 1\n\t"
    "s_cmp_lg_u32 s69, 0\n\t"
    "s_cbranch_scc1 LM_poll20_%=\n\t"
    "LM_polld21_%=:\n\t"
    "global_load_dwordx4 a[0:3], v1, s[36:37] sc1\n\t"
    "global_load_dwordx4 a[4:7], v2, s[36:37] sc1\n\t"
    "s_add_u32 s36, s36, 1024\n\t"
    "s_addc_u32 s37, s37, 0\n\t"
    "global_load_dwordx4 v[176:179], v11, s[40:41] sc1\n\t"
    "global_load_dwordx4 v[184:187], v12, s[40:41] sc1\n\t"
    "global_load_dwordx4 a[8:11], v1, s[36:37] sc1\n\t"
    "global_load_dwordx4 a[12:15], v2, s[36:37] sc1\n\t"
    "s_add_u32 s36, s36, 1024\n\t"
    "s_addc_u32 s37, s37, 0\n\t"
    "global_load_dwordx4 a[16:19], v1, s[36:37] sc1\n\t"
    "global_load_dwordx4 a[20:23], v2, s[36:37] sc1\n\t"
    "s_add_u32 s36, s36, 1024\n\t"
    "s_addc_u32 s37, s37, 0\n\t"
    "v_mov_b32 v64, 0\n\t"
    "v_mov_b32 v65, 0\n\t"
    "v_mov_b32 v66, 0\n\t"
    "v_mov_b32 v67, 0\n\t"
    "v_mov_b32 v68, 0\n\t"
    "v_mov_b32 v69, 0\n\t"
    "v_mov_b32 v70, 0\n\t"
    "v_mov_b32 v71, 0\n\t"
    "v_mov_b32 v72, 0\n\t"
    "v_mov_b32 v73, 0\n\t"
    "v_mov_b32 v74, 0\n\t"
    "v_mov_b32 v75, 0\n\t"
    "v_mov_b32 v76, 0\n\t"
    "v_mov_b32 v77, 0\n\t"
    "v_mov_b32 v78, 0\n\t"
    "v_mov_b32 v79, 0\n\t"
    "v_mov_b32 v80, 0\n\t"
    "v_mov_b32 v81, 0\n\t"
    "v_mov_b32 v82, 0\n\t"
    "v_mov_b32 v83, 0\n\t"
    "v_mov_b32 v84, 0\n\t"
    "v_mov_b32 v85, 0\n\t"
    "v_mov_b32 v86, 0\n\t"
    "v_mov_b32 v87, 0\n\t"
    "v_mov_b32 v88, 0\n\t"
    "v_mov_b32 v89, 0\n\t"
    "v_mov_b32 v90, 0\n\t"
    "v_mov_b32 v91, 0\n\t"
    "v_mov_b32 v92, 0\n\t"
    "v_mov_b32 v93, 0\n\t"
    "v_mov_b32 v94, 0\n\t"
    "v_mov_b32 v95, 0\n\t"
    "v_mov_b32 v96, 0\n\t"
    "v_mov_b32 v97, 0\n\t"
    "v_mov_b32 v98, 0\n\t"
    "v_mov_b32 v99, 0\n\t"
    "v_mov_b32 v100, 0\n\t"
    "v_mov_b32 v101, 0\n\t"
    "v_mov_b32 v102, 0\n\t"
    "v_mov_b32 v103, 0\n\t"
    "v_mov_b32 v104, 0\n\t"
    "v_mov_b32 v105, 0\n\t"
    "v_mov_b32 v106, 0\n\t"
    "v_mov_b32 v107, 0\n\t"
    "v_mov_b32 v108, 0\n\t"
    "v_mov_b32 v109, 0\n\t"
    "v_mov_b32 v110, 0\n\t"
    "v_mov_b32 v111, 0\n\t"
    "v_mov_b32 v112, 0\n\t"
    "v_mov_b32 v113, 0\n\t"
    "v_mov_b32 v114, 0\n\t"
    "v_mov_b32 v115, 0\n\t"
    "v_mov_b32 v116, 0\n\t"
    "v_mov_b32 v117, 0\n\t"
    "v_mov_b32 v118, 0\n\t"
    "v_mov_b32 v119, 0\n\t"
    "v_mov_b32 v120, 0\n\t"
    "v_mov_b32 v121, 0\n\t"
    "v_mov_b32 v122, 0\n\t"
    "v_mov_b32 v123, 0\n\t"
    "v_mov_b32 v124, 0\n\t"
    "v_mov_b32 v125, 0\n\t"
    "v_mov_b32 v126, 0\n\t"
    "v_mov_b32 v127, 0\n\t"
    "v_mov_b32 v128, 0\n\t"
    "v_mov_b32 v129, 0\n\t"
    "v_mov_b32 v130, 0\n\t"
    "v_mov_b32 v131, 0\n\t"
    "v_mov_b32 v132, 0\n\t"
    "v_mov_b32 v133, 0\n\t"
    "v_mov_b32 v134, 0\n\t"
    "v_mov_b32 v135, 0\n\t"
    "v_mov_b32 v136, 0\n\t"
    "v_mov_b32 v137, 0\n\t"
    "v_mov_b32 v138, 0\n\t"
    "v_mov_b32 v139, 0\n\t"
    "v_mov_b32 v140, 0\n\t"
    "v_mov_b32 v141, 0\n\t"
    "v_mov_b32 v142, 0\n\t"
    "v_mov_b32 v143, 0\n\t"
    "v_mov_b32 v144, 0\n\t"
    "v_mov_b32 v145, 0\n\t"
    "v_mov_b32 v146, 0\n\t"
    "v_mov_b32 v147, 0\n\t"
    "v_mov_b32 v148, 0\n\t"
    "v_mov_b32 v149, 0\n\t"
    "v_mov_b32 v150, 0\n\t"
    "v_mov_b32 v151, 0\n\t"
    "v_mov_b32 v152, 0\n\t"
    "v_mov_b32 v153, 0\n\t"
    "v_mov_b32 v154, 0\n\t"
    "v_mov_b32 v155, 0\n\t"
    "v_mov_b32 v156, 0\n\t"
    "v_mov_b32 v157, 0\n\t"
    "v_mov_b32 v158, 0\n\t"
    "v_mov_b32 v159, 0\n\t"
    "v_mov_b32 v160, 0\n\t"
    "v_mov_b32 v161, 0\n\t"
    "v_mov_b32 v162, 0\n\t"
    "v_mov_b32 v163, 0\n\t"
    "v_mov_b32 v164, 0\n\t"
    "v_mov_b32 v165, 0\n\t"
    "v_mov_b32 v166, 0\n\t"
    "v_mov_b32 v167, 0\n\t"
    "v_mov_b32 v168, 0\n\t"
    "v_mov_b32 v169, 0\n\t"
    "v_mov_b32 v170, 0\n\t"
    "v_mov_b32 v171, 0\n\t"
    "v_mov_b32 v172, 0\n\t"
    "v_mov_b32 v173, 0\n\t"
    "v_mov_b32 v174, 0\n\t"
    "v_mov_b32 v175, 0\n\t"
    "s_add_u32 s53, s46, 57344\n\t"
    "s_mov_b32 s54, 57344\n\t"
    "s_barrier\n\t"
    "ds_read_b64_tr_b16 a[32:33], v10 offset:0\n\t"
    "ds_read_b64_tr_b16 a[34:35], v10 offset:3584\n\t"
    "ds_read_b64_tr_b16 a[36:37], v10 offset:14336\n\t"
    "ds_read_b64_tr_b16 a[38:39], v10 offset:17920\n\t"
    "ds_read_b64_tr_b16 a[40:41], v10 offset:32\n\t"
    "ds_read_b64_tr_b16 a[42:43], v10 offset:3616\n\t"
    "ds_read_b64_tr_b16 a[44:45], v10 offset:14368\n\t"
    "ds_read_b64_tr_b16 a[46:47], v10 offset:17952\n\t"
    "ds_read_b64_tr_b16 a[48:49], v10 offset:64\n\t"
    "ds_read_b64_tr_b16 a[50:51], v10 offset:3648\n\t"
    "s_mov_b32 s52, 4\n\t"
    "LM_22_%=:\n\t"
    "s_waitcnt vmcnt(6)\n\t"
    "s_waitcnt lgkmcnt(6)\n\t"
    "v_mfma_f32_16x16x32_bf16 v[64:67], a[32:35], a[0:3], v[64:67]\n\t"
    "ds_read_b64_tr_b16 a[52:53], v10 offset:14400\n\t"
    "global_load_dwordx4 a[24:27], v1, s[36:37] sc1\n\t"
    "v_mfma_f32_16x16x32_bf16 v[68:71], a[32:35], a[4:7], v[68:71]\n\t"
    "ds_read_b64_tr_b16 a[54:55], v10 offset:17984\n\t"
    "v_mfma_f32_16x16x32_bf16 v[72:75], a[36:39], a[0:3], v[72:75]\n\t"
    "ds_read_b64_tr_b16 a[56:57], v10 offset:96\n\t"
    "v_mfma_f32_16x16x32_bf16 v[76:79], a[36:39], a[4:7], v[76:79]\n\t"
    "ds_read_b64_tr_b16 a[58:59], v10 offset:3680\n\t"
    "s_waitcnt lgkmcnt(6)\n\t"
    "v_mfma_f32_16x16x32_bf16 v[80:83], a[40:43], a[0:3], v[80:83]\n\t"
    "ds_read_b64_tr_b16 a[32:33], v10 offset:14432\n\t"
    "v_mfma_f32_16x16x32_bf16 v[84:87], a[40:43], a[4:7], v[84:87]\n\t"
    "ds_read_b64_tr_b16 a[34:35], v10 offset:18016\n\t"
    "global_load_dwordx4 a[28:31], v2, s[36:37] sc1\n\t"
    "v_mfma_f32_16x16x32_bf16 v[88:91], a[44:47], a[0:3], v[88:91]\n\t"
    "ds_read_b64_tr_b16 a[36:37], v10 offset:128\n\t"
    "s_add_u32 s36, s36, 1024\n\t"
    "s_addc_u32 s37, s37, 0\n\t"
    "v_mfma_f32_16x16x32_bf16 v[92:95], a[44:47], a[4:7], v[92:95]\n\t"
    "ds_read_b64_tr_b16 a[38:39], v10 offset:3712\n\t"
    "s_waitcnt lgkmcnt(6)\n\t"
    "v_mfma_f32_16x16x32_bf16 v[96:99], a[48:51], a[0:3], v[96:99]\n\t"
    "ds_read_b64_tr_b16 a[40:41], v10 offset:14464\n\t"
    "v_mfma_f32_16x16x32_bf16 v[100:103], a[48:51], a[4:7], v[100:103]\n\t"
    "ds_read_b64_tr_b16 a[42:43], v10 offset:18048\n\t"
    "v_mfma_f32_16x16x32_bf16 v[104:107], a[52:55], a[0:3], v[104:107]\n\t"
    "ds_read_b64_tr_b16 a[44:45], v10 offset:160\n\t"
    "s_add_u32 m0, s53, 0\n\t"
    "s_nop 0\n\t"
    "global_load_lds_dwordx4 v3, s[38:39] offset:-3072\n\t"
    "v_mfma_f32_16x16x32_bf16 v[108:111], a[52:55], a[4:7], v[108:111]\n\t"
    "ds_read_b64_tr_b16 a[46:47], v10 offset:3744\n\t"
    "s_waitcnt lgkmcnt(6)\n\t"
    "v_mfma_f32_16x16x32_bf16 v[112:115], a[56:59], a[0:3], v[112:115]\n\t"
    "ds_read_b64_tr_b16 a[48:49], v10 offset:14496\n\t"
    "v_mfma_f32_16x16x32_bf16 v[116:119], a[56:59], a[4:7], v[116:119]\n\t"
    "ds_read_b64_tr_b16 a[50:51], v10 offset:18080\n\t"
    "v_mfma_f32_16x16x32_bf16 v[120:123], a[32:35], a[0:3], v[120:123]\n\t"
    "ds_read_b64_tr_b16 a[52:53], v10 offset:192\n\t"
    "global_load_lds_dwordx4 v3, s[38:39] offset:-2048\n\t"
    "v_mfma_f32_16x16x32_bf16 v[124:127], a[32:35], a[4:7], v[124:127]\n\t"
    "ds_read_b64_tr_b16 a[54:55], v10 offset:3776\n\t"
    "s_waitcnt lgkmcnt(6)\n\t"
    "v_mfma_f32_16x16x32_bf16 v[128:131], a[36:39], a[0:3], v[128:131]\n\t"
    "ds_read_b64_tr_b16 a[56:57], v10 offset:14528\n\t"
    "v_mfma_f32_16x16x32_bf16 v[132:135], a[36:39], a[4:7], v[132:135]\n\t"
    "ds_read_b64_tr_b16 a[58:59], v10 offset:18112\n\t"
    "v_mfma_f32_16x16x32_bf16 v[136:139], a[40:43], a[0:3], v[136:139]\n\t"
    "ds_read_b64_tr_b16 a[32:33], v10 offset:7168\n\t"
    "v_mfma_f32_16x16x32_bf16 v[140:143], a[40:43], a[4:7], v[140:143]\n\t"
    "ds_read_b64_tr_b16 a[34:35], v10 offset:10752\n\t"
    "global_load_lds_dwordx4 v3, s[38:39] offset:-1024\n\t"
    "s_waitcnt lgkmcnt(6)\n\t"
    "v_mfma_f32_16x16x32_bf16 v[144:147], a[44:47], a[0:3], v[144:147]\n\t"
    "ds_read_b64_tr_b16 a[36:37], v10 offset:21504\n\t"
    "v_mfma_f32_16x16x32_bf16 v[148:151], a[44:47], a[4:7], v[148:151]\n\t"
    "ds_read_b64_tr_b16 a[38:39], v10 offset:25088\n\t"
    "v_mfma_f32_16x16x32_bf16 v[152:155], a[48:51], a[0:3], v[152:155]\n\t"
    "ds_read_b64_tr_b16 a[40:41], v10 offset:7200\n\t"
    "v_mfma_f32_16x16x32_bf16 v[156:159], a[48:51], a[4:7], v[156:159]\n\t"
    "ds_read_b64_tr_b16 a[42:43], v10 offset:10784\n\t"
    "s_waitcnt lgkmcnt(6)\n\t"
    "v_mfma_f32_16x16x32_bf16 v[160:163], a[52:55], a[0:3], v[160:163]\n\t"
    "ds_read_b64_tr_b16 a[44:45], v10 offset:21536\n\t"
    "global_load_lds_dwordx4 v3, s[38:39] offset:0\n\t"
    "v_mfma_f32_16x16x32_bf16 v[164:167], a[52:55], a[4:7], v[164:167]\n\t"
    "ds_read_b64_tr_b16 a[46:47], v10 offset:25120\n\t"
    "v_mfma_f32_16x16x32_bf16 v[168:171], a[56:59], a[0:3], v[168:171]\n\t"
    "ds_read_b64_tr_b16 a[48:49], v10 offset:7232\n\t"
    "v_mfma_f32_16x16x32_bf16 v[172:175], a[56:59], a[4:7], v[172:175]\n\t"
    "ds_read_b64_tr_b16 a[50:51], v10 offset:10816\n\t"
    "s_waitcnt vmcnt(8)\n\t"
    "s_waitcnt lgkmcnt(6)\n\t"
    "v_mfma_f32_16x16x32_bf16 v[64:67], a[32:35], a[8:11], v[64:67]\n\t"
    "ds_read_b64_tr_b16 a[52:53], v10 offset:21568\n\t"
    "global_load_dwordx4 a[0:3], v1, s[36:37] sc1\n\t"
    "v_mfma_f32_16x16x32_bf16 v[68:71], a[32:35], a[12:15], v[68:71]\n\t"
    "ds_read_b64_tr_b16 a[54:55], v10 offset:25152\n\t"
    "v_mfma_f32_16x16x32_bf16 v[72:75], a[36:39], a[8:11], v[72:75]\n\t"
    "ds_read_b64_tr_b16 a[56:57], v10 offset:7264\n\t"
    "v_mfma_f32_16x16x32_bf16 v[76:79], a[36:39], a[12:15], v[76:79]\n\t"
    "ds_read_b64_tr_b16 a[58:59], v10 offset:10848\n\t"
    "s_waitcnt lgkmcnt(6)\n\t"
    "v_mfma_f32_16x16x32_bf16 v[80:83], a[40:43], a[8:11], v[80:83]\n\t"
    "ds_read_b64_tr_b16 a[32:33], v10 offset:21600\n\t"
    "v_mfma_f32_16x16x32_bf16 v[84:87], a[40:43], a[12:15], v[84:87]\n\t"
    "ds_read_b64_tr_b16 a[34:35], v10 offset:25184\n\t"
    "global_load_dwordx4 a[4:7], v2, s[36:37] sc1\n\t"
    "v_mfma_f32_16x16x32_bf16 v[88:91], a[44:47], a[8:11], v[88:91]\n\t"
    "ds_read_b64_tr_b16 a[36:37], v10 offset:7296\n\t"
    "s_add_u32 s36, s36, 1024\n\t"
    "s_addc_u32 s37, s37, 0\n\t"
    "v_mfma_f32_16x16x32_bf16 v[92:95], a[44:47], a[12:15], v[92:95]\n\t"
    "ds_read_b64_tr_b16 a[38:39], v10 offset:10880\n\t"
    "s_waitcnt lgkmcnt(6)\n\t"
    "v_mfma_f32_16x16x32_bf16 v[96:99], a[48:51], a[8:11], v[96:99]\n\t"
    "ds_read_b64_tr_b16 a[40:41], v10 offset:21632\n\t"
    "v_mfma_f32_16x16x32_bf16 v[100:103], a[48:51], a[12:15], v[100:103]\n\t"
    "ds_read_b64_tr_b16 a[42:43], v10 offset:25216\n\t"
    "v_mfma_f32_16x16x32_bf16 v[104:107], a[52:55], a[8:11], v[104:107]\n\t"
    "ds_read_b64_tr_b16 a[44:45], v10 offset:7328\n\t"
    "global_load_lds_dwordx4 v3, s[38:39] offset:1024\n\t"
    "v_mfma_f32_16x16x32_bf16 v[108:111], a[52:55], a[12:15], v[108:111]\n\t"
    "ds_read_b64_tr_b16 a[46:47], v10 offset:10912\n\t"
    "s_waitcnt lgkmcnt(6)\n\t"
    "v_mfma_f32_16x16x32_bf16 v[112:115], a[56:59], a[8:11], v[112:115]\n\t"
    "ds_read_b64_tr_b16 a[48:49], v10 offset:21664\n\t"
    "v_mfma_f32_16x16x32_bf16 v[116:119], a[56:59], a[12:15], v[116:119]\n\t"
    "ds_read_b64_tr_b16 a[50:51], v10 offset:25248\n\t"
    "v_mfma_f32_16x16x32_bf16 v[120:123], a[32:35], a[8:11], v[120:123]\n\t"
    "ds_read_b64_tr_b16 a[52:53], v10 offset:7360\n\t"
    "global_load_lds_dwordx4 v3, s[38:39] offset:2048\n\t"
    "v_mfma_f32_16x16x32_bf16 v[124:127], a[32:35], a[12:15], v[124:127]\n\t"
    "ds_read_b64_tr_b16 a[54:55], v10 offset:10944\n\t"
    "s_waitcnt lgkmcnt(6)\n\t"
    "v_mfma_f32_16x16x32_bf16 v[128:131], a[36:39], a[8:11], v[128:131]\n\t"
    "ds_read_b64_tr_b16 a[56:57], v10 offset:21696\n\t"
    "v_mfma_f32_16x16x32_bf16 v[132:135], a[36:39], a[12:15], v[132:135]\n\t"
    "ds_read_b64_tr_b16 a[58:59], v10 offset:25280\n\t"
    "s_waitcnt vmcnt(10)\n\t"
    "s_barrier\n\t"
    "v_mfma_f32_16x16x32_bf16 v[136:139], a[40:43], a[8:11], v[136:139]\n\t"
    "ds_read_b64_tr_b16 a[32:33], v10 offset:28672\n\t"
    "v_mfma_f32_16x16x32_bf16 v[140:143], a[40:43], a[12:15], v[140:143]\n\t"
    "ds_read_b64_tr_b16 a[34:35], v10 offset:32256\n\t"
    "global_load_lds_dwordx4 v3, s[38:39] offset:3072\n\t"
    "s_waitcnt lgkmcnt(6)\n\t"
    "v_mfma_f32_16x16x32_bf16 v[144:147], a[44:47], a[8:11], v[144:147]\n\t"
    "ds_read_b64_tr_b16 a[36:37], v10 offset:43008\n\t"
    "v_mfma_f32_16x16x32_bf16 v[148:151], a[44:47], a[12:15], v[148:151]\n\t"
    "ds_read_b64_tr_b16 a[38:39], v10 offset:46592\n\t"
    "v_mfma_f32_16x16x32_bf16 v[152:155], a[48:51], a[8:11], v[152:155]\n\t"
    "ds_read_b64_tr_b16 a[40:41], v10 offset:28704\n\t"
    "v_mfma_f32_16x16x32_bf16 v[156:159], a[48:51], a[12:15], v[156:159]\n\t"
    "ds_read_b64_tr_b16 a[42:43], v10 offset:32288\n\t"
    "s_waitcnt lgkmcnt(6)\n\t"
    "v_mfma_f32_16x16x32_bf16 v[160:163], a[52:55], a[8:11], v[160:163]\n\t"
    "ds_read_b64_tr_b16 a[44:45], v10 offset:43040\n\t"
    "v_mfma_f32_16x16x32_bf16 v[164:167], a[52:55], a[12:15], v[164:167]\n\t"
    "ds_read_b64_tr_b16 a[46:47], v10 offset:46624\n\t"
    "v_mfma_f32_16x16x32_bf16 v[168:171], a[56:59], a[8:11], v[168:171]\n\t"
    "ds_read_b64_tr_b16 a[48:49], v10 offset:28736\n\t"
    "v_mfma_f32_16x16x32_bf16 v[172:175], a[56:59], a[12:15], v[172:175]\n\t"
    "ds_read_b64_tr_b16 a[50:51], v10 offset:32320\n\t"
    "s_add_u32 s38, s38, 14336\n\t"
    "s_addc_u32 s39, s39, 0\n\t"
    "s_waitcnt vmcnt(11)\n\t"
    "s_waitcnt lgkmcnt(6)\n\t"
    "v_mfma_f32_16x16x32_bf16 v[64:67], a[32:35], a[16:19], v[64:67]\n\t"
    "ds_read_b64_tr_b16 a[52:53], v10 offset:43072\n\t"
    "global_load_dwordx4 a[8:11], v1, s[36:37] sc1\n\t"
    "v_mfma_f32_16x16x32_bf16 v[68:71], a[32:35], a[20:23], v[68:71]\n\t"
    "ds_read_b64_tr_b16 a[54:55], v10 offset:46656\n\t"
    "v_mfma_f32_16x16x32_bf16 v[72:75], a[36:39], a[16:19], v[72:75]\n\t"
    "ds_read_b64_tr_b16 a[56:57], v10 offset:28768\n\t"
    "v_mfma_f32_16x16x32_bf16 v[76:79], a[36:39], a[20:23], v[76:79]\n\t"
    "ds_read_b64_tr_b16 a[58:59], v10 offset:32352\n\t"
    "s_waitcnt lgkmcnt(6)\n\t"
    "v_mfma_f32_16x16x32_bf16 v[80:83], a[40:43], a[16:19], v[80:83]\n\t"
    "ds_read_b64_tr_b16 a[32:33], v10 offset:43104\n\t"
    "v_mfma_f32_16x16x32_bf16 v[84:87], a[40:43], a[20:23], v[84:87]\n\t"
    "ds_read_b64_tr_b16 a[34:35], v10 offset:46688\n\t"
    "global_load_dwordx4 a[12:15], v2, s[36:37] sc1\n\t"
    "v_mfma_f32_16x16x32_bf16 v[88:91], a[44:47], a[16:19], v[88:91]\n\t"
    "ds_read_b64_tr_b16 a[36:37], v10 offset:28800\n\t"
    "s_add_u32 s36, s36, 1024\n\t"
    "s_addc_u32 s37, s37, 0\n\t"
    "v_mfma_f32_16x16x32_bf16 v[92:95], a[44:47], a[20:23], v[92:95]\n\t"
    "ds_read_b64_tr_b16 a[38:39], v10 offset:32384\n\t"
    "s_waitcnt lgkmcnt(6)\n\t"
    "v_mfma_f32_16x16x32_bf16 v[96:99], a[48:51], a[16:19], v[96:99]\n\t"
    "ds_read_b64_tr_b16 a[40:41], v10 offset:43136\n\t"
    "v_mfma_f32_16x16x32_bf16 v[100:103], a[48:51], a[20:23], v[100:103]\n\t"
    "ds_read_b64_tr_b16 a[42:43], v10 offset:46720\n\t"
    "v_mfma_f32_16x16x32_bf16 v[104:107], a[52:55], a[16:19], v[104:107]\n\t"
    "ds_read_b64_tr_b16 a[44:45], v10 offset:28832\n\t"
    "s_add_u32 m0, s53, 28672\n\t"
    "s_nop 0\n\t"
    "global_load_lds_dwordx4 v3, s[38:39] offset:-3072\n\t"
    "v_mfma_f32_16x16x32_bf16 v[108:111], a[52:55], a[20:23], v[108:111]\n\t"
    "ds_read_b64_tr_b16 a[46:47], v10 offset:32416\n\t"
    "s_waitcnt lgkmcnt(6)\n\t"
    "v_mfma_f32_16x16x32_bf16 v[112:115], a[56:59], a[16:19], v[112:115]\n\t"
    "ds_read_b64_tr_b16 a[48:49], v10 offset:43168\n\t"
    "v_mfma_f32_16x16x32_bf16 v[116:119], a[56:59], a[20:23], v[116:119]\n\t"
    "ds_read_b64_tr_b16 a[50:51], v10 offset:46752\n\t"
    "v_mfma_f32_16x16x32_bf16 v[120:123], a[32:35], a[16:19], v[120:123]\n\t"
    "ds_read_b64_tr_b16 a[52:53], v10 offset:28864\n\t"
    "global_load_lds_dwordx4 v3, s[38:39] offset:-2048\n\t"
    "v_mfma_f32_16x16x32_bf16 v[124:127], a[32:35], a[20:23], v[124:127]\n\t"
    "ds_read_b64_tr_b16 a[54:55], v10 offset:32448\n\t"
    "s_waitcnt lgkmcnt(6)\n\t"
    "v_mfma_f32_16x16x32_bf16 v[128:131], a[36:39], a[16:19], v[128:131]\n\t"
    "ds_read_b64_tr_b16 a[56:57], v10 offset:43200\n\t"
    "v_mfma_f32_16x16x32_bf16 v[132:135], a[36:39], a[20:23], v[132:135]\n\t"
    "ds_read_b64_tr_b16 a[58:59], v10 offset:46784\n\t"
    "v_mfma_f32_16x16x32_bf16 v[136:139], a[40:43], a[16:19], v[136:139]\n\t"
    "ds_read_b64_tr_b16 a[32:33], v10 offset:35840\n\t"
    "v_mfma_f32_16x16x32_bf16 v[140:143], a[40:43], a[20:23], v[140:143]\n\t"
    "ds_read_b64_tr_b16 a[34:35], v10 offset:39424\n\t"
    "global_load_lds_dwordx4 v3, s[38:39] offset:-1024\n\t"
    "s_waitcnt lgkmcnt(6)\n\t"
    "v_mfma_f32_16x16x32_bf16 v[144:147], a[44:47], a[16:19], v[144:147]\n\t"
    "ds_read_b64_tr_b16 a[36:37], v10 offset:50176\n\t"
    "v_mfma_f32_16x16x32_bf16 v[148:151], a[44:47], a[20:23], v[148:151]\n\t"
    "ds_read_b64_tr_b16 a[38:39], v10 offset:53760\n\t"
    "v_mfma_f32_16x16x32_bf16 v[152:155], a[48:51], a[16:19], v[152:155]\n\t"
    "ds_read_b64_tr_b16 a[40:41], v10 offset:35872\n\t"
    "v_mfma_f32_16x16x32_bf16 v[156:159], a[48:51], a[20:23], v[156:159]\n\t"
    "ds_read_b64_tr_b16 a[42:43], v10 offset:39456\n\t"
    "s_waitcnt lgkmcnt(6)\n\t"
    "v_mfma_f32_16x16x32_bf16 v[160:163], a[52:55], a[16:19], v[160:163]\n\t"
    "ds_read_b64_tr_b16 a[44:45], v10 offset:50208\n\t"
    "global_load_lds_dwordx4 v3, s[38:39] offset:0\n\t"
    "v_mfma_f32_16x16x32_bf16 v[164:167], a[52:55], a[20:23], v[164:167]\n\t"
    "ds_read_b64_tr_b16 a[46:47], v10 offset:53792\n\t"
    "v_mfma_f32_16x16x32_bf16 v[168:171], a[56:59], a[16:19], v[168:171]\n\t"
    "ds_read_b64_tr_b16 a[48:49], v10 offset:35904\n\t"
    "v_mfma_f32_16x16x32_bf16 v[172:175], a[56:59], a[20:23], v[172:175]\n\t"
    "ds_read_b64_tr_b16 a[50:51], v10 offset:39488\n\t"
    "s_waitcnt vmcnt(15)\n\t"
    "s_waitcnt lgkmcnt(6)\n\t"
    "v_mfma_f32_16x16x32_bf16 v[64:67], a[32:35], a[24:27], v[64:67]\n\t"
    "ds_read_b64_tr_b16 a[52:53], v10 offset:50240\n\t"
    "global_load_dwordx4 a[16:19], v1, s[36:37] sc1\n\t"
    "v_mfma_f32_16x16x32_bf16 v[68:71], a[32:35], a[28:31], v[68:71]\n\t"
    "ds_read_b64_tr_b16 a[54:55], v10 offset:53824\n\t"
    "v_mfma_f32_16x16x32_bf16 v[72:75], a[36:39], a[24:27], v[72:75]\n\t"
    "ds_read_b64_tr_b16 a[56:57], v10 offset:35936\n\t"
    "v_mfma_f32_16x16x32_bf16 v[76:79], a[36:39], a[28:31], v[76:79]\n\t"
    "ds_read_b64_tr_b16 a[58:59], v10 offset:39520\n\t"
    "s_waitcnt lgkmcnt(6)\n\t"
    "v_mfma_f32_16x16x32_bf16 v[80:83], a[40:43], a[24:27], v[80:83]\n\t"
    "ds_read_b64_tr_b16 a[32:33], v10 offset:50272\n\t"
    "v_mfma_f32_16x16x32_bf16 v[84:87], a[40:43], a[28:31], v[84:87]\n\t"
    "ds_read_b64_tr_b16 a[34:35], v10 offset:53856\n\t"
    "global_load_dwordx4 a[20:23], v2, s[36:37] sc1\n\t"
    "v_mfma_f32_16x16x32_bf16 v[88:91], a[44:47], a[24:27], v[88:91]\n\t"
    "ds_read_b64_tr_b16 a[36:37], v10 offset:35968\n\t"
    "s_add_u32 s36, s36, 1024\n\t"
    "s_addc_u32 s37, s37, 0\n\t"
    "v_mfma_f32_16x16x32_bf16 v[92:95], a[44:47], a[28:31], v[92:95]\n\t"
    "ds_read_b64_tr_b16 a[38:39], v10 offset:39552\n\t"
    "s_waitcnt lgkmcnt(6)\n\t"
    "v_mfma_f32_16x16x32_bf16 v[96:99], a[48:51], a[24:27], v[96:99]\n\t"
    "ds_read_b64_tr_b16 a[40:41], v10 offset:50304\n\t"
    "v_mfma_f32_16x16x32_bf16 v[100:103], a[48:51], a[28:31], v[100:103]\n\t"
    "ds_read_b64_tr_b16 a[42:43], v10 offset:53888\n\t"
    "v_mfma_f32_16x16x32_bf16 v[104:107], a[52:55], a[24:27], v[104:107]\n\t"
    "ds_read_b64_tr_b16 a[44:45], v10 offset:36000\n\t"
    "global_load_lds_dwordx4 v3, s[38:39] offset:1024\n\t"
    "v_mfma_f32_16x16x32_bf16 v[108:111], a[52:55], a[28:31], v[108:111]\n\t"
    "ds_read_b64_tr_b16 a[46:47], v10 offset:39584\n\t"
    "s_waitcnt lgkmcnt(6)\n\t"
    "v_mfma_f32_16x16x32_bf16 v[112:115], a[56:59], a[24:27], v[112:115]\n\t"
    "ds_read_b64_tr_b16 a[48:49], v10 offset:50336\n\t"
    "v_mfma_f32_16x16x32_bf16 v[116:119], a[56:59], a[28:31], v[116:119]\n\t"
    "ds_read_b64_tr_b16 a[50:51], v10 offset:53920\n\t"
    "v_mfma_f32_16x16x32_bf16 v[120:123], a[32:35], a[24:27], v[120:123]\n\t"
    "ds_read_b64_tr_b16 a[52:53], v10 offset:36032\n\t"
    "global_load_lds_dwordx4 v3, s[38:39] offset:2048\n\t"
    "v_mfma_f32_16x16x32_bf16 v[124:127], a[32:35], a[28:31], v[124:127]\n\t"
    "ds_read_b64_tr_b16 a[54:55], v10 offset:39616\n\t"
    "s_waitcnt lgkmcnt(6)\n\t"
    "v_mfma_f32_16x16x32_bf16 v[128:131], a[36:39], a[24:27], v[128:131]\n\t"
    "ds_read_b64_tr_b16 a[56:57], v10 offset:50368\n\t"
    "v_mfma_f32_16x16x32_bf16 v[132:135], a[36:39], a[28:31], v[132:135]\n\t"
    "ds_read_b64_tr_b16 a[58:59], v10 offset:53952\n\t"
    "v_add_u32 v10, s54, v10\n\t"
    "s_sub_u32 s54, 0, s54\n\t"
    "s_waitcnt vmcnt(10)\n\t"
    "s_barrier\n\t"
    "v_mfma_f32_16x16x32_bf16 v[136:139], a[40:43], a[24:27], v[136:139]\n\t"
    "ds_read_b64_tr_b16 a[32:33], v10 offset:0\n\t"
    "v_mfma_f32_16x16x32_bf16 v[140:143], a[40:43], a[28:31], v[140:143]\n\t"
    "ds_read_b64_tr_b16 a[34:35], v10 offset:3584\n\t"
    "global_load_lds_dwordx4 v3, s[38:39] offset:3072\n\t"
    "s_waitcnt lgkmcnt(6)\n\t"
    "v_mfma_f32_16x16x32_bf16 v[144:147], a[44:47], a[24:27], v[144:147]\n\t"
    "ds_read_b64_tr_b16 a[36:37], v10 offset:14336\n\t"
    "v_mfma_f32_16x16x32_bf16 v[148:151], a[44:47], a[28:31], v[148:151]\n\t"
    "ds_read_b64_tr_b16 a[38:39], v10 offset:17920\n\t"
    "v_mfma_f32_16x16x32_bf16 v[152:155], a[48:51], a[24:27], v[152:155]\n\t"
    "ds_read_b64_tr_b16 a[40:41], v10 offset:32\n\t"
    "v_mfma_f32_16x16x32_bf16 v[156:159], a[48:51], a[28:31], v[156:159]\n\t"
    "ds_read_b64_tr_b16 a[42:43], v10 offset:3616\n\t"
    "s_waitcnt lgkmcnt(6)\n\t"
    "v_mfma_f32_16x16x32_bf16 v[160:163], a[52:55], a[24:27], v[160:163]\n\t"
    "ds_read_b64_tr_b16 a[44:45], v10 offset:14368\n\t"
    "v_mfma_f32_16x16x32_bf16 v[164:167], a[52:55], a[28:31], v[164:167]\n\t"
    "ds_read_b64_tr_b16 a[46:47], v10 offset:17952\n\t"
    "v_mfma_f32_16x16x32_bf16 v[168:171], a[56:59], a[24:27], v[168:171]\n\t"
    "ds_read_b64_tr_b16 a[48:49], v10 offset:64\n\t"
    "v_mfma_f32_16x16x32_bf16 v[172:175], a[56:59], a[28:31], v[172:175]\n\t"
    "ds_read_b64_tr_b16 a[50:51], v10 offset:3648\n\t"
    "s_add_u32 s38, s38, 14336\n\t"
    "s_addc_u32 s39, s39, 0\n\t"
    "s_add_u32 s53, s53, s54\n\t"
    "s_sub_u32 s52, s52, 1\n\t"
    "s_cmp_lg_u32 s52, 0\n\t"
    "s_cbranch_scc1 LM_22_%=\n\t"
    "s_waitcnt lgkmcnt(6)\n\t"
    "v_mfma_f32_16x16x32_bf16 v[64:67], a[32:35], a[0:3], v[64:67]\n\t"
    "ds_read_b64_tr_b16 a[52:53], v10 offset:14400\n\t"
    "global_load_dwordx4 a[24:27], v1, s[36:37] sc1\n\t"
    "v_mfma_f32_16x16x32_bf16 v[68:71], a[32:35], a[4:7], v[68:71]\n\t"
    "ds_read_b64_tr_b16 a[54:55], v10 offset:17984\n\t"
    "v_mfma_f32_16x16x32_bf16 v[72:75], a[36:39], a[0:3], v[72:75]\n\t"
    "ds_read_b64_tr_b16 a[56:57], v10 offset:96\n\t"
    "v_mfma_f32_16x16x32_bf16 v[76:79], a[36:39], a[4:7], v[76:79]\n\t"
    "ds_read_b64_tr_b16 a[58:59], v10 offset:3680\n\t"
    "s_waitcnt lgkmcnt(6)\n\t"
    "v_mfma_f32_16x16x32_bf16 v[80:83], a[40:43], a[0:3], v[80:83]\n\t"
    "ds_read_b64_tr_b16 a[32:33], v10 offset:14432\n\t"
    "v_mfma_f32_16x16x32_bf16 v[84:87], a[40:43], a[4:7], v[84:87]\n\t"
    "ds_read_b64_tr_b16 a[34:35], v10 offset:18016\n\t"
    "global_load_dwordx4 a[28:31], v2, s[36:37] sc1\n\t"
    "v_mfma_f32_16x16x32_bf16 v[88:91], a[44:47], a[0:3], v[88:91]\n\t"
    "ds_read_b64_tr_b16 a[36:37], v10 offset:128\n\t"
    "s_add_u32 s36, s36, 1024\n\t"
    "s_addc_u32 s37, s37, 0\n\t"
    "v_mfma_f32_16x16x32_bf16 v[92:95], a[44:47], a[4:7], v[92:95]\n\t"
    "ds_read_b64_tr_b16 a[38:39], v10 offset:3712\n\t"
    "s_waitcnt lgkmcnt(6)\n\t"
    "v_mfma_f32_16x16x32_bf16 v[96:99], a[48:51], a[0:3], v[96:99]\n\t"
    "ds_read_b64_tr_b16 a[40:41], v10 offset:14464\n\t"
    "v_mfma_f32_16x16x32_bf16 v[100:103], a[48:51], a[4:7], v[100:103]\n\t"
    "ds_read_b64_tr_b16 a[42:43], v10 offset:18048\n\t"
    "v_mfma_f32_16x16x32_bf16 v[104:107], a[52:55], a[0:3], v[104:107]\n\t"
    "ds_read_b64_tr_b16 a[44:45], v10 offset:160\n\t"
    "s_mov_b32 m0, s47\n\t"
    "s_nop 0\n\t"
    "global_load_lds_dwordx4 v13, s[42:43]\n\t"
    "v_mfma_f32_16x16x32_bf16 v[108:111], a[52:55], a[4:7], v[108:111]\n\t"
    "ds_read_b64_tr_b16 a[46:47], v10 offset:3744\n\t"
    "s_waitcnt lgkmcnt(6)\n\t"
    "v_mfma_f32_16x16x32_bf16 v[112:115], a[56:59], a[0:3], v[112:115]\n\t"
    "ds_read_b64_tr_b16 a[48:49], v10 offset:14496\n\t"
    "v_mfma_f32_16x16x32_bf16 v[116:119], a[56:59], a[4:7], v[116:119]\n\t"
    "ds_read_b64_tr_b16 a[50:51], v10 offset:18080\n\t"
    "v_mfma_f32_16x16x32_bf16 v[120:123], a[32:35], a[0:3], v[120:123]\n\t"
    "ds_read_b64_tr_b16 a[52:53], v10 offset:192\n\t"
    "v_mfma_f32_16x16x32_bf16 v[124:127], a[32:35], a[4:7], v[124:127]\n\t"
    "ds_read_b64_tr_b16 a[54:55], v10 offset:3776\n\t"
    "s_waitcnt lgkmcnt(6)\n\t"
    "v_mfma_f32_16x16x32_bf16 v[128:131], a[36:39], a[0:3], v[128:131]\n\t"
    "ds_read_b64_tr_b16 a[56:57], v10 offset:14528\n\t"
    "v_mfma_f32_16x16x32_bf16 v[132:135], a[36:39], a[4:7], v[132:135]\n\t"
    "ds_read_b64_tr_b16 a[58:59], v10 offset:18112\n\t"
    "v_mfma_f32_16x16x32_bf16 v[136:139], a[40:43], a[0:3], v[136:139]\n\t"
    "ds_read_b64_tr_b16 a[32:33], v10 offset:7168\n\t"
    "v_mfma_f32_16x16x32_bf16 v[140:143], a[40:43], a[4:7], v[140:143]\n\t"
    "ds_read_b64_tr_b16 a[34:35], v10 offset:10752\n\t"
    "s_waitcnt lgkmcnt(6)\n\t"
    "v_mfma_f32_16x16x32_bf16 v[144:147], a[44:47], a[0:3], v[144:147]\n\t"
    "ds_read_b64_tr_b16 a[36:37], v10 offset:21504\n\t"
    "v_mfma_f32_16x16x32_bf16 v[148:151], a[44:47], a[4:7], v[148:151]\n\t"
    "ds_read_b64_tr_b16 a[38:39], v10 offset:25088\n\t"
    "v_mfma_f32_16x16x32_bf16 v[152:155], a[48:51], a[0:3], v[152:155]\n\t"
    "ds_read_b64_tr_b16 a[40:41], v10 offset:7200\n\t"
    "v_mfma_f32_16x16x32_bf16 v[156:159], a[48:51], a[4:7], v[156:159]\n\t"
    "ds_read_b64_tr_b16 a[42:43], v10 offset:10784\n\t"
    "s_waitcnt lgkmcnt(6)\n\t"
    "v_mfma_f32_16x16x32_bf16 v[160:163], a[52:55], a[0:3], v[160:163]\n\t"
    "ds_read_b64_tr_b16 a[44:45], v10 offset:21536\n\t"
    "v_mfma_f32_16x16x32_bf16 v[164:167], a[52:55], a[4:7], v[164:167]\n\t"
    "ds_read_b64_tr_b16 a[46:47], v10 offset:25120\n\t"
    "v_mfma_f32_16x16x32_bf16 v[168:171], a[56:59], a[0:3], v[168:171]\n\t"
    "ds_read_b64_tr_b16 a[48:49], v10 offset:7232\n\t"
    "v_mfma_f32_16x16x32_bf16 v[172:175], a[56:59], a[4:7], v[172:175]\n\t"
    "ds_read_b64_tr_b16 a[50:51], v10 offset:10816\n\t"
    "s_waitcnt vmcnt(12)\n\t"
    "s_waitcnt lgkmcnt(6)\n\t"
    "v_mfma_f32_16x16x32_bf16 v[64:67], a[32:35], a[8:11], v[64:67]\n\t"
    "ds_read_b64_tr_b16 a[52:53], v10 offset:21568\n\t"
    "v_mfma_f32_16x16x32_bf16 v[68:71], a[32:35], a[12:15], v[68:71]\n\t"
    "ds_read_b64_tr_b16 a[54:55], v10 offset:25152\n\t"
    "v_mfma_f32_16x16x32_bf16 v[72:75], a[36:39], a[8:11], v[72:75]\n\t"
    "ds_read_b64_tr_b16 a[56:57], v10 offset:7264\n\t"
    "v_mfma_f32_16x16x32_bf16 v[76:79], a[36:39], a[12:15], v[76:79]\n\t"
    "ds_read_b64_tr_b16 a[58:59], v10 offset:10848\n\t"
    "s_waitcnt lgkmcnt(6)\n\t"
    "v_mfma_f32_16x16x32_bf16 v[80:83], a[40:43], a[8:11], v[80:83]\n\t"
    "ds_read_b64_tr_b16 a[32:33], v10 offset:21600\n\t"
    "v_mfma_f32_16x16x32_bf16 v[84:87], a[40:43], a[12:15], v[84:87]\n\t"
    "ds_read_b64_tr_b16 a[34:35], v10 offset:25184\n\t"
    "v_mfma_f32_16x16x32_bf16 v[88:91], a[44:47], a[8:11], v[88:91]\n\t"
    "ds_read_b64_tr_b16 a[36:37], v10 offset:7296\n\t"
    "v_mfma_f32_16x16x32_bf16 v[92:95], a[44:47], a[12:15], v[92:95]\n\t"
    "ds_read_b64_tr_b16 a[38:39], v10 offset:10880\n\t"
    "s_waitcnt lgkmcnt(6)\n\t"
    "v_mfma_f32_16x16x32_bf16 v[96:99], a[48:51], a[8:11], v[96:99]\n\t"
    "ds_read_b64_tr_b16 a[40:41], v10 offset:21632\n\t"
    "v_mfma_f32_16x16x32_bf16 v[100:103], a[48:51], a[12:15], v[100:103]\n\t"
    "ds_read_b64_tr_b16 a[42:43], v10 offset:25216\n\t"
    "v_mfma_f32_16x16x32_bf16 v[104:107], a[52:55], a[8:11], v[104:107]\n\t"
    "ds_read_b64_tr_b16 a[44:45], v10 offset:7328\n\t"
    "v_mfma_f32_16x16x32_bf16 v[108:111], a[52:55], a[12:15], v[108:111]\n\t"
    "ds_read_b64_tr_b16 a[46:47], v10 offset:10912\n\t"
    "s_waitcnt lgkmcnt(6)\n\t"
    "v_mfma_f32_16x16x32_bf16 v[112:115], a[56:59], a[8:11], v[112:115]\n\t"
    "ds_read_b64_tr_b16 a[48:49], v10 offset:21664\n\t"
    "v_mfma_f32_16x16x32_bf16 v[116:119], a[56:59], a[12:15], v[116:119]\n\t"
    "ds_read_b64_tr_b16 a[50:51], v10 offset:25248\n\t"
    "v_mfma_f32_16x16x32_bf16 v[120:123], a[32:35], a[8:11], v[120:123]\n\t"
    "ds_read_b64_tr_b16 a[52:53], v10 offset:7360\n\t"
    "v_mfma_f32_16x16x32_bf16 v[124:127], a[32:35], a[12:15], v[124:127]\n\t"
    "ds_read_b64_tr_b16 a[54:55], v10 offset:10944\n\t"
    "s_waitcnt lgkmcnt(6)\n\t"
    "v_mfma_f32_16x16x32_bf16 v[128:131], a[36:39], a[8:11], v[128:131]\n\t"
    "ds_read_b64_tr_b16 a[56:57], v10 offset:21696\n\t"
    "v_mfma_f32_16x16x32_bf16 v[132:135], a[36:39], a[12:15], v[132:135]\n\t"
    "ds_read_b64_tr_b16 a[58:59], v10 offset:25280\n\t"
    "s_waitcnt vmcnt(0)\n\t"
    "s_barrier\n\t"
    "v_mfma_f32_16x16x32_bf16 v[136:139], a[40:43], a[8:11], v[136:139]\n\t"
    "ds_read_b64_tr_b16 a[32:33], v10 offset:28672\n\t"
    "v_mfma_f32_16x16x32_bf16 v[140:143], a[40:43], a[12:15], v[140:143]\n\t"
    "ds_read_b64_tr_b16 a[34:35], v10 offset:32256\n\t"
    "s_waitcnt lgkmcnt(6)\n\t"
    "v_mfma_f32_16x16x32_bf16 v[144:147], a[44:47], a[8:11], v[144:147]\n\t"
    "ds_read_b64_tr_b16 a[36:37], v10 offset:43008\n\t"
    "v_mfma_f32_16x16x32_bf16 v[148:151], a[44:47], a[12:15], v[148:151]\n\t"
    "ds_read_b64_tr_b16 a[38:39], v10 offset:46592\n\t"
    "v_mfma_f32_16x16x32_bf16 v[152:155], a[48:51], a[8:11], v[152:155]\n\t"
    "ds_read_b64_tr_b16 a[40:41], v10 offset:28704\n\t"
    "v_mfma_f32_16x16x32_bf16 v[156:159], a[48:51], a[12:15], v[156:159]\n\t"
    "ds_read_b64_tr_b16 a[42:43], v10 offset:32288\n\t"
    "s_waitcnt lgkmcnt(6)\n\t"
    "v_mfma_f32_16x16x32_bf16 v[160:163], a[52:55], a[8:11], v[160:163]\n\t"
    "ds_read_b64_tr_b16 a[44:45], v10 offset:43040\n\t"
    "v_mfma_f32_16x16x32_bf16 v[164:167], a[52:55], a[12:15], v[164:167]\n\t"
    "ds_read_b64_tr_b16 a[46:47], v10 offset:46624\n\t"
    "v_mfma_f32_16x16x32_bf16 v[168:171], a[56:59], a[8:11], v[168:171]\n\t"
    "ds_read_b64_tr_b16 a[48:49], v10 offset:28736\n\t"
    "v_mfma_f32_16x16x32_bf16 v[172:175], a[56:59], a[12:15], v[172:175]\n\t"
    "ds_read_b64_tr_b16 a[50:51], v10 offset:32320\n\t"
    "s_waitcnt lgkmcnt(6)\n\t"
    "v_mfma_f32_16x16x32_bf16 v[64:67], a[32:35], a[16:19], v[64:67]\n\t"
    "ds_read_b64_tr_b16 a[52:53], v10 offset:43072\n\t"
    "v_mfma_f32_16x16x32_bf16 v[68:71], a[32:35], a[20:23], v[68:71]\n\t"
    "ds_read_b64_tr_b16 a[54:55], v10 offset:46656\n\t"
    "v_mfma_f32_16x16x32_bf16 v[72:75], a[36:39], a[16:19], v[72:75]\n\t"
    "ds_read_b64_tr_b16 a[56:57], v10 offset:28768\n\t"
    "v_mfma_f32_16x16x32_bf16 v[76:79], a[36:39], a[20:23], v[76:79]\n\t"
    "ds_read_b64_tr_b16 a[58:59], v10 offset:32352\n\t"
    "s_waitcnt lgkmcnt(6)\n\t"
    "v_mfma_f32_16x16x32_bf16 v[80:83], a[40:43], a[16:19], v[80:83]\n\t"
    "ds_read_b64_tr_b16 a[32:33], v10 offset:43104\n\t"
    "v_mfma_f32_16x16x32_bf16 v[84:87], a[40:43], a[20:23], v[84:87]\n\t"
    "ds_read_b64_tr_b16 a[34:35], v10 offset:46688\n\t"
    "v_mfma_f32_16x16x32_bf16 v[88:91], a[44:47], a[16:19], v[88:91]\n\t"
    "ds_read_b64_tr_b16 a[36:37], v10 offset:28800\n\t"
    "v_mfma_f32_16x16x32_bf16 v[92:95], a[44:47], a[20:23], v[92:95]\n\t"
    "ds_read_b64_tr_b16 a[38:39], v10 offset:32384\n\t"
    "s_waitcnt lgkmcnt(6)\n\t"
    "v_mfma_f32_16x16x32_bf16 v[96:99], a[48:51], a[16:19], v[96:99]\n\t"
    "ds_read_b64_tr_b16 a[40:41], v10 offset:43136\n\t"
    "v_mfma_f32_16x16x32_bf16 v[100:103], a[48:51], a[20:23], v[100:103]\n\t"
    "ds_read_b64_tr_b16 a[42:43], v10 offset:46720\n\t"
    "v_mfma_f32_16x16x32_bf16 v[104:107], a[52:55], a[16:19], v[104:107]\n\t"
    "ds_read_b64_tr_b16 a[44:45], v10 offset:28832\n\t"
    "v_mfma_f32_16x16x32_bf16 v[108:111], a[52:55], a[20:23], v[108:111]\n\t"
    "ds_read_b64_tr_b16 a[46:47], v10 offset:32416\n\t"
    "s_waitcnt lgkmcnt(6)\n\t"
    "v_mfma_f32_16x16x32_bf16 v[112:115], a[56:59], a[16:19], v[112:115]\n\t"
    "ds_read_b64_tr_b16 a[48:49], v10 offset:43168\n\t"
    "v_mfma_f32_16x16x32_bf16 v[116:119], a[56:59], a[20:23], v[116:119]\n\t"
    "ds_read_b64_tr_b16 a[50:51], v10 offset:46752\n\t"
    "v_mfma_f32_16x16x32_bf16 v[120:123], a[32:35], a[16:19], v[120:123]\n\t"
    "ds_read_b64_tr_b16 a[52:53], v10 offset:28864\n\t"
    "v_mfma_f32_16x16x32_bf16 v[124:127], a[32:35], a[20:23], v[124:127]\n\t"
    "ds_read_b64_tr_b16 a[54:55], v10 offset:32448\n\t"
    "s_waitcnt lgkmcnt(6)\n\t"
    "v_mfma_f32_16x16x32_bf16 v[128:131], a[36:39], a[16:19], v[128:131]\n\t"
    "ds_read_b64_tr_b16 a[56:57], v10 offset:43200\n\t"
    "v_mfma_f32_16x16x32_bf16 v[132:135], a[36:39], a[20:23], v[132:135]\n\t"
    "ds_read_b64_tr_b16 a[58:59], v10 offset:46784\n\t"
    "v_mfma_f32_16x16x32_bf16 v[136:139], a[40:43], a[16:19], v[136:139]\n\t"
    "ds_read_b64_tr_b16 a[32:33], v10 offset:35840\n\t"
    "v_mfma_f32_16x16x32_bf16 v[140:143], a[40:43], a[20:23], v[140:143]\n\t"
    "ds_read_b64_tr_b16 a[34:35], v10 offset:39424\n\t"
    "s_waitcnt lgkmcnt(6)\n\t"
    "v_mfma_f32_16x16x32_bf16 v[144:147], a[44:47], a[16:19], v[144:147]\n\t"
    "ds_read_b64_tr_b16 a[36:37], v10 offset:50176\n\t"
    "v_mfma_f32_16x16x32_bf16 v[148:151], a[44:47], a[20:23], v[148:151]\n\t"
    "ds_read_b64_tr_b16 a[38:39], v10 offset:53760\n\t"
    "v_mfma_f32_16x16x32_bf16 v[152:155], a[48:51], a[16:19], v[152:155]\n\t"
    "ds_read_b64_tr_b16 a[40:41], v10 offset:35872\n\t"
    "v_mfma_f32_16x16x32_bf16 v[156:159], a[48:51], a[20:23], v[156:159]\n\t"
    "ds_read_b64_tr_b16 a[42:43], v10 offset:39456\n\t"
    "s_waitcnt lgkmcnt(6)\n\t"
    "v_mfma_f32_16x16x32_bf16 v[160:163], a[52:55], a[16:19], v[160:163]\n\t"
    "ds_read_b64_tr_b16 a[44:45], v10 offset:50208\n\t"
    "v_mfma_f32_16x16x32_bf16 v[164:167], a[52:55], a[20:23], v[164:167]\n\t"
    "ds_read_b64_tr_b16 a[46:47], v10 offset:53792\n\t"
    "v_mfma_f32_16x16x32_bf16 v[168:171], a[56:59], a[16:19], v[168:171]\n\t"
    "ds_read_b64_tr_b16 a[48:49], v10 offset:35904\n\t"
    "v_mfma_f32_16x16x32_bf16 v[172:175], a[56:59], a[20:23], v[172:175]\n\t"
    "ds_read_b64_tr_b16 a[50:51], v10 offset:39488\n\t"
    "s_waitcnt lgkmcnt(6)\n\t"
    "v_mfma_f32_16x16x32_bf16 v[64:67], a[32:35], a[24:27], v[64:67]\n\t"
    "ds_read_b64_tr_b16 a[52:53], v10 offset:50240\n\t"
    "v_mfma_f32_16x16x32_bf16 v[68:71], a[32:35], a[28:31], v[68:71]\n\t"
    "ds_read_b64_tr_b16 a[54:55], v10 offset:53824\n\t"
    "v_mfma_f32_16x16x32_bf16 v[72:75], a[36:39], a[24:27], v[72:75]\n\t"
    "ds_read_b64_tr_b16 a[56:57], v10 offset:35936\n\t"
    "v_mfma_f32_16x16x32_bf16 v[76:79], a[36:39], a[28:31], v[76:79]\n\t"
    "ds_read_b64_tr_b16 a[58:59], v10 offset:39520\n\t"
    "s_waitcnt lgkmcnt(6)\n\t"
    "v_mfma_f32_16x16x32_bf16 v[80:83], a[40:43], a[24:27], v[80:83]\n\t"
    "ds_read_b64_tr_b16 a[32:33], v10 offset:50272\n\t"
    "v_mfma_f32_16x16x32_bf16 v[84:87], a[40:43], a[28:31], v[84:87]\n\t"
    "ds_read_b64_tr_b16 a[34:35], v10 offset:53856\n\t"
    "v_mfma_f32_16x16x32_bf16 v[88:91], a[44:47], a[24:27], v[88:91]\n\t"
    "ds_read_b64_tr_b16 a[36:37], v10 offset:35968\n\t"
    "v_mfma_f32_16x16x32_bf16 v[92:95], a[44:47], a[28:31], v[92:95]\n\t"
    "ds_read_b64_tr_b16 a[38:39], v10 offset:39552\n\t"
    "s_waitcnt lgkmcnt(6)\n\t"
    "v_mfma_f32_16x16x32_bf16 v[96:99], a[48:51], a[24:27], v[96:99]\n\t"
    "ds_read_b64_tr_b16 a[40:41], v10 offset:50304\n\t"
    "v_mfma_f32_16x16x32_bf16 v[100:103], a[48:51], a[28:31], v[100:103]\n\t"
    "ds_read_b64_tr_b16 a[42:43], v10 offset:53888\n\t"
    "v_mfma_f32_16x16x32_bf16 v[104:107], a[52:55], a[24:27], v[104:107]\n\t"
    "ds_read_b64_tr_b16 a[44:45], v10 offset:36000\n\t"
    "v_mfma_f32_16x16x32_bf16 v[108:111], a[52:55], a[28:31], v[108:111]\n\t"
    "ds_read_b64_tr_b16 a[46:47], v10 offset:39584\n\t"
    "s_waitcnt lgkmcnt(6)\n\t"
    "v_mfma_f32_16x16x32_bf16 v[112:115], a[56:59], a[24:27], v[112:115]\n\t"
    "ds_read_b64_tr_b16 a[48:49], v10 offset:50336\n\t"
    "v_mfma_f32_16x16x32_bf16 v[116:119], a[56:59], a[28:31], v[116:119]\n\t"
    "ds_read_b64_tr_b16 a[50:51], v10 offset:53920\n\t"
    "v_mfma_f32_16x16x32_bf16 v[120:123], a[32:35], a[24:27], v[120:123]\n\t"
    "ds_read_b64_tr_b16 a[52:53], v10 offset:36032\n\t"
    "v_mfma_f32_16x16x32_bf16 v[124:127], a[32:35], a[28:31], v[124:127]\n\t"
    "ds_read_b64_tr_b16 a[54:55], v10 offset:39616\n\t"
    "s_waitcnt lgkmcnt(6)\n\t"
    "v_mfma_f32_16x16x32_bf16 v[128:131], a[36:39], a[24:27], v[128:131]\n\t"
    "ds_read_b64_tr_b16 a[56:57], v10 offset:50368\n\t"
    "v_mfma_f32_16x16x32_bf16 v[132:135], a[36:39], a[28:31], v[132:135]\n\t"
    "ds_read_b64_tr_b16 a[58:59], v10 offset:53952\n\t"
    "v_mfma_f32_16x16x32_bf16 v[136:139], a[40:43], a[24:27], v[136:139]\n\t"
    "v_mfma_f32_16x16x32_bf16 v[140:143], a[40:43], a[28:31], v[140:143]\n\t"
    "s_waitcnt lgkmcnt(4)\n\t"
    "v_mfma_f32_16x16x32_bf16 v[144:147], a[44:47], a[24:27], v[144:147]\n\t"
    "v_mfma_f32_16x16x32_bf16 v[148:151], a[44:47], a[28:31], v[148:151]\n\t"
    "v_mfma_f32_16x16x32_bf16 v[152:155], a[48:51], a[24:27], v[152:155]\n\t"
    "v_mfma_f32_16x16x32_bf16 v[156:159], a[48:51], a[28:31], v[156:159]\n\t"
    "s_waitcnt lgkmcnt(0)\n\t"
    "v_mfma_f32_16x16x32_bf16 v[160:163], a[52:55], a[24:27], v[160:163]\n\t"
    "v_mfma_f32_16x16x32_bf16 v[164:167], a[52:55], a[28:31], v[164:167]\n\t"
    "v_mfma_f32_16x16x32_bf16 v[168:171], a[56:59], a[24:27], v[168:171]\n\t"
    "v_mfma_f32_16x16x32_bf16 v[172:175], a[56:59], a[28:31], v[172:175]\n\t"
    "s_mov_b32 s56, 0x42a00000\n\t"
    "s_mov_b32 s57, 0x3e000000\n\t"
    "s_mov_b32 s58, 0x3acccccd\n\t"
    "s_mov_b32 s59, 0x3727c5ac\n\t"
    "v_add_f32 v30, v176, v178\n\t"
    "v_mov_b32 v28, v30\n\t"
    "s_nop 1\n\t"
    "v_permlane32_swap_b32 v30, v28\n\t"
    "v_add_f32 v30, v30, v28\n\t"
    "v_mov_b32 v28, v30\n\t"
    "s_nop 1\n\t"
    "v_permlane16_swap_b32 v30, v28\n\t"
    "v_add_f32 v30, v30, v28\n\t"
    "v_mul_f32 v31, s57, v30\n\t"
    "v_add_f32 v30, v177, v179\n\t"
    "v_sub_f32 v32, v176, v31\n\t"
    "v_mul_f32 v32, v32, v32\n\t"
    "v_fma_f32 v30, s56, v32, v30\n\t"
    "v_sub_f32 v32, v178, v31\n\t"
    "v_mul_f32 v32, v32, v32\n\t"
    "v_fma_f32 v30, s56, v32, v30\n\t"
    "v_mov_b32 v28, v30\n\t"
    "s_nop 1\n\t"
    "v_permlane32_swap_b32 v30, v28\n\t"
    "v_add_f32 v30, v30, v28\n\t"
    "v_mov_b32 v28, v30\n\t"
    "s_nop 1\n\t"
    "v_permlane16_swap_b32 v30, v28\n\t"
    "v_add_f32 v30, v30, v28\n\t"
    "v_mov_b32 v32, s59\n\t"
    "v_fma_f32 v30, s58, v30, v32\n\t"
    "v_rsq_f32 v24, v30\n\t"
    "v_sub_f32 v20, 0, v31\n\t"
    "v_mov_b32 v21, v20\n\t"
    "v_mov_b32 v25, v24\n\t"
    "v_add_f32 v30, v184, v186\n\t"
    "v_mov_b32 v28, v30\n\t"
    "s_nop 1\n\t"
    "v_permlane32_swap_b32 v30, v28\n\t"
    "v_add_f32 v30, v30, v28\n\t"
    "v_mov_b32 v28, v30\n\t"
    "s_nop 1\n\t"
    "v_permlane16_swap_b32 v30, v28\n\t"
    "v_add_f32 v30, v30, v28\n\t"
    "v_mul_f32 v31, s57, v30\n\t"
    "v_add_f32 v30, v185, v187\n\t"
    "v_sub_f32 v32, v184, v31\n\t"
    "v_mul_f32 v32, v32, v32\n\t"
    "v_fma_f32 v30, s56, v32, v30\n\t"
    "v_sub_f32 v32, v186, v31\n\t"
    "v_mul_f32 v32, v32, v32\n\t"
    "v_fma_f32 v30, s56, v32, v30\n\t"
    "v_mov_b32 v28, v30\n\t"
    "s_nop 1\n\t"
    "v_permlane32_swap_b32 v30, v28\n\t"
    "v_add_f32 v30, v30, v28\n\t"
    "v_mov_b32 v28, v30\n\t"
    "s_nop 1\n\t"
    "v_permlane16_swap_b32 v30, v28\n\t"
    "v_add_f32 v30, v30, v28\n\t"
    "v_mov_b32 v32, s59\n\t"
    "v_fma_f32 v30, s58, v30, v32\n\t"
    "v_rsq_f32 v26, v30\n\t"
    "v_sub_f32 v22, 0, v31\n\t"
    "v_mov_b32 v23, v22\n\t"
    "v_mov_b32 v27, v26\n\t"
    "ds_read_b128 v[176:179], v14 offset:0\n\t"
    "ds_read_b128 v[180:183], v14 offset:64\n\t"
    "ds_read_b128 v[184:187], v14 offset:128\n\t"
    "ds_read_b128 v[188:191], v14 offset:192\n\t"
    "s_waitcnt lgkmcnt(3)\n\t"
    "v_pk_fma_f32 v[64:65], v[20:21], v[176:177], v[64:65]\n\t"
    "v_pk_fma_f32 v[66:67], v[20:21], v[178:179], v[66:67]\n\t"
    "s_waitcnt lgkmcnt(2)\n\t"
    "v_pk_fma_f32 v[64:65], v[64:65], v[24:25], v[180:181]\n\t"
    "v_pk_fma_f32 v[66:67], v[66:67], v[24:25], v[182:183]\n\t"
    "s_waitcnt lgkmcnt(1)\n\t"
    "v_pk_fma_f32 v[72:73], v[20:21], v[184:185], v[72:73]\n\t"
    "v_pk_fma_f32 v[74:75], v[20:21], v[186:187], v[74:75]\n\t"
    "s_waitcnt lgkmcnt(0)\n\t"
    "v_pk_fma_f32 v[72:73], v[72:73], v[24:25], v[188:189]\n\t"
    "v_pk_fma_f32 v[74:75], v[74:75], v[24:25], v[190:191]\n\t"
    "v_pk_mul_f32 v[64:65], v[64:65], v[72:73]\n\t"
    "v_pk_mul_f32 v[66:67], v[66:67], v[74:75]\n\t"
    "v_cvt_pk_bf16_f32 v208, v64, v65\n\t"
    "v_cvt_pk_bf16_f32 v209, v66, v67\n\t"
    "v_pk_fma_f32 v[68:69], v[22:23], v[176:177], v[68:69]\n\t"
    "v_pk_fma_f32 v[70:71], v[22:23], v[178:179], v[70:71]\n\t"
    "v_pk_fma_f32 v[68:69], v[68:69], v[26:27], v[180:181]\n\t"
    "v_pk_fma_f32 v[70:71], v[70:71], v[26:27], v[182:183]\n\t"
    "v_pk_fma_f32 v[76:77], v[22:23], v[184:185], v[76:77]\n\t"
    "v_pk_fma_f32 v[78:79], v[22:23], v[186:187], v[78:79]\n\t"
    "v_pk_fma_f32 v[76:77], v[76:77], v[26:27], v[188:189]\n\t"
    "v_pk_fma_f32 v[78:79], v[78:79], v[26:27], v[190:191]\n\t"
    "v_pk_mul_f32 v[68:69], v[68:69], v[76:77]\n\t"
    "v_pk_mul_f32 v[70:71], v[70:71], v[78:79]\n\t"
    "v_cvt_pk_bf16_f32 v212, v68, v69\n\t"
    "v_cvt_pk_bf16_f32 v213, v70, v71\n\t"
    "ds_read_b128 v[192:195], v14 offset:256\n\t"
    "ds_read_b128 v[196:199], v14 offset:320\n\t"
    "ds_read_b128 v[200:203], v14 offset:384\n\t"
    "ds_read_b128 v[204:207], v14 offset:448\n\t"
    "s_waitcnt lgkmcnt(3)\n\t"
    "v_pk_fma_f32 v[80:81], v[20:21], v[192:193], v[80:81]\n\t"
    "v_pk_fma_f32 v[82:83], v[20:21], v[194:195], v[82:83]\n\t"
    "s_waitcnt lgkmcnt(2)\n\t"
    "v_pk_fma_f32 v[80:81], v[80:81], v[24:25], v[196:197]\n\t"
    "v_pk_fma_f32 v[82:83], v[82:83], v[24:25], v[198:199]\n\t"
    "s_waitcnt lgkmcnt(1)\n\t"
    "v_pk_fma_f32 v[88:89], v[20:21], v[200:201], v[88:89]\n\t"
    "v_pk_fma_f32 v[90:91], v[20:21], v[202:203], v[90:91]\n\t"
    "s_waitcnt lgkmcnt(0)\n\t"
    "v_pk_fma_f32 v[88:89], v[88:89], v[24:25], v[204:205]\n\t"
    "v_pk_fma_f32 v[90:91], v[90:91], v[24:25], v[206:207]\n\t"
    "v_pk_mul_f32 v[80:81], v[80:81], v[88:89]\n\t"
    "v_pk_mul_f32 v[82:83], v[82:83], v[90:91]\n\t"
    "v_cvt_pk_bf16_f32 v210, v80, v81\n\t"
    "v_cvt_pk_bf16_f32 v211, v82, v83\n\t"
    "v_pk_fma_f32 v[84:85], v[22:23], v[192:193], v[84:85]\n\t"
    "v_pk_fma_f32 v[86:87], v[22:23], v[194:195], v[86:87]\n\t"
    "v_pk_fma_f32 v[84:85], v[84:85], v[26:27], v[196:197]\n\t"
    "v_pk_fma_f32 v[86:87], v[86:87], v[26:27], v[198:199]\n\t"
    "v_pk_fma_f32 v[92:93], v[22:23], v[200:201], v[92:93]\n\t"
    "v_pk_fma_f32 v[94:95], v[22:23], v[202:203], v[94:95]\n\t"
    "v_pk_fma_f32 v[92:93], v[92:93], v[26:27], v[204:205]\n\t"
    "v_pk_fma_f32 v[94:95], v[94:95], v[26:27], v[206:207]\n\t"
    "v_pk_mul_f32 v[84:85], v[84:85], v[92:93]\n\t"
    "v_pk_mul_f32 v[86:87], v[86:87], v[94:95]\n\t"
    "v_cvt_pk_bf16_f32 v214, v84, v85\n\t"
    "v_cvt_pk_bf16_f32 v215, v86, v87\n\t"
    "global_store_dwordx4 v15, v[208:211], s[44:45] sc1\n\t"
    "global_store_dwordx4 v16, v[212:215], s[44:45] sc1\n\t"
    "s_add_u32 s44, s44, 4096\n\t"
    "s_addc_u32 s45, s45, 0\n\t"
    "ds_read_b128 v[176:179], v14 offset:512\n\t"
    "ds_read_b128 v[180:183], v14 offset:576\n\t"
    "ds_read_b128 v[184:187], v14 offset:640\n\t"
    "ds_read_b128 v[188:191], v14 offset:704\n\t"
    "s_waitcnt lgkmcnt(3)\n\t"
    "v_pk_fma_f32 v[96:97], v[20:21], v[176:177], v[96:97]\n\t"
    "v_pk_fma_f32 v[98:99], v[20:21], v[178:179], v[98:99]\n\t"
    "s_waitcnt lgkmcnt(2)\n\t"
    "v_pk_fma_f32 v[96:97], v[96:97], v[24:25], v[180:181]\n\t"
    "v_pk_fma_f32 v[98:99], v[98:99], v[24:25], v[182:183]\n\t"
    "s_waitcnt lgkmcnt(1)\n\t"
    "v_pk_fma_f32 v[104:105], v[20:21], v[184:185], v[104:105]\n\t"
    "v_pk_fma_f32 v[106:107], v[20:21], v[186:187], v[106:107]\n\t"
    "s_waitcnt lgkmcnt(0)\n\t"
    "v_pk_fma_f32 v[104:105], v[104:105], v[24:25], v[188:189]\n\t"
    "v_pk_fma_f32 v[106:107], v[106:107], v[24:25], v[190:191]\n\t"
    "v_pk_mul_f32 v[96:97], v[96:97], v[104:105]\n\t"
    "v_pk_mul_f32 v[98:99], v[98:99], v[106:107]\n\t"
    "v_cvt_pk_bf16_f32 v216, v96, v97\n\t"
    "v_cvt_pk_bf16_f32 v217, v98, v99\n\t"
    "v_pk_fma_f32 v[100:101], v[22:23], v[176:177], v[100:101]\n\t"
    "v_pk_fma_f32 v[102:103], v[22:23], v[178:179], v[102:103]\n\t"
    "v_pk_fma_f32 v[100:101], v[100:101], v[26:27], v[180:181]\n\t"
    "v_pk_fma_f32 v[102:103], v[102:103], v[26:27], v[182:183]\n\t"
    "v_pk_fma_f32 v[108:109], v[22:23], v[184:185], v[108:109]\n\t"
    "v_pk_fma_f32 v[110:111], v[22:23], v[186:187], v[110:111]\n\t"
    "v_pk_fma_f32 v[108:109], v[108:109], v[26:27], v[188:189]\n\t"
    "v_pk_fma_f32 v[110:111], v[110:111], v[26:27], v[190:191]\n\t"
    "v_pk_mul_f32 v[100:101], v[100:101], v[108:109]\n\t"
    "v_pk_mul_f32 v[102:103], v[102:103], v[110:111]\n\t"
    "v_cvt_pk_bf16_f32 v220, v100, v101\n\t"
    "v_cvt_pk_bf16_f32 v221, v102, v103\n\t"
    "ds_read_b128 v[192:195], v14 offset:768\n\t"
    "ds_read_b128 v[196:199], v14 offset:832\n\t"
    "ds_read_b128 v[200:203], v14 offset:896\n\t"
    "ds_read_b128 v[204:207], v14 offset:960\n\t"
    "s_waitcnt lgkmcnt(3)\n\t"
    "v_pk_fma_f32 v[112:113], v[20:21], v[192:193], v[112:113]\n\t"
    "v_pk_fma_f32 v[114:115], v[20:21], v[194:195], v[114:115]\n\t"
    "s_waitcnt lgkmcnt(2)\n\t"
    "v_pk_fma_f32 v[112:113], v[112:113], v[24:25], v[196:197]\n\t"
    "v_pk_fma_f32 v[114:115], v[114:115], v[24:25], v[198:199]\n\t"
    "s_waitcnt lgkmcnt(1)\n\t"
    "v_pk_fma_f32 v[120:121], v[20:21], v[200:201], v[120:121]\n\t"
    "v_pk_fma_f32 v[122:123], v[20:21], v[202:203], v[122:123]\n\t"
    "s_waitcnt lgkmcnt(0)\n\t"
    "v_pk_fma_f32 v[120:121], v[120:121], v[24:25], v[204:205]\n\t"
    "v_pk_fma_f32 v[122:123], v[122:123], v[24:25], v[206:207]\n\t"
    "v_pk_mul_f32 v[112:113], v[112:113], v[120:121]\n\t"
    "v_pk_mul_f32 v[114:115], v[114:115], v[122:123]\n\t"
    "v_cvt_pk_bf16_f32 v218, v112, v113\n\t"
    "v_cvt_pk_bf16_f32 v219, v114, v115\n\t"
    "v_pk_fma_f32 v[116:117], v[22:23], v[192:193], v[116:117]\n\t"
    "v_pk_fma_f32 v[118:119], v[22:23], v[194:195], v[118:119]\n\t"
    "v_pk_fma_f32 v[116:117], v[116:117], v[26:27], v[196:197]\n\t"
    "v_pk_fma_f32 v[118:119], v[118:119], v[26:27], v[198:199]\n\t"
    "v_pk_fma_f32 v[124:125], v[22:23], v[200:201], v[124:125]\n\t"
    "v_pk_fma_f32 v[126:127], v[22:23], v[202:203], v[126:127]\n\t"
    "v_pk_fma_f32 v[124:125], v[124:125], v[26:27], v[204:205]\n\t"
    "v_pk_fma_f32 v[126:127], v[126:127], v[26:27], v[206:207]\n\t"
    "v_pk_mul_f32 v[116:117], v[116:117], v[124:125]\n\t"
    "v_pk_mul_f32 v[118:119], v[118:119], v[126:127]\n\t"
    "v_cvt_pk_bf16_f32 v222, v116, v117\n\t"
    "v_cvt_pk_bf16_f32 v223, v118, v119\n\t"
    "global_store_dwordx4 v15, v[216:219], s[44:45] sc1\n\t"
    "global_store_dwordx4 v16, v[220:223], s[44:45] sc1\n\t"
    "s_add_u32 s44, s44, 4096\n\t"
    "s_addc_u32 s45, s45, 0\n\t"
    "ds_read_b128 v[176:179], v14 offset:1024\n\t"
    "ds_read_b128 v[180:183], v14 offset:1088\n\t"
    "ds_read_b128 v[184:187], v14 offset:1152\n\t"
    "ds_read_b128 v[188:191], v14 offset:1216\n\t"
    "s_waitcnt lgkmcnt(3)\n\t"
    "v_pk_fma_f32 v[128:129], v[20:21], v[176:177], v[128:129]\n\t"
    "v_pk_fma_f32 v[130:131], v[20:21], v[178:179], v[130:131]\n\t"
    "s_waitcnt lgkmcnt(2)\n\t"
    "v_pk_fma_f32 v[128:129], v[128:129], v[24:25], v[180:181]\n\t"
    "v_pk_fma_f32 v[130:131], v[130:131], v[24:25], v[182:183]\n\t"
    "s_waitcnt lgkmcnt(1)\n\t"
    "v_pk_fma_f32 v[136:137], v[20:21], v[184:185], v[136:137]\n\t"
    "v_pk_fma_f32 v[138:139], v[20:21], v[186:187], v[138:139]\n\t"
    "s_waitcnt lgkmcnt(0)\n\t"
    "v_pk_fma_f32 v[136:137], v[136:137], v[24:25], v[188:189]\n\t"
    "v_pk_fma_f32 v[138:139], v[138:139], v[24:25], v[190:191]\n\t"
    "v_pk_mul_f32 v[128:129], v[128:129], v[136:137]\n\t"
    "v_pk_mul_f32 v[130:131], v[130:131], v[138:139]\n\t"
    "v_cvt_pk_bf16_f32 v208, v128, v129\n\t"
    "v_cvt_pk_bf16_f32 v209, v130, v131\n\t"
    "v_pk_fma_f32 v[132:133], v[22:23], v[176:177], v[132:133]\n\t"
    "v_pk_fma_f32 v[134:135], v[22:23], v[178:179], v[134:135]\n\t"
    "v_pk_fma_f32 v[132:133], v[132:133], v[26:27], v[180:181]\n\t"
    "v_pk_fma_f32 v[134:135], v[134:135], v[26:27], v[182:183]\n\t"
    "v_pk_fma_f32 v[140:141], v[22:23], v[184:185], v[140:141]\n\t"
    "v_pk_fma_f32 v[142:143], v[22:23], v[186:187], v[142:143]\n\t"
    "v_pk_fma_f32 v[140:141], v[140:141], v[26:27], v[188:189]\n\t"
    "v_pk_fma_f32 v[142:143], v[142:143], v[26:27], v[190:191]\n\t"
    "v_pk_mul_f32 v[132:133], v[132:133], v[140:141]\n\t"
    "v_pk_mul_f32 v[134:135], v[134:135], v[142:143]\n\t"
    "v_cvt_pk_bf16_f32 v212, v132, v133\n\t"
    "v_cvt_pk_bf16_f32 v213, v134, v135\n\t"
    "ds_read_b128 v[192:195], v14 offset:1280\n\t"
    "ds_read_b128 v[196:199], v14 offset:1344\n\t"
    "ds_read_b128 v[200:203], v14 offset:1408\n\t"
    "ds_read_b128 v[204:207], v14 offset:1472\n\t"
    "s_waitcnt lgkmcnt(3)\n\t"
    "v_pk_fma_f32 v[144:145], v[20:21], v[192:193], v[144:145]\n\t"
    "v_pk_fma_f32 v[146:147], v[20:21], v[194:195], v[146:147]\n\t"
    "s_waitcnt lgkmcnt(2)\n\t"
    "v_pk_fma_f32 v[144:145], v[144:145], v[24:25], v[196:197]\n\t"
    "v_pk_fma_f32 v[146:147], v[146:147], v[24:25], v[198:199]\n\t"
    "s_waitcnt lgkmcnt(1)\n\t"
    "v_pk_fma_f32 v[152:153], v[20:21], v[200:201], v[152:153]\n\t"
    "v_pk_fma_f32 v[154:155], v[20:21], v[202:203], v[154:155]\n\t"
    "s_waitcnt lgkmcnt(0)\n\t"
    "v_pk_fma_f32 v[152:153], v[152:153], v[24:25], v[204:205]\n\t"
    "v_pk_fma_f32 v[154:155], v[154:155], v[24:25], v[206:207]\n\t"
    "v_pk_mul_f32 v[144:145], v[144:145], v[152:153]\n\t"
    "v_pk_mul_f32 v[146:147], v[146:147], v[154:155]\n\t"
    "v_cvt_pk_bf16_f32 v210, v144, v145\n\t"
    "v_cvt_pk_bf16_f32 v211, v146, v147\n\t"
    "v_pk_fma_f32 v[148:149], v[22:23], v[192:193], v[148:149]\n\t"
    "v_pk_fma_f32 v[150:151], v[22:23], v[194:195], v[150:151]\n\t"
    "v_pk_fma_f32 v[148:149], v[148:149], v[26:27], v[196:197]\n\t"
    "v_pk_fma_f32 v[150:151], v[150:151], v[26:27], v[198:199]\n\t"
    "v_pk_fma_f32 v[156:157], v[22:23], v[200:201], v[156:157]\n\t"
    "v_pk_fma_f32 v[158:159], v[22:23], v[202:203], v[158:159]\n\t"
    "v_pk_fma_f32 v[156:157], v[156:157], v[26:27], v[204:205]\n\t"
    "v_pk_fma_f32 v[158:159], v[158:159], v[26:27], v[206:207]\n\t"
    "v_pk_mul_f32 v[148:149], v[148:149], v[156:157]\n\t"
    "v_pk_mul_f32 v[150:151], v[150:151], v[158:159]\n\t"
    "v_cvt_pk_bf16_f32 v214, v148, v149\n\t"
    "v_cvt_pk_bf16_f32 v215, v150, v151\n\t"
    "global_store_dwordx4 v15, v[208:211], s[44:45] sc1\n\t"
    "global_store_dwordx4 v16, v[212:215], s[44:45] sc1\n\t"
    "ds_read_b128 v[176:179], v14 offset:1536\n\t"
    "ds_read_b128 v[180:183], v14 offset:1600\n\t"
    "ds_read_b128 v[184:187], v14 offset:1664\n\t"
    "ds_read_b128 v[188:191], v14 offset:1728\n\t"
    "s_waitcnt lgkmcnt(3)\n\t"
    "v_pk_fma_f32 v[160:161], v[20:21], v[176:177], v[160:161]\n\t"
    "v_pk_fma_f32 v[162:163], v[20:21], v[178:179], v[162:163]\n\t"
    "s_waitcnt lgkmcnt(2)\n\t"
    "v_pk_fma_f32 v[160:161], v[160:161], v[24:25], v[180:181]\n\t"
    "v_pk_fma_f32 v[162:163], v[162:163], v[24:25], v[182:183]\n\t"
    "s_waitcnt lgkmcnt(1)\n\t"
    "v_pk_fma_f32 v[168:169], v[20:21], v[184:185], v[168:169]\n\t"
    "v_pk_fma_f32 v[170:171], v[20:21], v[186:187], v[170:171]\n\t"
    "s_waitcnt lgkmcnt(0)\n\t"
    "v_pk_fma_f32 v[168:169], v[168:169], v[24:25], v[188:189]\n\t"
    "v_pk_fma_f32 v[170:171], v[170:171], v[24:25], v[190:191]\n\t"
    "v_pk_mul_f32 v[160:161], v[160:161], v[168:169]\n\t"
    "v_pk_mul_f32 v[162:163], v[162:163], v[170:171]\n\t"
    "v_cvt_pk_bf16_f32 v224, v160, v161\n\t"
    "v_cvt_pk_bf16_f32 v225, v162, v163\n\t"
    "v_pk_fma_f32 v[164:165], v[22:23], v[176:177], v[164:165]\n\t"
    "v_pk_fma_f32 v[166:167], v[22:23], v[178:179], v[166:167]\n\t"
    "v_pk_fma_f32 v[164:165], v[164:165], v[26:27], v[180:181]\n\t"
    "v_pk_fma_f32 v[166:167], v[166:167], v[26:27], v[182:183]\n\t"
    "v_pk_fma_f32 v[172:173], v[22:23], v[184:185], v[172:173]\n\t"
    "v_pk_fma_f32 v[174:175], v[22:23], v[186:187], v[174:175]\n\t"
    "v_pk_fma_f32 v[172:173], v[172:173], v[26:27], v[188:189]\n\t"
    "v_pk_fma_f32 v[174:175], v[174:175], v[26:27], v[190:191]\n\t"
    "v_pk_mul_f32 v[164:165], v[164:165], v[172:173]\n\t"
    "v_pk_mul_f32 v[166:167], v[166:167], v[174:175]\n\t"
    "v_cvt_pk_bf16_f32 v226, v164, v165\n\t"
    "v_cvt_pk_bf16_f32 v227, v166, v167\n\t"
    "s_sub_u32 s44, s44, 8192\n\t"
    "s_subb_u32 s45, s45, 0\n\t"
    "global_store_dwordx2 v17, v[224:225], s[44:45] sc1\n\t"
    "global_store_dwordx2 v18, v[226:227], s[44:45] sc1\n\t"
    "s_nop 1\n\t"
      : "+{v10}"(va), "+{s[36:37]}"(sX), "+{s[38:39]}"(sW), "+{s[44:45]}"(sO)
      : "{v1}"(xo0), "{v2}"(xo1), "{v3}"(lo),
        "{v11}"(so0), "{v12}"(so1), "{v13}"(vo), "{v14}"(vrd), "{v15}"(st0), "{v16}"(st1), "{v17}"(su0), "{v18}"(su1), "{v9}"(pl),
        "{s[40:41]}"(ws + WS_ST), "{s[42:43]}"(ws + WS_VECS), "{s46}"(m0b), "{s47}"(m0v),
        "{s[64:65]}"(tag), "{s[66:67]}"(ws), "{s68}"(wv), "{s70}"(myf)
      : "v4", "v5", "v6", "v7", "v8", "v20", "v21", "v22", "v23", "v24", "v25", "v26", "v27", "v28", "v29", "v30", "v31", "v32", "v33", "v34", "v35", "v36", "v37", "v38", "v39", "v40", "v41", "v42", "v43", "v44", "v45", "v46", "v47", "v48", "v49", "v50", "v51", "v52", "v53", "v54", "v55", "v56", "v57", "v58", "v59", "v60", "v61", "v62", "v63", "v64", "v65", "v66", "v67", "v68", "v69", "v70", "v71", "v72", "v73", "v74", "v75", "v76", "v77", "v78", "v79", "v80", "v81", "v82", "v83", "v84", "v85", "v86", "v87", "v88", "v89", "v90", "v91", "v92", "v93", "v94", "v95", "v96", "v97", "v98", "v99", "v100", "v101", "v102", "v103", "v104", "v105", "v106", "v107", "v108", "v109", "v110", "v111", "v112", "v113", "v114", "v115", "v116", "v117", "v118", "v119", "v120", "v121", "v122", "v123", "v124", "v125", "v126", "v127", "v128", "v129", "v130", "v131", "v132", "v133", "v134", "v135", "v136", "v137", "v138", "v139", "v140", "v141", "v142", "v143", "v144", "v145", "v146", "v147", "v148", "v149", "v150", "v151", "v152", "v153", "v154", "v155", "v156", "v157", "v158", "v159", "v160", "v161", "v162", "v163", "v164", "v165", "v166", "v167", "v168", "v169", "v170", "v171", "v172", "v173", "v174", "v175", "v176", "v177", "v178", "v179", "v180", "v181", "v182", "v183", "v184", "v185", "v186", "v187", "v188", "v189", "v190", "v191", "v192", "v193", "v194", "v195", "v196", "v197", "v198", "v199", "v200", "v201", "v202", "v203", "v204", "v205", "v206", "v207", "v208", "v209", "v210", "v211", "v212", "v213", "v214", "v215", "v216", "v217", "v218", "v219", "v220", "v221", "v222", "v223", "v224", "v225", "v226", "v227", "v228", "v229", "v230", "v231", "v232", "v233", "v234", "v235", "v236", "v237", "v238", "v239", "v240", "v241", "v242", "v243", "v244", "v245", "v246", "v247", "v248", "v249", "a0", "a1", "a2", "a3", "a4", "a5", "a6", "a7", "a8", "a9", "a10", "a11", "a12", "a13", "a14", "a15", "a16", "a17", "a18", "a19", "a20", "a21", "a22", "a23", "a24", "a25", "a26", "a27", "a28", "a29", "a30", "a31", "a32", "a33", "a34", "a35", "a36", "a37", "a38", "a39", "a40", "a41", "a42", "a43", "a44", "a45", "a46", "a47", "a48", "a49", "a50", "a51", "a52", "a53", "a54", "a55", "a56", "a57", "a58", "a59", "a60", "a61", "a62", "a63", "s48", "s49", "s50", "s51", "s52", "s53", "s54", "s55", "s56", "s57", "s58", "s59", "s60", "s61", "s62", "s63", "s69", "s72", "s73", "vcc", "memory");
    STAMP_OUT(2)
  }
  {
    BD_GLUE(false)
    const unsigned ctc = 4u * (x & 3u) + (jj & 3u), rtc = 8u * (x >> 2) + (jj >> 2);
    const unsigned myf = __builtin_amdgcn_readfirstlane(F5 + 8u * (16u * rtc + ctc));
    const unsigned pl = F5 + 8u * (16u * (qt >> 1) + c16);
    asm volatile(
    "v_add_u32 v5, s59, v1\n\t"
    "v_add_u32 v4, s58, v1\n\t"
    "s_waitcnt vmcnt(0)\n\t"
    "s_barrier\n\t"
    "s_cmp_lg_u32 s68, 0\n\t"
    "s_cbranch_scc1 LM_pub23_%=\n\t"
    "v_mov_b32 v100, s64\n\t"
    "v_mov_b32 v101, s65\n\t"
    "v_mov_b32 v102, s70\n\t"
    "s_mov_b64 exec, 1\n\t"
    "global_store_dwordx2 v102, v[100:101], s[66:67] sc1\n\t"
    "s_mov_b64 exec, -1\n\t"
    "LM_pub23_%=:\n\t"
    "global_load_dwordx4 a[0:3], v5, s[38:39] offset:-2048 sc1\n\t"
    "global_load_dwordx4 a[4:7], v5, s[38:39] offset:-1024 sc1\n\t"
    "global_load_dwordx4 a[8:11], v5, s[38:39] offset:0 sc1\n\t"
    "global_load_dwordx4 a[12:15], v5, s[38:39] offset:1024 sc1\n\t"
    "global_load_dwordx4 a[16:19], v5, s[38:39] offset:2048 sc1\n\t"
    "s_add_u32 s59, s59, 5120\n\t"
    "s_cmp_eq_u32 s59, 71680\n\t"
    "s_cselect_b32 s59, 0, s59\n\t"
    "v_add_u32 v5, s59, v1\n\t"
    "global_load_dwordx4 a[36:39], v5, s[38:39] offset:-2048 sc1\n\t"
    "global_load_dwordx4 a[40:43], v5, s[38:39] offset:-1024 sc1\n\t"
    "global_load_dwordx4 a[44:47], v5, s[38:39] offset:0 sc1\n\t"
    "global_load_dwordx4 a[48:51], v5, s[38:39] offset:1024 sc1\n\t"
    "global_load_dwordx4 a[52:55], v5, s[38:39] offset:2048 sc1\n\t"
    "s_add_u32 s59, s59, 5120\n\t"
    "s_cmp_eq_u32 s59, 71680\n\t"
    "s_cselect_b32 s59, 0, s59\n\t"
    "v_add_u32 v5, s59, v1\n\t"
    "global_load_dwordx4 a[72:75], v5, s[38:39] offset:-2048 sc1\n\t"
    "global_load_dwordx4 a[76:79], v5, s[38:39] offset:-1024 sc1\n\t"
    "global_load_dwordx4 a[80:83], v5, s[38:39] offset:0 sc1\n\t"
    "global_load_dwordx4 a[84:87], v5, s[38:39] offset:1024 sc1\n\t"
    "global_load_dwordx4 a[88:91], v5, s[38:39] offset:2048 sc1\n\t"
    "s_add_u32 s59, s59, 5120\n\t"
    "s_cmp_eq_u32 s59, 71680\n\t"
    "s_cselect_b32 s59, 0, s59\n\t"
    "v_add_u32 v5, s59, v1\n\t"
    "global_load_dwordx4 a[108:111], v5, s[38:39] offset:-2048 sc1\n\t"
    "global_load_dwordx4 a[112:115], v5, s[38:39] offset:-1024 sc1\n\t"
    "global_load_dwordx4 a[116:119], v5, s[38:39] offset:0 sc1\n\t"
    "global_load_dwordx4 a[120:123], v5, s[38:39] offset:1024 sc1\n\t"
    "global_load_dwordx4 a[124:127], v5, s[38:39] offset:2048 sc1\n\t"
    "s_add_u32 s59, s59, 5120\n\t"
    "s_cmp_eq_u32 s59, 71680\n\t"
    "s_cselect_b32 s59, 0, s59\n\t"
    "v_add_u32 v5, s59, v1\n\t"
    "global_load_dwordx4 a[144:147], v5, s[38:39] offset:-2048 sc1\n\t"
    "global_load_dwordx4 a[148:151], v5, s[38:39] offset:-1024 sc1\n\t"
    "global_load_dwordx4 a[152:155], v5, s[38:39] offset:0 sc1\n\t"
    "global_load_dwordx4 a[156:159], v5, s[38:39] offset:1024 sc1\n\t"
    "global_load_dwordx4 a[160:163], v5, s[38:39] offset:2048 sc1\n\t"
    "s_add_u32 s59, s59, 5120\n\t"
    "s_cmp_eq_u32 s59, 71680\n\t"
    "s_cselect_b32 s59, 0, s59\n\t"
    "v_add_u32 v5, s59, v1\n\t"
    "v_mov_b32 v20, 0\n\t"
    "v_mov_b32 v21, 0\n\t"
    "v_mov_b32 v22, 0\n\t"
    "v_mov_b32 v23, 0\n\t"
    "v_mov_b32 v24, 0\n\t"
    "v_mov_b32 v25, 0\n\t"
    "v_mov_b32 v26, 0\n\t"
    "v_mov_b32 v27, 0\n\t"
    "v_mov_b32 v28, 0\n\t"
    "v_mov_b32 v29, 0\n\t"
    "v_mov_b32 v30, 0\n\t"
    "v_mov_b32 v31, 0\n\t"
    "v_mov_b32 v32, 0\n\t"
    "v_mov_b32 v33, 0\n\t"
    "v_mov_b32 v34, 0\n\t"
    "v_mov_b32 v35, 0\n\t"
    "v_mov_b32 v36, 0\n\t"
    "v_mov_b32 v37, 0\n\t"
    "v_mov_b32 v38, 0\n\t"
    "v_mov_b32 v39, 0\n\t"
    "v_mov_b32 v40, 0\n\t"
    "v_mov_b32 v41, 0\n\t"
    "v_mov_b32 v42, 0\n\t"
    "v_mov_b32 v43, 0\n\t"
    "v_mov_b32 v44, 0\n\t"
    "v_mov_b32 v45, 0\n\t"
    "v_mov_b32 v46, 0\n\t"
    "v_mov_b32 v47, 0\n\t"
    "v_mov_b32 v48, 0\n\t"
    "v_mov_b32 v49, 0\n\t"
    "v_mov_b32 v50, 0\n\t"
    "v_mov_b32 v51, 0\n\t"
    "v_mov_b32 v52, 0\n\t"
    "v_mov_b32 v53, 0\n\t"
    "v_mov_b32 v54, 0\n\t"
    "v_mov_b32 v55, 0\n\t"
    "v_mov_b32 v56, 0\n\t"
    "v_mov_b32 v57, 0\n\t"
    "v_mov_b32 v58, 0\n\t"
    "v_mov_b32 v59, 0\n\t"
    "v_mov_b32 v60, 0\n\t"
    "v_mov_b32 v61, 0\n\t"
    "v_mov_b32 v62, 0\n\t"
    "v_mov_b32 v63, 0\n\t"
    "v_mov_b32 v64, 0\n\t"
    "v_mov_b32 v65, 0\n\t"
    "v_mov_b32 v66, 0\n\t"
    "v_mov_b32 v67, 0\n\t"
    "v_mov_b32 v68, 0\n\t"
    "v_mov_b32 v69, 0\n\t"
    "v_mov_b32 v70, 0\n\t"
    "v_mov_b32 v71, 0\n\t"
    "v_mov_b32 v72, 0\n\t"
    "v_mov_b32 v73, 0\n\t"
    "v_mov_b32 v74, 0\n\t"
    "v_mov_b32 v75, 0\n\t"
    "v_mov_b32 v76, 0\n\t"
    "v_mov_b32 v77, 0\n\t"
    "v_mov_b32 v78, 0\n\t"
    "v_mov_b32 v79, 0\n\t"
    "v_mov_b32 v80, 0\n\t"
    "v_mov_b32 v81, 0\n\t"
    "v_mov_b32 v82, 0\n\t"
    "v_mov_b32 v83, 0\n\t"
    "v_mov_b32 v84, 0\n\t"
    "v_mov_b32 v85, 0\n\t"
    "v_mov_b32 v86, 0\n\t"
    "v_mov_b32 v87, 0\n\t"
    "v_mov_b32 v88, 0\n\t"
    "v_mov_b32 v89, 0\n\t"
    "v_mov_b32 v90, 0\n\t"
    "v_mov_b32 v91, 0\n\t"
    "v_mov_b32 v92, 0\n\t"
    "v_mov_b32 v93, 0\n\t"
    "v_mov_b32 v94, 0\n\t"
    "v_mov_b32 v95, 0\n\t"
    "v_mov_b32 v96, 0\n\t"
    "v_mov_b32 v97, 0\n\t"
    "v_mov_b32 v98, 0\n\t"
    "v_mov_b32 v99, 0\n\t"
    "s_mov_b32 s69, 100000\n\t"
    "global_load_dwordx2 v[100:101], v18, s[66:67] sc1\n\t"
    "s_sleep 2\n\t"
    "LM_poll24_%=:\n\t"
    "global_load_dwordx2 v[102:103], v18, s[66:67] sc1\n\t"
    "s_waitcnt vmcnt(1)\n\t"
    "v_cmp_eq_u64 vcc, s[64:65], v[100:101]\n\t"
    "s_cmp_eq_u64 vcc, -1\n\t"
    "s_cbranch_scc1 LM_polld25_%=\n\t"
    "global_load_dwordx2 v[100:101], v18, s[66:67] sc1\n\t"
    "s_waitcnt vmcnt(1)\n\t"
    "v_cmp_eq_u64 vcc, s[64:65], v[102:103]\n\t"
    "s_cmp_eq_u64 vcc, -1\n\t"
    "s_cbranch_scc1 LM_polld25_%=\n\t"
    "s_sub_u32 s69, s69, 1\n\t"
    "s_cmp_lg_u32 s69, 0\n\t"
    "s_cbranch_scc1 LM_poll24_%=\n\t"
    "LM_polld25_%=:\n\t"
    "global_load_dwordx4 a[20:23], v4, s[36:37] offset:0 sc1\n\t"
    "global_load_dwordx4 a[24:27], v4, s[36:37] offset:1024 sc1\n\t"
    "global_load_dwordx4 a[28:31], v4, s[36:37] offset:2048 sc1\n\t"
    "global_load_dwordx4 a[32:35], v4, s[36:37] offset:3072 sc1\n\t"
    "s_add_u32 s58, s58, 4096\n\t"
    "s_cmp_eq_u32 s58, 57344\n\t"
    "s_cselect_b32 s58, 0, s58\n\t"
    "v_add_u32 v4, s58, v1\n\t"
    "global_load_dwordx4 a[56:59], v4, s[36:37] offset:0 sc1\n\t"
    "global_load_dwordx4 a[60:63], v4, s[36:37] offset:1024 sc1\n\t"
    "global_load_dwordx4 a[64:67], v4, s[36:37] offset:2048 sc1\n\t"
    "global_load_dwordx4 a[68:71], v4, s[36:37] offset:3072 sc1\n\t"
    "s_add_u32 s58, s58, 4096\n\t"
    "s_cmp_eq_u32 s58, 57344\n\t"
    "s_cselect_b32 s58, 0, s58\n\t"
    "v_add_u32 v4, s58, v1\n\t"
    "global_load_dwordx4 a[92:95], v4, s[36:37] offset:0 sc1\n\t"
    "global_load_dwordx4 a[96:99], v4, s[36:37] offset:1024 sc1\n\t"
    "global_load_dwordx4 a[100:103], v4, s[36:37] offset:2048 sc1\n\t"
    "global_load_dwordx4 a[104:107], v4, s[36:37] offset:3072 sc1\n\t"
    "s_add_u32 s58, s58, 4096\n\t"
    "s_cmp_eq_u32 s58, 57344\n\t"
    "s_cselect_b32 s58, 0, s58\n\t"
    "v_add_u32 v4, s58, v1\n\t"
    "global_load_dwordx4 a[128:131], v4, s[36:37] offset:0 sc1\n\t"
    "global_load_dwordx4 a[132:135], v4, s[36:37] offset:1024 sc1\n\t"
    "global_load_dwordx4 a[136:139], v4, s[36:37] offset:2048 sc1\n\t"
    "global_load_dwordx4 a[140:143], v4, s[36:37] offset:3072 sc1\n\t"
    "s_add_u32 s58, s58, 4096\n\t"
    "s_cmp_eq_u32 s58, 57344\n\t"
    "s_cselect_b32 s58, 0, s58\n\t"
    "v_add_u32 v4, s58, v1\n\t"
    "global_load_dwordx4 a[164:167], v4, s[36:37] offset:0 sc1\n\t"
    "global_load_dwordx4 a[168:171], v4, s[36:37] offset:1024 sc1\n\t"
    "global_load_dwordx4 a[172:175], v4, s[36:37] offset:2048 sc1\n\t"
    "global_load_dwordx4 a[176:179], v4, s[36:37] offset:3072 sc1\n\t"
    "s_add_u32 s58, s58, 4096\n\t"
    "s_cmp_eq_u32 s58, 57344\n\t"
    "s_cselect_b32 s58, 0, s58\n\t"
    "v_add_u32 v4, s58, v1\n\t"
    "s_waitcnt vmcnt(16)\n\t"
    "v_mfma_f32_16x16x32_bf16 v[20:23], a[0:3], a[20:23], v[20:23]\n\t"
    "global_load_dwordx4 a[180:183], v5, s[38:39] offset:-2048 sc1\n\t"
    "v_mfma_f32_16x16x32_bf16 v[24:27], a[0:3], a[24:27], v[24:27]\n\t"
    "v_mfma_f32_16x16x32_bf16 v[28:31], a[0:3], a[28:31], v[28:31]\n\t"
    "global_load_dwordx4 a[200:203], v4, s[36:37] offset:0 sc1\n\t"
    "v_mfma_f32_16x16x32_bf16 v[32:35], a[0:3], a[32:35], v[32:35]\n\t"
    "v_mfma_f32_16x16x32_bf16 v[36:39], a[4:7], a[20:23], v[36:39]\n\t"
    "global_load_dwordx4 a[204:207], v4, s[36:37] offset:1024 sc1\n\t"
    "v_mfma_f32_16x16x32_bf16 v[40:43], a[4:7], a[24:27], v[40:43]\n\t"
    "v_mfma_f32_16x16x32_bf16 v[44:47], a[4:7], a[28:31], v[44:47]\n\t"
    "global_load_dwordx4 a[208:211], v4, s[36:37] offset:2048 sc1\n\t"
    "v_mfma_f32_16x16x32_bf16 v[48:51], a[4:7], a[32:35], v[48:51]\n\t"
    "v_mfma_f32_16x16x32_bf16 v[52:55], a[8:11], a[20:23], v[52:55]\n\t"
    "global_load_dwordx4 a[212:215], v4, s[36:37] offset:3072 sc1\n\t"
    "v_mfma_f32_16x16x32_bf16 v[56:59], a[8:11], a[24:27], v[56:59]\n\t"
    "v_mfma_f32_16x16x32_bf16 v[60:63], a[8:11], a[28:31], v[60:63]\n\t"
    "global_load_dwordx4 a[184:187], v5, s[38:39] offset:-1024 sc1\n\t"
    "v_mfma_f32_16x16x32_bf16 v[64:67], a[8:11], a[32:35], v[64:67]\n\t"
    "v_mfma_f32_16x16x32_bf16 v[68:71], a[12:15], a[20:23], v[68:71]\n\t"
    "global_load_dwordx4 a[188:191], v5, s[38:39] offset:0 sc1\n\t"
    "v_mfma_f32_16x16x32_bf16 v[72:75], a[12:15], a[24:27], v[72:75]\n\t"
    "v_mfma_f32_16x16x32_bf16 v[76:79], a[12:15], a[28:31], v[76:79]\n\t"
    "global_load_dwordx4 a[192:195], v5, s[38:39] offset:1024 sc1\n\t"
    "v_mfma_f32_16x16x32_bf16 v[80:83], a[12:15], a[32:35], v[80:83]\n\t"
    "v_mfma_f32_16x16x32_bf16 v[84:87], a[16:19], a[20:23], v[84:87]\n\t"
    "global_load_dwordx4 a[196:199], v5, s[38:39] offset:2048 sc1\n\t"
    "v_mfma_f32_16x16x32_bf16 v[88:91], a[16:19], a[24:27], v[88:91]\n\t"
    "v_mfma_f32_16x16x32_bf16 v[92:95], a[16:19], a[28:31], v[92:95]\n\t"
    "v_mfma_f32_16x16x32_bf16 v[96:99], a[16:19], a[32:35], v[96:99]\n\t"
    "s_add_u32 s58, s58, 4096\n\t"
    "s_cmp_eq_u32 s58, 57344\n\t"
    "s_cselect_b32 s58, 0, s58\n\t"
    "v_add_u32 v4, s58, v1\n\t"
    "s_add_u32 s59, s59, 5120\n\t"
    "s_cmp_eq_u32 s59, 71680\n\t"
    "s_cselect_b32 s59, 0, s59\n\t"
    "v_add_u32 v5, s59, v1\n\t"
    "s_waitcnt vmcnt(21)\n\t"
    "v_mfma_f32_16x16x32_bf16 v[20:23], a[36:39], a[56:59], v[20:23]\n\t"
    "global_load_dwordx4 a[0:3], v5, s[38:39] offset:-2048 sc1\n\t"
    "v_mfma_f32_16x16x32_bf16 v[24:27], a[36:39], a[60:63], v[24:27]\n\t"
    "v_mfma_f32_16x16x32_bf16 v[28:31], a[36:39], a[64:67], v[28:31]\n\t"
    "global_load_dwordx4 a[20:23], v4, s[36:37] offset:0 sc1\n\t"
    "v_mfma_f32_16x16x32_bf16 v[32:35], a[36:39], a[68:71], v[32:35]\n\t"
    "v_mfma_f32_16x16x32_bf16 v[36:39], a[40:43], a[56:59], v[36:39]\n\t"
    "global_load_dwordx4 a[24:27], v4, s[36:37] offset:1024 sc1\n\t"
    "v_mfma_f32_16x16x32_bf16 v[40:43], a[40:43], a[60:63], v[40:43]\n\t"
    "v_mfma_f32_16x16x32_bf16 v[44:47], a[40:43], a[64:67], v[44:47]\n\t"
    "global_load_dwordx4 a[28:31], v4, s[36:37] offset:2048 sc1\n\t"
    "v_mfma_f32_16x16x32_bf16 v[48:51], a[40:43], a[68:71], v[48:51]\n\t"
    "v_mfma_f32_16x16x32_bf16 v[52:55], a[44:47], a[56:59], v[52:55]\n\t"
    "global_load_dwordx4 a[32:35], v4, s[36:37] offset:3072 sc1\n\t"
    "v_mfma_f32_16x16x32_bf16 v[56:59], a[44:47], a[60:63], v[56:59]\n\t"
    "v_mfma_f32_16x16x32_bf16 v[60:63], a[44:47], a[64:67], v[60:63]\n\t"
    "global_load_dwordx4 a[4:7], v5, s[38:39] offset:-1024 sc1\n\t"
    "v_mfma_f32_16x16x32_bf16 v[64:67], a[44:47], a[68:71], v[64:67]\n\t"
    "v_mfma_f32_16x16x32_bf16 v[68:71], a[48:51], a[56:59], v[68:71]\n\t"
    "global_load_dwordx4 a[8:11], v5, s[38:39] offset:0 sc1\n\t"
    "v_mfma_f32_16x16x32_bf16 v[72:75], a[48:51], a[60:63], v[72:75]\n\t"
    "v_mfma_f32_16x16x32_bf16 v[76:79], a[48:51], a[64:67], v[76:79]\n\t"
    "global_load_dwordx4 a[12:15], v5, s[38:39] offset:1024 sc1\n\t"
    "v_mfma_f32_16x16x32_bf16 v[80:83], a[48:51], a[68:71], v[80:83]\n\t"
    "v_mfma_f32_16x16x32_bf16 v[84:87], a[52:55], a[56:59], v[84:87]\n\t"
    "global_load_dwordx4 a[16:19], v5, s[38:39] offset:2048 sc1\n\t"
    "v_mfma_f32_16x16x32_bf16 v[88:91], a[52:55], a[60:63], v[88:91]\n\t"
    "v_mfma_f32_16x16x32_bf16 v[92:95], a[52:55], a[64:67], v[92:95]\n\t"
    "v_mfma_f32_16x16x32_bf16 v[96:99], a[52:55], a[68:71], v[96:99]\n\t"
    "s_add_u32 s58, s58, 4096\n\t"
    "s_cmp_eq_u32 s58, 57344\n\t"
    "s_cselect_b32 s58, 0, s58\n\t"
    "v_add_u32 v4, s58, v1\n\t"
    "s_add_u32 s59, s59, 5120\n\t"
    "s_cmp_eq_u32 s59, 71680\n\t"
    "s_cselect_b32 s59, 0, s59\n\t"
    "v_add_u32 v5, s59, v1\n\t"
    "s_waitcnt vmcnt(26)\n\t"
    "v_mfma_f32_16x16x32_bf16 v[20:23], a[72:75], a[92:95], v[20:23]\n\t"
    "global_load_dwordx4 a[36:39], v5, s[38:39] offset:-2048 sc1\n\t"
    "v_mfma_f32_16x16x32_bf16 v[24:27], a[72:75], a[96:99], v[24:27]\n\t"
    "v_mfma_f32_16x16x32_bf16 v[28:31], a[72:75], a[100:103], v[28:31]\n\t"
    "global_load_dwordx4 a[56:59], v4, s[36:37] offset:0 sc1\n\t"
    "v_mfma_f32_16x16x32_bf16 v[32:35], a[72:75], a[104:107], v[32:35]\n\t"
    "v_mfma_f32_16x16x32_bf16 v[36:39], a[76:79], a[92:95], v[36:39]\n\t"
    "global_load_dwordx4 a[60:63], v4, s[36:37] offset:1024 sc1\n\t"
    "v_mfma_f32_16x16x32_bf16 v[40:43], a[76:79], a[96:99], v[40:43]\n\t"
    "v_mfma_f32_16x16x32_bf16 v[44:47], a[76:79], a[100:103], v[44:47]\n\t"
    "global_load_dwordx4 a[64:67], v4, s[36:37] offset:2048 sc1\n\t"
    "v_mfma_f32_16x16x32_bf16 v[48:51], a[76:79], a[104:107], v[48:51]\n\t"
    "v_mfma_f32_16x16x32_bf16 v[52:55], a[80:83], a[92:95], v[52:55]\n\t"
    "global_load_dwordx4 a[68:71], v4, s[36:37] offset:3072 sc1\n\t"
    "v_mfma_f32_16x16x32_bf16 v[56:59], a[80:83], a[96:99], v[56:59]\n\t"
    "v_mfma_f32_16x16x32_bf16 v[60:63], a[80:83], a[100:103], v[60:63]\n\t"
    "global_load_dwordx4 a[40:43], v5, s[38:39] offset:-1024 sc1\n\t"
    "v_mfma_f32_16x16x32_bf16 v[64:67], a[80:83], a[104:107], v[64:67]\n\t"
    "v_mfma_f32_16x16x32_bf16 v[68:71], a[84:87], a[92:95], v[68:71]\n\t"
    "global_load_dwordx4 a[44:47], v5, s[38:39] offset:0 sc1\n\t"
    "v_mfma_f32_16x16x32_bf16 v[72:75], a[84:87], a[96:99], v[72:75]\n\t"
    "v_mfma_f32_16x16x32_bf16 v[76:79], a[84:87], a[100:103], v[76:79]\n\t"
    "global_load_dwordx4 a[48:51], v5, s[38:39] offset:1024 sc1\n\t"
    "v_mfma_f32_16x16x32_bf16 v[80:83], a[84:87], a[104:107], v[80:83]\n\t"
    "v_mfma_f32_16x16x32_bf16 v[84:87], a[88:91], a[92:95], v[84:87]\n\t"
    "global_load_dwordx4 a[52:55], v5, s[38:39] offset:2048 sc1\n\t"
    "v_mfma_f32_16x16x32_bf16 v[88:91], a[88:91], a[96:99], v[88:91]\n\t"
    "v_mfma_f32_16x16x32_bf16 v[92:95], a[88:91], a[100:103], v[92:95]\n\t"
    "v_mfma_f32_16x16x32_bf16 v[96:99], a[88:91], a[104:107], v[96:99]\n\t"
    "s_add_u32 s58, s58, 4096\n\t"
    "s_cmp_eq_u32 s58, 57344\n\t"
    "s_cselect_b32 s58, 0, s58\n\t"
    "v_add_u32 v4, s58, v1\n\t"
    "s_add_u32 s59, s59, 5120\n\t"
    "s_cmp_eq_u32 s59, 71680\n\t"
    "s_cselect_b32 s59, 0, s59\n\t"
    "v_add_u32 v5, s59, v1\n\t"
    "s_waitcnt vmcnt(31)\n\t"
    "v_mfma_f32_16x16x32_bf16 v[20:23], a[108:111], a[128:131], v[20:23]\n\t"
    "global_load_dwordx4 a[72:75], v5, s[38:39] offset:-2048 sc1\n\t"
    "v_mfma_f32_16x16x32_bf16 v[24:27], a[108:111], a[132:135], v[24:27]\n\t"
    "v_mfma_f32_16x16x32_bf16 v[28:31], a[108:111], a[136:139], v[28:31]\n\t"
    "global_load_dwordx4 a[92:95], v4, s[36:37] offset:0 sc1\n\t"
    "v_mfma_f32_16x16x32_bf16 v[32:35], a[108:111], a[140:143], v[32:35]\n\t"
    "v_mfma_f32_16x16x32_bf16 v[36:39], a[112:115], a[128:131], v[36:39]\n\t"
    "global_load_dwordx4 a[96:99], v4, s[36:37] offset:1024 sc1\n\t"
    "v_mfma_f32_16x16x32_bf16 v[40:43], a[112:115], a[132:135], v[40:43]\n\t"
    "v_mfma_f32_16x16x32_bf16 v[44:47], a[112:115], a[136:139], v[44:47]\n\t"
    "global_load_dwordx4 a[100:103], v4, s[36:37] offset:2048 sc1\n\t"
    "v_mfma_f32_16x16x32_bf16 v[48:51], a[112:115], a[140:143], v[48:51]\n\t"
    "v_mfma_f32_16x16x32_bf16 v[52:55], a[116:119], a[128:131], v[52:55]\n\t"
    "global_load_dwordx4 a[104:107], v4, s[36:37] offset:3072 sc1\n\t"
    "v_mfma_f32_16x16x32_bf16 v[56:59], a[116:119], a[132:135], v[56:59]\n\t"
    "v_mfma_f32_16x16x32_bf16 v[60:63], a[116:119], a[136:139], v[60:63]\n\t"
    "global_load_dwordx4 a[76:79], v5, s[38:39] offset:-1024 sc1\n\t"
    "v_mfma_f32_16x16x32_bf16 v[64:67], a[116:119], a[140:143], v[64:67]\n\t"
    "v_mfma_f32_16x16x32_bf16 v[68:71], a[120:123], a[128:131], v[68:71]\n\t"
    "global_load_dwordx4 a[80:83], v5, s[38:39] offset:0 sc1\n\t"
    "v_mfma_f32_16x16x32_bf16 v[72:75], a[120:123], a[132:135], v[72:75]\n\t"
    "v_mfma_f32_16x16x32_bf16 v[76:79], a[120:123], a[136:139], v[76:79]\n\t"
    "global_load_dwordx4 a[84:87], v5, s[38:39] offset:1024 sc1\n\t"
    "v_mfma_f32_16x16x32_bf16 v[80:83], a[120:123], a[140:143], v[80:83]\n\t"
    "v_mfma_f32_16x16x32_bf16 v[84:87], a[124:127], a[128:131], v[84:87]\n\t"
    "global_load_dwordx4 a[88:91], v5, s[38:39] offset:2048 sc1\n\t"
    "v_mfma_f32_16x16x32_bf16 v[88:91], a[124:127], a[132:135], v[88:91]\n\t"
    "v_mfma_f32_16x16x32_bf16 v[92:95], a[124:127], a[136:139], v[92:95]\n\t"
    "v_mfma_f32_16x16x32_bf16 v[96:99], a[124:127], a[140:143], v[96:99]\n\t"
    "s_add_u32 s58, s58, 4096\n\t"
    "s_cmp_eq_u32 s58, 57344\n\t"
    "s_cselect_b32 s58, 0, s58\n\t"
    "v_add_u32 v4, s58, v1\n\t"
    "s_add_u32 s59, s59, 5120\n\t"
    "s_cmp_eq_u32 s59, 71680\n\t"
    "s_cselect_b32 s59, 0, s59\n\t"
    "v_add_u32 v5, s59, v1\n\t"
    "s_waitcnt vmcnt(36)\n\t"
    "v_mfma_f32_16x16x32_bf16 v[20:23], a[144:147], a[164:167], v[20:23]\n\t"
    "global_load_dwordx4 a[108:111], v5, s[38:39] offset:-2048 sc1\n\t"
    "v_mfma_f32_16x16x32_bf16 v[24:27], a[144:147], a[168:171], v[24:27]\n\t"
    "v_mfma_f32_16x16x32_bf16 v[28:31], a[144:147], a[172:175], v[28:31]\n\t"
    "global_load_dwordx4 a[128:131], v4, s[36:37] offset:0 sc1\n\t"
    "v_mfma_f32_16x16x32_bf16 v[32:35], a[144:147], a[176:179], v[32:35]\n\t"
    "v_mfma_f32_16x16x32_bf16 v[36:39], a[148:151], a[164:167], v[36:39]\n\t"
    "global_load_dwordx4 a[132:135], v4, s[36:37] offset:1024 sc1\n\t"
    "v_mfma_f32_16x16x32_bf16 v[40:43], a[148:151], a[168:171], v[40:43]\n\t"
    "v_mfma_f32_16x16x32_bf16 v[44:47], a[148:151], a[172:175], v[44:47]\n\t"
    "global_load_dwordx4 a[136:139], v4, s[36:37] offset:2048 sc1\n\t"
    "v_mfma_f32_16x16x32_bf16 v[48:51], a[148:151], a[176:179], v[48:51]\n\t"
    "v_mfma_f32_16x16x32_bf16 v[52:55], a[152:155], a[164:167], v[52:55]\n\t"
    "global_load_dwordx4 a[140:143], v4, s[36:37] offset:3072 sc1\n\t"
    "v_mfma_f32_16x16x32_bf16 v[56:59], a[152:155], a[168:171], v[56:59]\n\t"
    "v_mfma_f32_16x16x32_bf16 v[60:63], a[152:155], a[172:175], v[60:63]\n\t"
    "global_load_dwordx4 a[112:115], v5, s[38:39] offset:-1024 sc1\n\t"
    "v_mfma_f32_16x16x32_bf16 v[64:67], a[152:155], a[176:179], v[64:67]\n\t"
    "v_mfma_f32_16x16x32_bf16 v[68:71], a[156:159], a[164:167], v[68:71]\n\t"
    "global_load_dwordx4 a[116:119], v5, s[38:39] offset:0 sc1\n\t"
    "v_mfma_f32_16x16x32_bf16 v[72:75], a[156:159], a[168:171], v[72:75]\n\t"
    "v_mfma_f32_16x16x32_bf16 v[76:79], a[156:159], a[172:175], v[76:79]\n\t"
    "global_load_dwordx4 a[120:123], v5, s[38:39] offset:1024 sc1\n\t"
    "v_mfma_f32_16x16x32_bf16 v[80:83], a[156:159], a[176:179], v[80:83]\n\t"
    "v_mfma_f32_16x16x32_bf16 v[84:87], a[160:163], a[164:167], v[84:87]\n\t"
    "global_load_dwordx4 a[124:127], v5, s[38:39] offset:2048 sc1\n\t"
    "v_mfma_f32_16x16x32_bf16 v[88:91], a[160:163], a[168:171], v[88:91]\n\t"
    "v_mfma_f32_16x16x32_bf16 v[92:95], a[160:163], a[172:175], v[92:95]\n\t"
    "v_mfma_f32_16x16x32_bf16 v[96:99], a[160:163], a[176:179], v[96:99]\n\t"
    "s_add_u32 s58, s58, 4096\n\t"
    "s_cmp_eq_u32 s58, 57344\n\t"
    "s_cselect_b32 s58, 0, s58\n\t"
    "v_add_u32 v4, s58, v1\n\t"
    "s_add_u32 s59, s59, 5120\n\t"
    "s_cmp_eq_u32 s59, 71680\n\t"
    "s_cselect_b32 s59, 0, s59\n\t"
    "v_add_u32 v5, s59, v1\n\t"
    "s_waitcnt vmcnt(40)\n\t"
    "v_mfma_f32_16x16x32_bf16 v[20:23], a[180:183], a[200:203], v[20:23]\n\t"
    "global_load_dwordx4 a[144:147], v5, s[38:39] offset:-2048 sc1\n\t"
    "v_mfma_f32_16x16x32_bf16 v[24:27], a[180:183], a[204:207], v[24:27]\n\t"
    "v_mfma_f32_16x16x32_bf16 v[28:31], a[180:183], a[208:211], v[28:31]\n\t"
    "global_load_dwordx4 a[164:167], v4, s[36:37] offset:0 sc1\n\t"
    "v_mfma_f32_16x16x32_bf16 v[32:35], a[180:183], a[212:215], v[32:35]\n\t"
    "s_waitcnt vmcnt(38)\n\t"
    "v_mfma_f32_16x16x32_bf16 v[36:39], a[184:187], a[200:203], v[36:39]\n\t"
    "global_load_dwordx4 a[168:171], v4, s[36:37] offset:1024 sc1\n\t"
    "v_mfma_f32_16x16x32_bf16 v[40:43], a[184:187], a[204:207], v[40:43]\n\t"
    "v_mfma_f32_16x16x32_bf16 v[44:47], a[184:187], a[208:211], v[44:47]\n\t"
    "global_load_dwordx4 a[172:175], v4, s[36:37] offset:2048 sc1\n\t"
    "v_mfma_f32_16x16x32_bf16 v[48:51], a[184:187], a[212:215], v[48:51]\n\t"
    "v_mfma_f32_16x16x32_bf16 v[52:55], a[188:191], a[200:203], v[52:55]\n\t"
    "global_load_dwordx4 a[176:179], v4, s[36:37] offset:3072 sc1\n\t"
    "v_mfma_f32_16x16x32_bf16 v[56:59], a[188:191], a[204:207], v[56:59]\n\t"
    "v_mfma_f32_16x16x32_bf16 v[60:63], a[188:191], a[208:211], v[60:63]\n\t"
    "global_load_dwordx4 a[148:151], v5, s[38:39] offset:-1024 sc1\n\t"
    "v_mfma_f32_16x16x32_bf16 v[64:67], a[188:191], a[212:215], v[64:67]\n\t"
    "v_mfma_f32_16x16x32_bf16 v[68:71], a[192:195], a[200:203], v[68:71]\n\t"
    "global_load_dwordx4 a[152:155], v5, s[38:39] offset:0 sc1\n\t"
    "v_mfma_f32_16x16x32_bf16 v[72:75], a[192:195], a[204:207], v[72:75]\n\t"
    "v_mfma_f32_16x16x32_bf16 v[76:79], a[192:195], a[208:211], v[76:79]\n\t"
    "global_load_dwordx4 a[156:159], v5, s[38:39] offset:1024 sc1\n\t"
    "v_mfma_f32_16x16x32_bf16 v[80:83], a[192:195], a[212:215], v[80:83]\n\t"
    "v_mfma_f32_16x16x32_bf16 v[84:87], a[196:199], a[200:203], v[84:87]\n\t"
    "global_load_dwordx4 a[160:163], v5, s[38:39] offset:2048 sc1\n\t"
    "v_mfma_f32_16x16x32_bf16 v[88:91], a[196:199], a[204:207], v[88:91]\n\t"
    "v_mfma_f32_16x16x32_bf16 v[92:95], a[196:199], a[208:211], v[92:95]\n\t"
    "v_mfma_f32_16x16x32_bf16 v[96:99], a[196:199], a[212:215], v[96:99]\n\t"
    "s_add_u32 s58, s58, 4096\n\t"
    "s_cmp_eq_u32 s58, 57344\n\t"
    "s_cselect_b32 s58, 0, s58\n\t"
    "v_add_u32 v4, s58, v1\n\t"
    "s_add_u32 s59, s59, 5120\n\t"
    "s_cmp_eq_u32 s59, 71680\n\t"
    "s_cselect_b32 s59, 0, s59\n\t"
    "v_add_u32 v5, s59, v1\n\t"
    "s_waitcnt vmcnt(40)\n\t"
    "v_mfma_f32_16x16x32_bf16 v[20:23], a[0:3], a[20:23], v[20:23]\n\t"
    "global_load_dwordx4 a[180:183], v5, s[38:39] offset:-2048 sc1\n\t"
    "v_mfma_f32_16x16x32_bf16 v[24:27], a[0:3], a[24:27], v[24:27]\n\t"
    "v_mfma_f32_16x16x32_bf16 v[28:31], a[0:3], a[28:31], v[28:31]\n\t"
    "global_load_dwordx4 a[200:203], v4, s[36:37] offset:0 sc1\n\t"
    "v_mfma_f32_16x16x32_bf16 v[32:35], a[0:3], a[32:35], v[32:35]\n\t"
    "s_waitcnt vmcnt(38)\n\t"
    "v_mfma_f32_16x16x32_bf16 v[36:39], a[4:7], a[20:23], v[36:39]\n\t"
    "global_load_dwordx4 a[204:207], v4, s[36:37] offset:1024 sc1\n\t"
    "v_mfma_f32_16x16x32_bf16 v[40:43], a[4:7], a[24:27], v[40:43]\n\t"
    "v_mfma_f32_16x16x32_bf16 v[44:47], a[4:7], a[28:31], v[44:47]\n\t"
    "global_load_dwordx4 a[208:211], v4, s[36:37] offset:2048 sc1\n\t"
    "v_mfma_f32_16x16x32_bf16 v[48:51], a[4:7], a[32:35], v[48:51]\n\t"
    "v_mfma_f32_16x16x32_bf16 v[52:55], a[8:11], a[20:23], v[52:55]\n\t"
    "global_load_dwordx4 a[212:215], v4, s[36:37] offset:3072 sc1\n\t"
    "v_mfma_f32_16x16x32_bf16 v[56:59], a[8:11], a[24:27], v[56:59]\n\t"
    "v_mfma_f32_16x16x32_bf16 v[60:63], a[8:11], a[28:31], v[60:63]\n\t"
    "global_load_dwordx4 a[184:187], v5, s[38:39] offset:-1024 sc1\n\t"
    "v_mfma_f32_16x16x32_bf16 v[64:67], a[8:11], a[32:35], v[64:67]\n\t"
    "v_mfma_f32_16x16x32_bf16 v[68:71], a[12:15], a[20:23], v[68:71]\n\t"
    "global_load_dwordx4 a[188:191], v5, s[38:39] offset:0 sc1\n\t"
    "v_mfma_f32_16x16x32_bf16 v[72:75], a[12:15], a[24:27], v[72:75]\n\t"
    "v_mfma_f32_16x16x32_bf16 v[76:79], a[12:15], a[28:31], v[76:79]\n\t"
    "global_load_dwordx4 a[192:195], v5, s[38:39] offset:1024 sc1\n\t"
    "v_mfma_f32_16x16x32_bf16 v[80:83], a[12:15], a[32:35], v[80:83]\n\t"
    "v_mfma_f32_16x16x32_bf16 v[84:87], a[16:19], a[20:23], v[84:87]\n\t"
    "global_load_dwordx4 a[196:199], v5, s[38:39] offset:2048 sc1\n\t"
    "v_mfma_f32_16x16x32_bf16 v[88:91], a[16:19], a[24:27], v[88:91]\n\t"
    "v_mfma_f32_16x16x32_bf16 v[92:95], a[16:19], a[28:31], v[92:95]\n\t"
    "v_mfma_f32_16x16x32_bf16 v[96:99], a[16:19], a[32:35], v[96:99]\n\t"
    "s_add_u32 s58, s58, 4096\n\t"
    "s_cmp_eq_u32 s58, 57344\n\t"
    "s_cselect_b32 s58, 0, s58\n\t"
    "v_add_u32 v4, s58, v1\n\t"
    "s_add_u32 s59, s59, 5120\n\t"
    "s_cmp_eq_u32 s59, 71680\n\t"
    "s_cselect_b32 s59, 0, s59\n\t"
    "v_add_u32 v5, s59, v1\n\t"
    "s_waitcnt vmcnt(40)\n\t"
    "v_mfma_f32_16x16x32_bf16 v[20:23], a[36:39], a[56:59], v[20:23]\n\t"
    "global_load_dwordx4 a[0:3], v5, s[38:39] offset:-2048 sc1\n\t"
    "v_mfma_f32_16x16x32_bf16 v[24:27], a[36:39], a[60:63], v[24:27]\n\t"
    "v_mfma_f32_16x16x32_bf16 v[28:31], a[36:39], a[64:67], v[28:31]\n\t"
    "global_load_dwordx4 a[20:23], v4, s[36:37] offset:0 sc1\n\t"
    "v_mfma_f32_16x16x32_bf16 v[32:35], a[36:39], a[68:71], v[32:35]\n\t"
    "s_waitcnt vmcnt(38)\n\t"
    "v_mfma_f32_16x16x32_bf16 v[36:39], a[40:43], a[56:59], v[36:39]\n\t"
    "global_load_dwordx4 a[24:27], v4, s[36:37] offset:1024 sc1\n\t"
    "v_mfma_f32_16x16x32_bf16 v[40:43], a[40:43], a[60:63], v[40:43]\n\t"
    "v_mfma_f32_16x16x32_bf16 v[44:47], a[40:43], a[64:67], v[44:47]\n\t"
    "global_load_dwordx4 a[28:31], v4, s[36:37] offset:2048 sc1\n\t"
    "v_mfma_f32_16x16x32_bf16 v[48:51], a[40:43], a[68:71], v[48:51]\n\t"
    "v_mfma_f32_16x16x32_bf16 v[52:55], a[44:47], a[56:59], v[52:55]\n\t"
    "global_load_dwordx4 a[32:35], v4, s[36:37] offset:3072 sc1\n\t"
    "v_mfma_f32_16x16x32_bf16 v[56:59], a[44:47], a[60:63], v[56:59]\n\t"
    "v_mfma_f32_16x16x32_bf16 v[60:63], a[44:47], a[64:67], v[60:63]\n\t"
    "global_load_dwordx4 a[4:7], v5, s[38:39] offset:-1024 sc1\n\t"
    "v_mfma_f32_16x16x32_bf16 v[64:67], a[44:47], a[68:71], v[64:67]\n\t"
    "v_mfma_f32_16x16x32_bf16 v[68:71], a[48:51], a[56:59], v[68:71]\n\t"
    "global_load_dwordx4 a[8:11], v5, s[38:39] offset:0 sc1\n\t"
    "v_mfma_f32_16x16x32_bf16 v[72:75], a[48:51], a[60:63], v[72:75]\n\t"
    "v_mfma_f32_16x16x32_bf16 v[76:79], a[48:51], a[64:67], v[76:79]\n\t"
    "global_load_dwordx4 a[12:15], v5, s[38:39] offset:1024 sc1\n\t"
    "v_mfma_f32_16x16x32_bf16 v[80:83], a[48:51], a[68:71], v[80:83]\n\t"
    "v_mfma_f32_16x16x32_bf16 v[84:87], a[52:55], a[56:59], v[84:87]\n\t"
    "global_load_dwordx4 a[16:19], v5, s[38:39] offset:2048 sc1\n\t"
    "v_mfma_f32_16x16x32_bf16 v[88:91], a[52:55], a[60:63], v[88:91]\n\t"
    "v_mfma_f32_16x16x32_bf16 v[92:95], a[52:55], a[64:67], v[92:95]\n\t"
    "v_mfma_f32_16x16x32_bf16 v[96:99], a[52:55], a[68:71], v[96:99]\n\t"
    "s_add_u32 s58, s58, 4096\n\t"
    "s_cmp_eq_u32 s58, 57344\n\t"
    "s_cselect_b32 s58, 0, s58\n\t"
    "v_add_u32 v4, s58, v1\n\t"
    "s_add_u32 s59, s59, 5120\n\t"
    "s_cmp_eq_u32 s59, 71680\n\t"
    "s_cselect_b32 s59, 0, s59\n\t"
    "v_add_u32 v5, s59, v1\n\t"
    "s_waitcnt vmcnt(40)\n\t"
    "v_mfma_f32_16x16x32_bf16 v[20:23], a[72:75], a[92:95], v[20:23]\n\t"
    "global_load_dwordx4 a[36:39], v5, s[38:39] offset:-2048 sc1\n\t"
    "v_mfma_f32_16x16x32_bf16 v[24:27], a[72:75], a[96:99], v[24:27]\n\t"
    "v_mfma_f32_16x16x32_bf16 v[28:31], a[72:75], a[100:103], v[28:31]\n\t"
    "global_load_dwordx4 a[56:59], v4, s[36:37] offset:0 sc1\n\t"
    "v_mfma_f32_16x16x32_bf16 v[32:35], a[72:75], a[104:107], v[32:35]\n\t"
    "s_waitcnt vmcnt(38)\n\t"
    "v_mfma_f32_16x16x32_bf16 v[36:39], a[76:79], a[92:95], v[36:39]\n\t"
    "global_load_dwordx4 a[60:63], v4, s[36:37] offset:1024 sc1\n\t"
    "v_mfma_f32_16x16x32_bf16 v[40:43], a[76:79], a[96:99], v[40:43]\n\t"
    "v_mfma_f32_16x16x32_bf16 v[44:47], a[76:79], a[100:103], v[44:47]\n\t"
    "global_load_dwordx4 a[64:67], v4, s[36:37] offset:2048 sc1\n\t"
    "v_mfma_f32_16x16x32_bf16 v[48:51], a[76:79], a[104:107], v[48:51]\n\t"
    "v_mfma_f32_16x16x32_bf16 v[52:55], a[80:83], a[92:95], v[52:55]\n\t"
    "global_load_dwordx4 a[68:71], v4, s[36:37] offset:3072 sc1\n\t"
    "v_mfma_f32_16x16x32_bf16 v[56:59], a[80:83], a[96:99], v[56:59]\n\t"
    "v_mfma_f32_16x16x32_bf16 v[60:63], a[80:83], a[100:103], v[60:63]\n\t"
    "global_load_dwordx4 a[40:43], v5, s[38:39] offset:-1024 sc1\n\t"
    "v_mfma_f32_16x16x32_bf16 v[64:67], a[80:83], a[104:107], v[64:67]\n\t"
    "v_mfma_f32_16x16x32_bf16 v[68:71], a[84:87], a[92:95], v[68:71]\n\t"
    "global_load_dwordx4 a[44:47], v5, s[38:39] offset:0 sc1\n\t"
    "v_mfma_f32_16x16x32_bf16 v[72:75], a[84:87], a[96:99], v[72:75]\n\t"
    "v_mfma_f32_16x16x32_bf16 v[76:79], a[84:87], a[100:103], v[76:79]\n\t"
    "global_load_dwordx4 a[48:51], v5, s[38:39] offset:1024 sc1\n\t"
    "v_mfma_f32_16x16x32_bf16 v[80:83], a[84:87], a[104:107], v[80:83]\n\t"
    "v_mfma_f32_16x16x32_bf16 v[84:87], a[88:91], a[92:95], v[84:87]\n\t"
    "global_load_dwordx4 a[52:55], v5, s[38:39] offset:2048 sc1\n\t"
    "v_mfma_f32_16x16x32_bf16 v[88:91], a[88:91], a[96:99], v[88:91]\n\t"
    "v_mfma_f32_16x16x32_bf16 v[92:95], a[88:91], a[100:103], v[92:95]\n\t"
    "v_mfma_f32_16x16x32_bf16 v[96:99], a[88:91], a[104:107], v[96:99]\n\t"
    "s_add_u32 s58, s58, 4096\n\t"
    "s_cmp_eq_u32 s58, 57344\n\t"
    "s_cselect_b32 s58, 0, s58\n\t"
    "v_add_u32 v4, s58, v1\n\t"
    "s_add_u32 s59, s59, 5120\n\t"
    "s_cmp_eq_u32 s59, 71680\n\t"
    "s_cselect_b32 s59, 0, s59\n\t"
    "v_add_u32 v5, s59, v1\n\t"
    "s_waitcnt vmcnt(40)\n\t"
    "v_mfma_f32_16x16x32_bf16 v[20:23], a[108:111], a[128:131], v[20:23]\n\t"
    "global_load_dwordx4 v[100:103], v10, s[40:41] offset:0 sc1\n\t"
    "v_mfma_f32_16x16x32_bf16 v[24:27], a[108:111], a[132:135], v[24:27]\n\t"
    "v_mfma_f32_16x16x32_bf16 v[28:31], a[108:111], a[136:139], v[28:31]\n\t"
    "global_load_dwordx4 v[112:115], v12, s[42:43] offset:0 sc1\n\t"
    "v_mfma_f32_16x16x32_bf16 v[32:35], a[108:111], a[140:143], v[32:35]\n\t"
    "s_waitcnt vmcnt(38)\n\t"
    "v_mfma_f32_16x16x32_bf16 v[36:39], a[112:115], a[128:131], v[36:39]\n\t"
    "global_load_dwordx4 v[116:119], v12, s[42:43] offset:64 sc1\n\t"
    "v_mfma_f32_16x16x32_bf16 v[40:43], a[112:115], a[132:135], v[40:43]\n\t"
    "v_mfma_f32_16x16x32_bf16 v[44:47], a[112:115], a[136:139], v[44:47]\n\t"
    "global_load_dwordx4 v[104:107], v10, s[40:41] offset:1024 sc1\n\t"
    "v_mfma_f32_16x16x32_bf16 v[48:51], a[112:115], a[140:143], v[48:51]\n\t"
    "v_mfma_f32_16x16x32_bf16 v[52:55], a[116:119], a[128:131], v[52:55]\n\t"
    "global_load_dwordx4 v[120:123], v12, s[42:43] offset:128 sc1\n\t"
    "v_mfma_f32_16x16x32_bf16 v[56:59], a[116:119], a[132:135], v[56:59]\n\t"
    "v_mfma_f32_16x16x32_bf16 v[60:63], a[116:119], a[136:139], v[60:63]\n\t"
    "global_load_dwordx4 v[124:127], v12, s[42:43] offset:192 sc1\n\t"
    "v_mfma_f32_16x16x32_bf16 v[64:67], a[116:119], a[140:143], v[64:67]\n\t"
    "v_mfma_f32_16x16x32_bf16 v[68:71], a[120:123], a[128:131], v[68:71]\n\t"
    "global_load_dwordx2 v[108:109], v11, s[40:41] sc1\n\t"
    "v_mfma_f32_16x16x32_bf16 v[72:75], a[120:123], a[132:135], v[72:75]\n\t"
    "v_mfma_f32_16x16x32_bf16 v[76:79], a[120:123], a[136:139], v[76:79]\n\t"
    "global_load_dwordx4 v[128:131], v13, s[42:43] sc1\n\t"
    "v_mfma_f32_16x16x32_bf16 v[80:83], a[120:123], a[140:143], v[80:83]\n\t"
    "v_mfma_f32_16x16x32_bf16 v[84:87], a[124:127], a[128:131], v[84:87]\n\t"
    "v_mfma_f32_16x16x32_bf16 v[88:91], a[124:127], a[132:135], v[88:91]\n\t"
    "v_mfma_f32_16x16x32_bf16 v[92:95], a[124:127], a[136:139], v[92:95]\n\t"
    "v_mfma_f32_16x16x32_bf16 v[96:99], a[124:127], a[140:143], v[96:99]\n\t"
    "s_waitcnt vmcnt(39)\n\t"
    "v_mfma_f32_16x16x32_bf16 v[20:23], a[144:147], a[164:167], v[20:23]\n\t"
    "v_mfma_f32_16x16x32_bf16 v[24:27], a[144:147], a[168:171], v[24:27]\n\t"
    "v_mfma_f32_16x16x32_bf16 v[28:31], a[144:147], a[172:175], v[28:31]\n\t"
    "v_mfma_f32_16x16x32_bf16 v[32:35], a[144:147], a[176:179], v[32:35]\n\t"
    "s_waitcnt vmcnt(35)\n\t"
    "v_mfma_f32_16x16x32_bf16 v[36:39], a[148:151], a[164:167], v[36:39]\n\t"
    "v_mfma_f32_16x16x32_bf16 v[40:43], a[148:151], a[168:171], v[40:43]\n\t"
    "v_mfma_f32_16x16x32_bf16 v[44:47], a[148:151], a[172:175], v[44:47]\n\t"
    "v_mfma_f32_16x16x32_bf16 v[48:51], a[148:151], a[176:179], v[48:51]\n\t"
    "v_mfma_f32_16x16x32_bf16 v[52:55], a[152:155], a[164:167], v[52:55]\n\t"
    "v_mfma_f32_16x16x32_bf16 v[56:59], a[152:155], a[168:171], v[56:59]\n\t"
    "v_mfma_f32_16x16x32_bf16 v[60:63], a[152:155], a[172:175], v[60:63]\n\t"
    "v_mfma_f32_16x16x32_bf16 v[64:67], a[152:155], a[176:179], v[64:67]\n\t"
    "v_mfma_f32_16x16x32_bf16 v[68:71], a[156:159], a[164:167], v[68:71]\n\t"
    "v_mfma_f32_16x16x32_bf16 v[72:75], a[156:159], a[168:171], v[72:75]\n\t"
    "v_mfma_f32_16x16x32_bf16 v[76:79], a[156:159], a[172:175], v[76:79]\n\t"
    "v_mfma_f32_16x16x32_bf16 v[80:83], a[156:159], a[176:179], v[80:83]\n\t"
    "v_mfma_f32_16x16x32_bf16 v[84:87], a[160:163], a[164:167], v[84:87]\n\t"
    "v_mfma_f32_16x16x32_bf16 v[88:91], a[160:163], a[168:171], v[88:91]\n\t"
    "v_mfma_f32_16x16x32_bf16 v[92:95], a[160:163], a[172:175], v[92:95]\n\t"
    "v_mfma_f32_16x16x32_bf16 v[96:99], a[160:163], a[176:179], v[96:99]\n\t"
    "s_waitcnt vmcnt(30)\n\t"
    "v_mfma_f32_16x16x32_bf16 v[20:23], a[180:183], a[200:203], v[20:23]\n\t"
    "v_mfma_f32_16x16x32_bf16 v[24:27], a[180:183], a[204:207], v[24:27]\n\t"
    "v_mfma_f32_16x16x32_bf16 v[28:31], a[180:183], a[208:211], v[28:31]\n\t"
    "v_mfma_f32_16x16x32_bf16 v[32:35], a[180:183], a[212:215], v[32:35]\n\t"
    "s_waitcnt vmcnt(26)\n\t"
    "v_mfma_f32_16x16x32_bf16 v[36:39], a[184:187], a[200:203], v[36:39]\n\t"
    "v_mfma_f32_16x16x32_bf16 v[40:43], a[184:187], a[204:207], v[40:43]\n\t"
    "v_mfma_f32_16x16x32_bf16 v[44:47], a[184:187], a[208:211], v[44:47]\n\t"
    "v_mfma_f32_16x16x32_bf16 v[48:51], a[184:187], a[212:215], v[48:51]\n\t"
    "v_mfma_f32_16x16x32_bf16 v[52:55], a[188:191], a[200:203], v[52:55]\n\t"
    "v_mfma_f32_16x16x32_bf16 v[56:59], a[188:191], a[204:207], v[56:59]\n\t"
    "v_mfma_f32_16x16x32_bf16 v[60:63], a[188:191], a[208:211], v[60:63]\n\t"
    "v_mfma_f32_16x16x32_bf16 v[64:67], a[188:191], a[212:215], v[64:67]\n\t"
    "v_mfma_f32_16x16x32_bf16 v[68:71], a[192:195], a[200:203], v[68:71]\n\t"
    "v_mfma_f32_16x16x32_bf16 v[72:75], a[192:195], a[204:207], v[72:75]\n\t"
    "v_mfma_f32_16x16x32_bf16 v[76:79], a[192:195], a[208:211], v[76:79]\n\t"
    "v_mfma_f32_16x16x32_bf16 v[80:83], a[192:195], a[212:215], v[80:83]\n\t"
    "v_mfma_f32_16x16x32_bf16 v[84:87], a[196:199], a[200:203], v[84:87]\n\t"
    "v_mfma_f32_16x16x32_bf16 v[88:91], a[196:199], a[204:207], v[88:91]\n\t"
    "v_mfma_f32_16x16x32_bf16 v[92:95], a[196:199], a[208:211], v[92:95]\n\t"
    "v_mfma_f32_16x16x32_bf16 v[96:99], a[196:199], a[212:215], v[96:99]\n\t"
    "s_waitcnt vmcnt(21)\n\t"
    "v_mfma_f32_16x16x32_bf16 v[20:23], a[0:3], a[20:23], v[20:23]\n\t"
    "v_mfma_f32_16x16x32_bf16 v[24:27], a[0:3], a[24:27], v[24:27]\n\t"
    "v_mfma_f32_16x16x32_bf16 v[28:31], a[0:3], a[28:31], v[28:31]\n\t"
    "v_mfma_f32_16x16x32_bf16 v[32:35], a[0:3], a[32:35], v[32:35]\n\t"
    "s_waitcnt vmcnt(17)\n\t"
    "v_mfma_f32_16x16x32_bf16 v[36:39], a[4:7], a[20:23], v[36:39]\n\t"
    "v_mfma_f32_16x16x32_bf16 v[40:43], a[4:7], a[24:27], v[40:43]\n\t"
    "v_mfma_f32_16x16x32_bf16 v[44:47], a[4:7], a[28:31], v[44:47]\n\t"
    "v_mfma_f32_16x16x32_bf16 v[48:51], a[4:7], a[32:35], v[48:51]\n\t"
    "v_mfma_f32_16x16x32_bf16 v[52:55], a[8:11], a[20:23], v[52:55]\n\t"
    "v_mfma_f32_16x16x32_bf16 v[56:59], a[8:11], a[24:27], v[56:59]\n\t"
    "v_mfma_f32_16x16x32_bf16 v[60:63], a[8:11], a[28:31], v[60:63]\n\t"
    "v_mfma_f32_16x16x32_bf16 v[64:67], a[8:11], a[32:35], v[64:67]\n\t"
    "v_mfma_f32_16x16x32_bf16 v[68:71], a[12:15], a[20:23], v[68:71]\n\t"
    "v_mfma_f32_16x16x32_bf16 v[72:75], a[12:15], a[24:27], v[72:75]\n\t"
    "v_mfma_f32_16x16x32_bf16 v[76:79], a[12:15], a[28:31], v[76:79]\n\t"
    "v_mfma_f32_16x16x32_bf16 v[80:83], a[12:15], a[32:35], v[80:83]\n\t"
    "v_mfma_f32_16x16x32_bf16 v[84:87], a[16:19], a[20:23], v[84:87]\n\t"
    "v_mfma_f32_16x16x32_bf16 v[88:91], a[16:19], a[24:27], v[88:91]\n\t"
    "v_mfma_f32_16x16x32_bf16 v[92:95], a[16:19], a[28:31], v[92:95]\n\t"
    "v_mfma_f32_16x16x32_bf16 v[96:99], a[16:19], a[32:35], v[96:99]\n\t"
    "s_waitcnt vmcnt(12)\n\t"
    "v_mfma_f32_16x16x32_bf16 v[20:23], a[36:39], a[56:59], v[20:23]\n\t"
    "v_mfma_f32_16x16x32_bf16 v[24:27], a[36:39], a[60:63], v[24:27]\n\t"
    "v_mfma_f32_16x16x32_bf16 v[28:31], a[36:39], a[64:67], v[28:31]\n\t"
    "v_mfma_f32_16x16x32_bf16 v[32:35], a[36:39], a[68:71], v[32:35]\n\t"
    "s_waitcnt vmcnt(8)\n\t"
    "v_mfma_f32_16x16x32_bf16 v[36:39], a[40:43], a[56:59], v[36:39]\n\t"
    "v_mfma_f32_16x16x32_bf16 v[40:43], a[40:43], a[60:63], v[40:43]\n\t"
    "v_mfma_f32_16x16x32_bf16 v[44:47], a[40:43], a[64:67], v[44:47]\n\t"
    "v_mfma_f32_16x16x32_bf16 v[48:51], a[40:43], a[68:71], v[48:51]\n\t"
    "v_mfma_f32_16x16x32_bf16 v[52:55], a[44:47], a[56:59], v[52:55]\n\t"
    "ds_write_b128 v8, v[20:23] offset:0\n\t"
    "v_mfma_f32_16x16x32_bf16 v[56:59], a[44:47], a[60:63], v[56:59]\n\t"
    "ds_write_b128 v8, v[24:27] offset:1024\n\t"
    "v_mfma_f32_16x16x32_bf16 v[60:63], a[44:47], a[64:67], v[60:63]\n\t"
    "ds_write_b128 v8, v[28:31] offset:2048\n\t"
    "v_mfma_f32_16x16x32_bf16 v[64:67], a[44:47], a[68:71], v[64:67]\n\t"
    "ds_write_b128 v8, v[32:35] offset:3072\n\t"
    "v_mfma_f32_16x16x32_bf16 v[68:71], a[48:51], a[56:59], v[68:71]\n\t"
    "ds_write_b128 v8, v[36:39] offset:4096\n\t"
    "v_mfma_f32_16x16x32_bf16 v[72:75], a[48:51], a[60:63], v[72:75]\n\t"
    "ds_write_b128 v8, v[40:43] offset:5120\n\t"
    "v_mfma_f32_16x16x32_bf16 v[76:79], a[48:51], a[64:67], v[76:79]\n\t"
    "ds_write_b128 v8, v[44:47] offset:6144\n\t"
    "v_mfma_f32_16x16x32_bf16 v[80:83], a[48:51], a[68:71], v[80:83]\n\t"
    "ds_write_b128 v8, v[48:51] offset:7168\n\t"
    "v_mfma_f32_16x16x32_bf16 v[84:87], a[52:55], a[56:59], v[84:87]\n\t"
    "ds_write_b128 v8, v[52:55] offset:8192\n\t"
    "v_mfma_f32_16x16x32_bf16 v[88:91], a[52:55], a[60:63], v[88:91]\n\t"
    "ds_write_b128 v8, v[56:59] offset:9216\n\t"
    "v_mfma_f32_16x16x32_bf16 v[92:95], a[52:55], a[64:67], v[92:95]\n\t"
    "ds_write_b128 v8, v[60:63] offset:10240\n\t"
    "v_mfma_f32_16x16x32_bf16 v[96:99], a[52:55], a[68:71], v[96:99]\n\t"
    "ds_write_b128 v8, v[64:67] offset:11264\n\t"
    "ds_write_b128 v8, v[68:71] offset:12288\n\t"
    "ds_write_b128 v8, v[72:75] offset:13312\n\t"
    "ds_write_b128 v8, v[76:79] offset:14336\n\t"
    "s_waitcnt lgkmcnt(14)\n\t"
    "ds_write_b128 v8, v[80:83] offset:15360\n\t"
    "s_waitcnt lgkmcnt(14)\n\t"
    "ds_write_b128 v8, v[84:87] offset:16384\n\t"
    "s_waitcnt lgkmcnt(14)\n\t"
    "ds_write_b128 v8, v[88:91] offset:17408\n\t"
    "s_waitcnt lgkmcnt(14)\n\t"
    "ds_write_b128 v8, v[92:95] offset:18432\n\t"
    "s_waitcnt lgkmcnt(14)\n\t"
    "ds_write_b128 v8, v[96:99] offset:19456\n\t"
    "s_waitcnt lgkmcnt(0)\n\t"
    "s_barrier\n\t"
    "ds_read_b128 v[152:155], v9 offset:0\n\t"
    "ds_read_b128 v[156:159], v9 offset:20480\n\t"
    "ds_read_b128 v[160:163], v9 offset:40960\n\t"
    "ds_read_b128 v[164:167], v17 offset:0\n\t"
    "ds_read_b128 v[168:171], v9 offset:4096\n\t"
    "ds_read_b128 v[172:175], v9 offset:24576\n\t"
    "ds_read_b128 v[176:179], v9 offset:45056\n\t"
    "ds_read_b128 v[180:183], v17 offset:4096\n\t"
    "ds_read_b128 v[184:187], v9 offset:8192\n\t"
    "ds_read_b128 v[188:191], v9 offset:28672\n\t"
    "ds_read_b128 v[192:195], v9 offset:49152\n\t"
    "ds_read_b128 v[196:199], v17 offset:8192\n\t"
    "ds_read_b128 v[200:203], v9 offset:12288\n\t"
    "ds_read_b128 v[204:207], v9 offset:32768\n\t"
    "ds_read_b128 v[208:211], v9 offset:53248\n\t"
    "s_waitcnt lgkmcnt(14)\n\t"
    "ds_read_b128 v[212:215], v17 offset:12288\n\t"
    "s_waitcnt lgkmcnt(14)\n\t"
    "ds_read_b128 v[216:219], v9 offset:16384\n\t"
    "s_waitcnt lgkmcnt(14)\n\t"
    "ds_read_b128 v[220:223], v9 offset:36864\n\t"
    "s_waitcnt lgkmcnt(14)\n\t"
    "ds_read_b128 v[224:227], v9 offset:57344\n\t"
    "s_waitcnt lgkmcnt(14)\n\t"
    "ds_read_b128 v[228:231], v17 offset:16384\n\t"
    "v_pk_add_f32 v[152:153], v[152:153], v[156:157]\n\t"
    "v_pk_add_f32 v[154:155], v[154:155], v[158:159]\n\t"
    "v_pk_add_f32 v[160:161], v[160:161], v[164:165]\n\t"
    "v_pk_add_f32 v[162:163], v[162:163], v[166:167]\n\t"
    "v_pk_add_f32 v[152:153], v[152:153], v[160:161]\n\t"
    "v_pk_add_f32 v[154:155], v[154:155], v[162:163]\n\t"
    "s_waitcnt vmcnt(7)\n\t"
    "v_lshlrev_b32 v156, 16, v100\n\t"
    "v_and_b32 v157, 0xffff0000, v100\n\t"
    "v_lshlrev_b32 v158, 16, v101\n\t"
    "v_and_b32 v159, 0xffff0000, v101\n\t"
    "s_waitcnt vmcnt(6)\n\t"
    "v_pk_fma_f32 v[152:153], v[152:153], v[112:113], v[156:157]\n\t"
    "v_pk_fma_f32 v[154:155], v[154:155], v[114:115], v[158:159]\n\t"
    "v_cvt_pk_bf16_f32 v20, v152, v153\n\t"
    "v_cvt_pk_bf16_f32 v21, v154, v155\n\t"
    "s_waitcnt lgkmcnt(14)\n\t"
    "v_pk_add_f32 v[168:169], v[168:169], v[172:173]\n\t"
    "v_pk_add_f32 v[170:171], v[170:171], v[174:175]\n\t"
    "s_waitcnt lgkmcnt(12)\n\t"
    "v_pk_add_f32 v[176:177], v[176:177], v[180:181]\n\t"
    "v_pk_add_f32 v[178:179], v[178:179], v[182:183]\n\t"
    "v_pk_add_f32 v[168:169], v[168:169], v[176:177]\n\t"
    "v_pk_add_f32 v[170:171], v[170:171], v[178:179]\n\t"
    "v_lshlrev_b32 v172, 16, v102\n\t"
    "v_and_b32 v173, 0xffff0000, v102\n\t"
    "v_lshlrev_b32 v174, 16, v103\n\t"
    "v_and_b32 v175, 0xffff0000, v103\n\t"
    "s_waitcnt vmcnt(5)\n\t"
    "v_pk_fma_f32 v[168:169], v[168:169], v[116:117], v[172:173]\n\t"
    "v_pk_fma_f32 v[170:171], v[170:171], v[118:119], v[174:175]\n\t"
    "v_cvt_pk_bf16_f32 v22, v168, v169\n\t"
    "v_cvt_pk_bf16_f32 v23, v170, v171\n\t"
    "s_waitcnt lgkmcnt(10)\n\t"
    "v_pk_add_f32 v[184:185], v[184:185], v[188:189]\n\t"
    "v_pk_add_f32 v[186:187], v[186:187], v[190:191]\n\t"
    "s_waitcnt lgkmcnt(8)\n\t"
    "v_pk_add_f32 v[192:193], v[192:193], v[196:197]\n\t"
    "v_pk_add_f32 v[194:195], v[194:195], v[198:199]\n\t"
    "v_pk_add_f32 v[184:185], v[184:185], v[192:193]\n\t"
    "v_pk_add_f32 v[186:187], v[186:187], v[194:195]\n\t"
    "s_waitcnt vmcnt(4)\n\t"
    "v_lshlrev_b32 v188, 16, v104\n\t"
    "v_and_b32 v189, 0xffff0000, v104\n\t"
    "v_lshlrev_b32 v190, 16, v105\n\t"
    "v_and_b32 v191, 0xffff0000, v105\n\t"
    "s_waitcnt vmcnt(3)\n\t"
    "v_pk_fma_f32 v[184:185], v[184:185], v[120:121], v[188:189]\n\t"
    "v_pk_fma_f32 v[186:187], v[186:187], v[122:123], v[190:191]\n\t"
    "v_cvt_pk_bf16_f32 v24, v184, v185\n\t"
    "v_cvt_pk_bf16_f32 v25, v186, v187\n\t"
    "s_waitcnt lgkmcnt(6)\n\t"
    "v_pk_add_f32 v[200:201], v[200:201], v[204:205]\n\t"
    "v_pk_add_f32 v[202:203], v[202:203], v[206:207]\n\t"
    "s_waitcnt lgkmcnt(4)\n\t"
    "v_pk_add_f32 v[208:209], v[208:209], v[212:213]\n\t"
    "v_pk_add_f32 v[210:211], v[210:211], v[214:215]\n\t"
    "v_pk_add_f32 v[200:201], v[200:201], v[208:209]\n\t"
    "v_pk_add_f32 v[202:203], v[202:203], v[210:211]\n\t"
    "v_lshlrev_b32 v204, 16, v106\n\t"
    "v_and_b32 v205, 0xffff0000, v106\n\t"
    "v_lshlrev_b32 v206, 16, v107\n\t"
    "v_and_b32 v207, 0xffff0000, v107\n\t"
    "s_waitcnt vmcnt(2)\n\t"
    "v_pk_fma_f32 v[200:201], v[200:201], v[124:125], v[204:205]\n\t"
    "v_pk_fma_f32 v[202:203], v[202:203], v[126:127], v[206:207]\n\t"
    "v_cvt_pk_bf16_f32 v26, v200, v201\n\t"
    "v_cvt_pk_bf16_f32 v27, v202, v203\n\t"
    "s_waitcnt lgkmcnt(2)\n\t"
    "v_pk_add_f32 v[216:217], v[216:217], v[220:221]\n\t"
    "v_pk_add_f32 v[218:219], v[218:219], v[222:223]\n\t"
    "s_waitcnt lgkmcnt(0)\n\t"
    "v_pk_add_f32 v[224:225], v[224:225], v[228:229]\n\t"
    "v_pk_add_f32 v[226:227], v[226:227], v[230:231]\n\t"
    "v_pk_add_f32 v[216:217], v[216:217], v[224:225]\n\t"
    "v_pk_add_f32 v[218:219], v[218:219], v[226:227]\n\t"
    "s_waitcnt vmcnt(1)\n\t"
    "v_lshlrev_b32 v220, 16, v108\n\t"
    "v_and_b32 v221, 0xffff0000, v108\n\t"
    "v_lshlrev_b32 v222, 16, v109\n\t"
    "v_and_b32 v223, 0xffff0000, v109\n\t"
    "s_waitcnt vmcnt(0)\n\t"
    "v_pk_fma_f32 v[216:217], v[216:217], v[128:129], v[220:221]\n\t"
    "v_pk_fma_f32 v[218:219], v[218:219], v[130:131], v[222:223]\n\t"
    "v_cvt_pk_bf16_f32 v28, v216, v217\n\t"
    "v_cvt_pk_bf16_f32 v29, v218, v219\n\t"
    "v_permlane16_swap_b32 v20, v22\n\t"
    "v_permlane16_swap_b32 v21, v23\n\t"
    "v_permlane16_swap_b32 v24, v26\n\t"
    "v_permlane16_swap_b32 v25, v27\n\t"
    "global_store_dwordx4 v14, v[20:23], s[44:45] offset:0\n\t"
    "global_store_dwordx4 v14, v[24:27], s[44:45] offset:64\n\t"
    "global_store_dwordx2 v15, v[28:29], s[44:45]\n\t"
      : "+{s58}"(fo0), "+{s59}"(wo0)
      : "{v1}"(fo), "{v2}"(co), "{v3}"(co1), "{v8}"(rw), "{v9}"(rr), "{v17}"(rr + 61440u),
        "{v10}"(rs), "{v11}"(rs1), "{v12}"(go), "{v13}"(go1), "{v14}"(st), "{v15}"(st1), "{v16}"(ss), "{v18}"(pl),
        "{s[36:37]}"(sF), "{s[38:39]}"(sW), "{s[40:41]}"(ws + WS_X1F), "{s[42:43]}"(ws + WS_MOD + 5 * 2560), "{s[44:45]}"(a.out), "{s[46:47]}"(ws + WS_ST),
        "{s[56:57]}"(ws + WS_X1C),
        "{s[64:65]}"(tag), "{s[66:67]}"(ws), "{s68}"(wv), "{s70}"(myf)
      : "v4", "v5", "v6", "v7", "v20", "v21", "v22", "v23", "v24", "v25", "v26", "v27", "v28", "v29", "v30", "v31", "v32", "v33", "v34", "v35", "v36", "v37", "v38", "v39", "v40", "v41", "v42", "v43", "v44", "v45", "v46", "v47", "v48", "v49", "v50", "v51", "v52", "v53", "v54", "v55", "v56", "v57", "v58", "v59", "v60", "v61", "v62", "v63", "v64", "v65", "v66", "v67", "v68", "v69", "v70", "v71", "v72", "v73", "v74", "v75", "v76", "v77", "v78", "v79", "v80", "v81", "v82", "v83", "v84", "v85", "v86", "v87", "v88", "v89", "v90", "v91", "v92", "v93", "v94", "v95", "v96", "v97", "v98", "v99", "v100", "v101", "v102", "v103", "v104", "v105", "v106", "v107", "v108", "v109", "v110", "v111", "v112", "v113", "v114", "v115", "v116", "v117", "v118", "v119", "v120", "v121", "v122", "v123", "v124", "v125", "v126", "v127", "v128", "v129", "v130", "v131", "v132", "v133", "v134", "v135", "v136", "v137", "v138", "v139", "v140", "v141", "v142", "v143", "v144", "v145", "v146", "v147", "v148", "v149", "v150", "v151", "v152", "v153", "v154", "v155", "v156", "v157", "v158", "v159", "v160", "v161", "v162", "v163", "v164", "v165", "v166", "v167", "v168", "v169", "v170", "v171", "v172", "v173", "v174", "v175", "v176", "v177", "v178", "v179", "v180", "v181", "v182", "v183", "v184", "v185", "v186", "v187", "v188", "v189", "v190", "v191", "v192", "v193", "v194", "v195", "v196", "v197", "v198", "v199", "v200", "v201", "v202", "v203", "v204", "v205", "v206", "v207", "v208", "v209", "v210", "v211", "v212", "v213", "v214", "v215", "v216", "v217", "v218", "v219", "v220", "v221", "v222", "v223", "v224", "v225", "v226", "v227", "v228", "v229", "v230", "v231", "v232", "v233", "v234", "v235", "v236", "v237", "v238", "v239", "a0", "a1", "a2", "a3", "a4", "a5", "a6", "a7", "a8", "a9", "a10", "a11", "a12", "a13", "a14", "a15", "a16", "a17", "a18", "a19", "a20", "a21", "a22", "a23", "a24", "a25", "a26", "a27", "a28", "a29", "a30", "a31", "a32", "a33", "a34", "a35", "a36", "a37", "a38", "a39", "a40", "a41", "a42", "a43", "a44", "a45", "a46", "a47", "a48", "a49", "a50", "a51", "a52", "a53", "a54", "a55", "a56", "a57", "a58", "a59", "a60", "a61", "a62", "a63", "a64", "a65", "a66", "a67", "a68", "a69", "a70", "a71", "a72", "a73", "a74", "a75", "a76", "a77", "a78", "a79", "a80", "a81", "a82", "a83", "a84", "a85", "a86", "a87", "a88", "a89", "a90", "a91", "a92", "a93", "a94", "a95", "a96", "a97", "a98", "a99", "a100", "a101", "a102", "a103", "a104", "a105", "a106", "a107", "a108", "a109", "a110", "a111", "a112", "a113", "a114", "a115", "a116", "a117", "a118", "a119", "a120", "a121", "a122", "a123", "a124", "a125", "a126", "a127", "a128", "a129", "a130", "a131", "a132", "a133", "a134", "a135", "a136", "a137", "a138", "a139", "a140", "a141", "a142", "a143", "a144", "a145", "a146", "a147", "a148", "a149", "a150", "a151", "a152", "a153", "a154", "a155", "a156", "a157", "a158", "a159", "a160", "a161", "a162", "a163", "a164", "a165", "a166", "a167", "a168", "a169", "a170", "a171", "a172", "a173", "a174", "a175", "a176", "a177", "a178", "a179", "a180", "a181", "a182", "a183", "a184", "a185", "a186", "a187", "a188", "a189", "a190", "a191", "a192", "a193", "a194", "a195", "a196", "a197", "a198", "a199", "a200", "a201", "a202", "a203", "a204", "a205", "a206", "a207", "a208", "a209", "a210", "a211", "a212", "a213", "a214", "a215", "a216", "a217", "a218", "a219", "a220", "a221", "a222", "a223", "a224", "a225", "a226", "a227", "a228", "a229", "a230", "a231", "a232", "a233", "a234", "a235", "a236", "a237", "a238", "a239", "a240", "a241", "a242", "a243", "a244", "a245", "a246", "a247", "a248", "a249", "a250", "a251", "a252", "a253", "a254", "a255", "s48", "s49", "s50", "s51", "s52", "s53", "s54", "s55", "s60", "s61", "s62", "s63", "s69", "s72", "s73", "vcc", "memory");
    asm volatile("mega_code_end:" ::: "memory");
    STAMP_OUT(3)
  }
#undef MAKE_TAG
#undef BD_GLUE
#undef STAMP_OUT
#undef STAMP_P0
}

extern "C" void kernel_launch(void* const* d_in, const int* in_sizes, int n_in, void* d_out, int out_size, void* d_ws, size_t ws_size,
                              hipStream_t stream) {
  Args a;
  a.x = (const char*)d_in[0];
  a.kc = (const char*)d_in[1];
  a.vc = (const char*)d_in[2];
  a.wq = (const char*)d_in[3];
  a.wo = (const char*)d_in[4];
  a.wg = (const char*)d_in[6];
  a.wu = (const char*)d_in[7];
  a.wd = (const char*)d_in[8];
  a.c = (const char*)d_in[9];
  a.wmod = (const char*)d_in[10];
  a.out = (char*)d_out;
  a.ws = (char*)d_ws;
  mega<<<256, 256, 0, stream>>>(a);
}
```
